```python
import torch

def _softmax(x, axis=-1):
    return torch.softmax(x, dim=axis)
def _gelu(x):
    return torch.nn.functional.gelu(x, approximate="tanh")
def _silu(x):
    return torch.nn.functional.silu(x)
def _sigmoid(x):
    return torch.sigmoid(x)
def _layernorm(x, axis=-1, eps=1e-5):
    m = x.mean(dim=axis, keepdim=True)
    v = x.var(dim=axis, keepdim=True, unbiased=False)
    return (x - m) / torch.sqrt(v + eps)
def _scatter_add(tgt, idx, src, axis=0):
    return tgt.clone().index_add_(axis, idx, src)
def _segment_sum(src, seg, n):
    out = torch.zeros((n,) + src.shape[1:], dtype=src.dtype)
    return out.index_add_(0, seg, src)
def _pad(x, pw):
    flat = tuple(v for pair in reversed(pw) for v in pair)
    return torch.nn.functional.pad(x, flat)

kernel_name = 'interleave_layers__sliding_local_dilated__linear_kernel_hedgehog__prompt'

def setup_inputs():

    _w = (lambda a: (lambda t: t.to(torch.bfloat16) if t.is_floating_point() else t)(torch.as_tensor(a)))
    def _A_setup():
        B, S, D = 1, 2048, 1024
        return {
            "x": _w((lambda *s: torch.randn(*s, dtype=torch.bfloat16) * 0.05)(B, S, D)),
            "wqkv": _w((lambda *s: torch.randn(*s, dtype=torch.bfloat16) * 0.05)(D, 3 * D)),
            "wo": _w((lambda *s: torch.randn(*s, dtype=torch.bfloat16) * 0.05)(D, D)),
        }
    _A_in = _A_setup()

    def _B_setup():
        B, S, D, H = 1, 2048, 1024, 32
        Dh = D // H
        return {
            "x": _w((lambda *s: torch.randn(*s, dtype=torch.bfloat16) * 0.05)(B, S, D)),
            "wqkv": _w((lambda *s: torch.randn(*s, dtype=torch.bfloat16) * 0.4)(D, 3 * D)),
            "w_phi": _w((lambda *s: torch.randn(*s, dtype=torch.bfloat16) * 0.4)(H, Dh, Dh) * (Dh ** -0.5)),
            "wo": _w((lambda *s: torch.randn(*s, dtype=torch.bfloat16) * 0.1)(D, D)),
        }
    _B_in = _B_setup()

    return {
        "A_x": _A_in["x"],
        "A_wqkv": _A_in["wqkv"],
        "A_wo": _A_in["wo"],
        "B_x": _B_in["x"],
        "B_wqkv": _B_in["wqkv"],
        "B_w_phi": _B_in["w_phi"],
        "B_wo": _B_in["wo"],
    }

def reference(A_x, A_wqkv, A_wo, B_x, B_wqkv, B_w_phi, B_wo):
    def _A_ref(x, wqkv, wo):
        B, S, D = x.shape
        H = 32; Dh = D // H
        W, dil = 8, 2
        qkv = (lambda x, *d: x.permute(*d))((x @ wqkv).reshape(B, S, 3, H, Dh), 2, 0, 3, 1, 4)
        q, k, v = qkv[0], qkv[1], qkv[2]
        scale = Dh ** -0.5
        pos = torch.arange(S)
        off = torch.arange(W) * dil
        idx = pos[:, None] - off[None, :]
        mask = idx >= 0
        idx_c = torch.where(mask, idx, 0).reshape(-1)
        kg = k[:, :, idx_c, :].reshape(B, H, S, W, Dh)
        vg = v[:, :, idx_c, :].reshape(B, H, S, W, Dh)
        logits = torch.einsum("bhsd,bhswd->bhsw", q, kg) * scale
        logits = torch.where(mask[None, None, :, :], logits, -1e9)
        attn = _softmax(logits, axis=-1)
        o = torch.einsum("bhsw,bhswd->bhsd", attn, vg)
        return (lambda x, *d: x.permute(*d))(o, 0, 2, 1, 3).reshape(B, S, D) @ wo

    def _B_ref(x, wqkv, w_phi, wo):
        B, S, D = x.shape
        H = 32; Dh = D // H
        qkv = (lambda x, *d: x.permute(*d))((x @ wqkv).reshape(B, S, 3, H, Dh), 2, 0, 3, 1, 4)
        q, k, v = qkv[0], qkv[1], qkv[2]
        pq = torch.einsum("bhsd,hdf->bhsf", q, w_phi)
        pk = torch.einsum("bhsd,hdf->bhsf", k, w_phi)
        phi_q = torch.concatenate([_softmax(pq, axis=-1), _softmax(-pq, axis=-1)], axis=-1)
        phi_k = torch.concatenate([_softmax(pk, axis=-1), _softmax(-pk, axis=-1)], axis=-1)
        kv = torch.einsum("bhsf,bhsd->bhfd", phi_k, v)
        z  = torch.einsum("bhsf->bhf", phi_k)
        num = torch.einsum("bhqf,bhfd->bhqd", phi_q, kv)
        den = torch.einsum("bhqf,bhf->bhq", phi_q, z)[..., None] + 1e-6
        o = num / den
        return (lambda x, *d: x.permute(*d))(o, 0, 2, 1, 3).reshape(B, S, D) @ wo

    x = A_x
    x = x + _A_ref(_layernorm(x, axis=-1), A_wqkv, A_wo)
    x = x + _B_ref(_layernorm(x, axis=-1), B_wqkv, B_w_phi, B_wo)
    x = x + _A_ref(_layernorm(x, axis=-1), A_wqkv, A_wo)
    x = x + _B_ref(_layernorm(x, axis=-1), B_wqkv, B_w_phi, B_wo)
    return x
```

```cpp
#include <hip/hip_runtime.h>
#include <hip/hip_bf16.h>
#include <stdint.h>
#include <type_traits>
#pragma clang diagnostic ignored "-Winline-asm"
#pragma clang diagnostic ignored "-Wunused-value"

typedef unsigned short u16;
typedef __attribute__((ext_vector_type(8))) short bf16x8;
typedef __attribute__((ext_vector_type(4))) short bf16x4;
typedef __attribute__((ext_vector_type(4))) float f32x4;
typedef __attribute__((ext_vector_type(4))) unsigned u32x4;
typedef __attribute__((ext_vector_type(2))) unsigned u32x2;
typedef __attribute__((ext_vector_type(8))) float f32x8;
typedef __attribute__((ext_vector_type(16))) unsigned u32x16;
typedef __attribute__((ext_vector_type(16))) float f32x16;
typedef __attribute__((ext_vector_type(32))) float f32x32;
template <int I, typename T> __device__ __forceinline__ f32x4 quad(const T& v) { return __builtin_shufflevector(v, v, 4 * I, 4 * I + 1, 4 * I + 2, 4 * I + 3); }

constexpr int SEQ = 2048, DM = 1024, NH = 32;
#define ST(i)
constexpr int KVP = 64 * 32 + 64;

typedef __attribute__((ext_vector_type(2))) float f32x2;
typedef __attribute__((ext_vector_type(2))) __bf16 bf16x2_t;
__device__ __forceinline__ unsigned pk2(float a, float b) {
  f32x2 v = {a, b};
  bf16x2_t r = __builtin_convertvector(v, bf16x2_t);
  return __builtin_bit_cast(unsigned, r);
}

__device__ __forceinline__ bf16x4 tr_read(const char* p) {
  return __builtin_amdgcn_ds_read_tr16_b64_v4i16((__attribute__((address_space(3))) bf16x4*)p);
}
template <typename T> __device__ __forceinline__ __attribute__((address_space(3))) T& lds(unsigned a) { return *(__attribute__((address_space(3))) T*)(size_t)a; }
__device__ __forceinline__ bf16x4 tr_read(unsigned a) { return __builtin_amdgcn_ds_read_tr16_b64_v4i16(&lds<bf16x4>(a)); }
__device__ __forceinline__ bf16x8 cat8(bf16x4 a, bf16x4 b) {
  bf16x8 f = {a[0], a[1], a[2], a[3], b[0], b[1], b[2], b[3]};
  return f;
}
__device__ __forceinline__ void glds16s(const void* sbase, unsigned voff, unsigned lds_base) {
  asm volatile("s_mov_b32 m0, %2\n\ts_nop 0\n\tglobal_load_lds_dwordx4 %0, %1" :: "v"(voff), "s"(sbase), "s"(lds_base) : "memory", "m0");
}
__device__ __forceinline__ void glds16c(const void* sbase, unsigned voff, unsigned lds_base) {
  asm volatile("s_mov_b32 m0, %2\n\ts_nop 0\n\tglobal_load_lds_dwordx4 %0, %1 sc1" :: "v"(voff), "s"(sbase), "s"(lds_base) : "memory", "m0");
}
#define GL_ "\n\tglobal_load_lds_dwordx4 "
__device__ __forceinline__ void glds4c(const void* sbase, unsigned v, unsigned lds_base) {
  asm volatile("s_mov_b32 m0, %2\n\ts_nop 0" GL_ "%0, %1 sc1" GL_ "%0, %1 offset:1024 sc1" GL_ "%0, %1 offset:2048 sc1" GL_ "%0, %1 offset:3072 sc1"
               :: "v"(v), "s"(sbase), "s"(lds_base) : "memory", "m0");
}
__device__ __forceinline__ void glds4v(const void* sbase, unsigned v0, unsigned v1, unsigned v2, unsigned v3, unsigned lds_base) {
  asm volatile("s_mov_b32 m0, %5\n\ts_nop 0" GL_ "%0, %4" GL_ "%1, %4 offset:1024" GL_ "%2, %4 offset:2048" GL_ "%3, %4 offset:3072"
               :: "v"(v0), "v"(v1), "v"(v2), "v"(v3), "s"(sbase), "s"(lds_base) : "memory", "m0");
}
__device__ __forceinline__ void glds2v(const void* sbase, unsigned v0, unsigned v1, unsigned lds_base) {
  asm volatile("s_mov_b32 m0, %3\n\ts_nop 0" GL_ "%0, %2" GL_ "%1, %2 offset:1024" :: "v"(v0), "v"(v1), "s"(sbase), "s"(lds_base) : "memory", "m0");
}
__device__ __forceinline__ void gld16(u32x4& d, unsigned voff, const void* sbase) {
  asm volatile("global_load_dwordx4 %0, %1, %2" : "=v"(d) : "v"(voff), "s"(sbase) : "memory");
}
__device__ __forceinline__ unsigned lds_addr(const void* p) { return (unsigned)(size_t)p; }
#define WAITV(n) asm volatile("s_waitcnt vmcnt(" #n ")" ::: "memory")
#define WAITVN(n) asm volatile("s_waitcnt vmcnt(%0)" :: "n"(n) : "memory")
#define WAITL0 do { asm volatile("" ::: "memory"); __builtin_amdgcn_s_waitcnt(0xC07F); asm volatile("" ::: "memory"); } while (0)
#define BAR __builtin_amdgcn_s_barrier()
#define ARRIVED(...) do { WAITV(0); __builtin_amdgcn_sched_barrier(0); asm volatile("" : __VA_ARGS__); } while (0)

__device__ __forceinline__ float vmaxf(float a, float b) { float r; asm("v_max_f32 %0, %1, %2" : "=v"(r) : "v"(a), "v"(b)); return r; }
template <int N> __device__ __forceinline__ float dpp_ror(float x) {
  return __uint_as_float(__builtin_amdgcn_update_dpp(0u, __float_as_uint(x), 0x120 + N, 0xf, 0xf, false));
}
__device__ __forceinline__ float r16_sum(float x) { x += dpp_ror<8>(x); x += dpp_ror<4>(x); x += dpp_ror<2>(x); return x + dpp_ror<1>(x); }
__device__ __forceinline__ float vmax3f(float a, float b, float c) { float r; asm("v_max3_f32 %0, %1, %2, %3" : "=v"(r) : "v"(a), "v"(b), "v"(c)); return r; }
__device__ __forceinline__ void max8pn(const f32x4& a, const f32x4& b, float& mx, float& mn) {
  asm("v_max3_f32 %0, %2, %3, %4\n\tv_max3_f32 %1, -%2, -%3, -%4\n\tv_max3_f32 %0, %0, %5, %6\n\tv_max3_f32 %1, %1, -%5, -%6\n\t"
      "v_max3_f32 %0, %0, %7, %8\n\tv_max3_f32 %1, %1, -%7, -%8\n\tv_max_f32 %0, %0, %9\n\tv_max_f32 %1, %1, -%9\n\ts_nop 0"
      : "=&v"(mx), "=&v"(mn) : "v"(a[0]), "v"(a[1]), "v"(a[2]), "v"(a[3]), "v"(b[0]), "v"(b[1]), "v"(b[2]), "v"(b[3]));
}
__device__ __forceinline__ float vmaxf_n(float a, float b) { float r; asm("v_max_f32 %0, %1, %2\n\ts_nop 0" : "=v"(r) : "v"(a), "v"(b)); return r; }
__device__ __forceinline__ float vmax3f_n(float a, float b, float c) { float r; asm("v_max3_f32 %0, %1, %2, %3\n\ts_nop 0" : "=v"(r) : "v"(a), "v"(b), "v"(c)); return r; }
template <bool MAXOP> __device__ __forceinline__ float rop(float a, float b) { return MAXOP ? vmaxf_n(a, b) : a + b; }
template <bool MAXOP> __device__ __forceinline__ void xr2(float& a, float& b) {
  auto r = __builtin_amdgcn_permlane16_swap(__float_as_uint(a), __float_as_uint(b), false, false);
  unsigned u = __float_as_uint(rop<MAXOP>(__uint_as_float(r[0]), __uint_as_float(r[1])));
  auto r2 = __builtin_amdgcn_permlane32_swap(u, u, false, false);
  u = __float_as_uint(rop<MAXOP>(__uint_as_float(r2[0]), __uint_as_float(r2[1])));
  auto r3 = __builtin_amdgcn_permlane16_swap(u, u, false, false);
  a = __uint_as_float(r3[0]); b = __uint_as_float(r3[1]);
}
template <bool MAXOP> __device__ __forceinline__ void xr4(float& a, float& b, float& c, float& d) {
  auto r = __builtin_amdgcn_permlane16_swap(__float_as_uint(a), __float_as_uint(b), false, false);
  auto q = __builtin_amdgcn_permlane16_swap(__float_as_uint(c), __float_as_uint(d), false, false);
  const unsigned ab = __float_as_uint(rop<MAXOP>(__uint_as_float(r[0]), __uint_as_float(r[1])));
  const unsigned cd = __float_as_uint(rop<MAXOP>(__uint_as_float(q[0]), __uint_as_float(q[1])));
  auto t = __builtin_amdgcn_permlane32_swap(ab, cd, false, false);
  const unsigned u = __float_as_uint(rop<MAXOP>(__uint_as_float(t[0]), __uint_as_float(t[1])));
  auto h = __builtin_amdgcn_permlane32_swap(u, u, false, false);
  auto p0 = __builtin_amdgcn_permlane16_swap(h[0], h[0], false, false);
  auto p1 = __builtin_amdgcn_permlane16_swap(h[1], h[1], false, false);
  a = __uint_as_float(p0[0]); b = __uint_as_float(p0[1]); c = __uint_as_float(p1[0]); d = __uint_as_float(p1[1]);
}

__device__ __forceinline__ unsigned xs4(float a, float b, float c, float d) {
  auto r = __builtin_amdgcn_permlane16_swap(__float_as_uint(a), __float_as_uint(b), false, false);
  auto q = __builtin_amdgcn_permlane16_swap(__float_as_uint(c), __float_as_uint(d), false, false);
  auto t = __builtin_amdgcn_permlane32_swap(__float_as_uint(__uint_as_float(r[0]) + __uint_as_float(r[1])), __float_as_uint(__uint_as_float(q[0]) + __uint_as_float(q[1])), false, false);
  return __float_as_uint(__uint_as_float(t[0]) + __uint_as_float(t[1]));
}

#define MFMA(a, b, c) __builtin_amdgcn_mfma_f32_16x16x32_bf16(a, b, c, 0, 0, 0)

typedef unsigned long long u64;
typedef __attribute__((address_space(1))) unsigned long long gu64;
constexpr unsigned WS_PL = 0x1a0000, WS_PR = 1024;
constexpr unsigned WS_FLAGS = 16384;
constexpr int FLS = 64;
constexpr unsigned WS_G0 = 65536;
constexpr unsigned WS_CSP = WS_G0 + SEQ * 16;
constexpr unsigned WS_ST = WS_CSP + 16 * 16 * 768;
constexpr unsigned WS_X = 1u << 21;
constexpr unsigned WS_O = WS_X + 3u * SEQ * DM * 2;
constexpr unsigned KVPB = KVP * 2, KVFB = KVP * 4;
constexpr unsigned EMPTY = 0xffffffffu;
constexpr unsigned WS_KV = WS_O + 4u * SEQ * DM * 2;
constexpr unsigned KVLB = 256u * 16384;
constexpr unsigned WS_KF = 0x110000, KFLB = 17u * 16384;
constexpr unsigned WS_HL = WS_KV + 2u * KVLB;
static_assert(WS_ST % 16384 == 0 && WS_ST + 3u * SEQ * 128 <= WS_KF && WS_KF + 2 * KFLB <= WS_PL && WS_PL + 16384 <= WS_X && (WS_X >> 21 & 1) && (WS_O >> 21 & 1) && (WS_KV >> 21 & 1) && (WS_HL >> 21 & 1) && WS_HL % (1u << 21) == 0, "");
constexpr unsigned WS_CSA = WS_HL + 512u * 4096;
constexpr unsigned WS_CPA = WS_CSA + 256u * 1024;
constexpr unsigned WS_END = WS_CPA + 16 * 16 * 768;
constexpr int LDS_TOTAL = 163840;
constexpr int OFF_E = 139264;
constexpr int OFF_WO = 49152;
template <int V> using IC = std::integral_constant<int, V>;
__device__ __forceinline__ u32x4 empty4() { unsigned e = EMPTY; asm volatile("" : "+v"(e)); return u32x4{e, e, e, e}; }
__device__ __forceinline__ u64 korder(int v, int own) {
  const u64 t = v == 0 ? 0x3b210fedca987654ull : v == 1 ? 0xb37654210fedca98ull : v == 2 ? 0x3ba987654210fedcull : 0xb3fedca987654210ull;
  const u64 inv = v == 0 ? 0xa987e6543210fdcbull : v == 1 ? 0x6543f210dcbae987ull : v == 2 ? 0x3210edcba987f654ull : 0xdcbaf9876543e210ull;
  const u64 low = (1ull << (4 * ((inv >> (4 * own)) & 15))) - 1ull;
  return (u64)own | ((t & low) << 4) | (t & ~((low << 4) | 15ull));
}
__device__ __forceinline__ u64 korder0(int rt) {
  const int r = rt & 7;
  const u64 own = (0x765432765432ull >> (4 * (r < 6 ? r : r - 6))) & 0xffffffull, oth = 0x7654321076543210ull >> (4 * r);
  const u64 t = 0x10ull | ((oth & 15ull) << 8) | (own << 12) | ((oth >> 4) << 36);
  return t | (rt < 8 ? 0x8888888000000800ull : 0x0000000888888088ull);
}
__device__ __forceinline__ int knib(u64 t, int s) { return (int)(t >> (4 * s)) & 15; }
__device__ __forceinline__ unsigned kfirst(u64 t) { return (1u << knib(t, 1)) | (1u << knib(t, 2)) | (1u << knib(t, 3)) | (1u << knib(t, 4)); }
__device__ __forceinline__ int kv1(int bid) { return (bid + (0x4924 >> ((bid >> 2) & 14))) & 3; }

template <typename T> __device__ __forceinline__ T* uni(T* p) {
  const unsigned long long v = (unsigned long long)p;
  const unsigned lo = __builtin_amdgcn_readfirstlane((unsigned)v), hi = __builtin_amdgcn_readfirstlane((unsigned)(v >> 32));
  return (T*)(((unsigned long long)hi << 32) | lo);
}
__device__ __forceinline__ int lane_id() {
  int l;
  asm volatile("v_mbcnt_lo_u32_b32 %0, -1, 0\n\tv_mbcnt_hi_u32_b32 %0, -1, %0" : "=v"(l));
  __builtin_assume(l >= 0 && l < 64);
  return l;
}
#define IDS \
  const int lane = lane_id(); \
  asm volatile("" : "+s"(w)); \
  const int tid = w * 64 + lane; \
  int bid = blockIdx.x; asm volatile("" : "+s"(bid)); \
  ws = uni(ws); asm volatile("" : "+s"(ws)); \
  layer = __builtin_amdgcn_readfirstlane(layer); x0 = uni(x0); wo = uni(wo); \
    \
  const int rt = 2 * (bid & 4) + ((bid >> 3) & 7); \
  const int hp = 4 * (bid & 3) + (bid >> 6); \
  const int m0 = rt * 128; \
  P = __builtin_amdgcn_readfirstlane(P); \
  const unsigned SX = (unsigned)((rt & 7) * 524288) + ((((rt >> 3) ^ (rt >> 2) ^ 1) & 1) * 8192 ^ P); (void)SX; \
  const int fr = lane & 15, g = lane >> 4; \
  const int q4 = (lane & 15) >> 2, p4 = lane & 3; \
  const __amdgpu_buffer_rsrc_t rsW = __builtin_amdgcn_make_buffer_rsrc((void*)ws, (short)0, (int)WS_END, 0x00020000); \
  (void)m0; (void)fr; (void)q4; (void)p4; (void)rsW; (void)g;

__device__ __forceinline__ void publish(__amdgpu_buffer_rsrc_t rsW, int tid, unsigned idx, u64 v, unsigned P) {
  unsigned off = (WS_FLAGS + idx * FLS) ^ P;
  asm volatile("" : "+v"(off), "+v"(v));
  WAITV(0);
  WAITL0; BAR;
  if (tid == 192) __builtin_amdgcn_raw_buffer_store_b64(__builtin_bit_cast(u32x2, v), rsW, off, 0, 16);
  asm volatile("" ::: "memory");
}
__device__ __forceinline__ u64 poll_flags(char* ws, unsigned P, u64 E, int set, int rt, int hp, int lane, unsigned n, u64 mask, u64 now = ~0ull) {
  u64 miss = 0;
  const int l = lane & 15, g = lane >> 4;
  const int fi = set * 256 + (g == 2 ? l * 16 + hp : rt * 16 + l);
  const unsigned need = ((mask >> lane) & 1) ? n : 0u;
  gu64* fp = (gu64*)(ws + (need ? (WS_FLAGS + fi * FLS) ^ P : 0));
  _Pragma("unroll 1") for (unsigned spins = 0; spins < (1u << 18); ++spins) {
    const u64 d = __hip_atomic_load(fp, __ATOMIC_RELAXED, __HIP_MEMORY_SCOPE_AGENT) - E;
    const bool ok = (need == 0u) || (d >= (u64)need && d <= 16ull);
    miss = __ballot(!ok);
    if (!(miss & now)) break;
    __builtin_amdgcn_s_sleep(1);
  }
  asm volatile("s_waitcnt vmcnt(0)" ::: "memory");
  return miss;
}

__device__ __forceinline__ u64 poll_eq(const char* ws, unsigned off, u64 want, u64 mask, u64 now) {
  u64 miss;
  _Pragma("unroll 1") for (unsigned spins = 0; spins < (1u << 18); ++spins) {
    u64 d;
    asm volatile("global_load_dwordx2 %0, %1, %2 sc1\n\ts_waitcnt vmcnt(0)" : "=v"(d) : "v"(off), "s"(ws) : "memory");
    miss = __ballot(d != want) & mask;
    if (!(miss & now)) break;
    __builtin_amdgcn_s_sleep(1);
  }
  return miss;
}

#define G1_MACROS \
  ".macro G1M rb, j, s, z\n" \
  " .set g1d, 160+24*(\\rb)+4*(\\j)\n .set g1a, 80+40*(\\s)+4*(\\rb)\n .set g1b, 96+40*(\\s)+4*(\\j)\n" \
  " .if (\\j) >= 4\n  .set g1t, g1a\n  .set g1a, g1b\n  .set g1b, g1t\n .endif\n" \
  " .if \\z\n  v_mfma_f32_16x16x32_bf16 v[g1d:g1d+3], v[g1b:g1b+3], v[g1a:g1a+3], 0\n" \
  " .else\n  v_mfma_f32_16x16x32_bf16 v[g1d:g1d+3], v[g1b:g1b+3], v[g1a:g1a+3], v[g1d:g1d+3]\n .endif\n" \
  ".endm\n" \
  ".macro G1RA rb, s, ra, oa\n" \
  " ds_read_b128 v[80+40*(\\s)+4*(\\rb):83+40*(\\s)+4*(\\rb)], \\ra offset:(\\oa)+(((\\rb)*2048)^(4096*(1-%[hh])))\n" \
  ".endm\n" \
  ".macro G1RB k, s, r0, r1, ob\n" \
  " .if (\\k) & 2\n  ds_read_b64_tr_b16 v[96+40*(\\s)+2*(\\k):97+40*(\\s)+2*(\\k)], \\r1 offset:(\\ob)+((\\k)>>2)*2048+((\\k)&1)*512\n" \
  " .else\n  ds_read_b64_tr_b16 v[96+40*(\\s)+2*(\\k):97+40*(\\s)+2*(\\k)], \\r0 offset:(\\ob)+((\\k)>>2)*2048+((\\k)&1)*512\n .endif\n" \
  ".endm\n" \
    \
  ".macro G1BK i, g, r0, r1, ob\n G1RB 4*(\\g)+((\\i)&3), (\\i)>>2, \\r0, \\r1, (\\ob)+((\\i)>>2)*12288\n.endm\n" \
  ".macro G1AK rb, s, a0, a1, oa\n .if \\s\n  G1RA \\rb, 1, \\a1, \\oa\n .else\n  G1RA \\rb, 0, \\a0, \\oa\n .endif\n.endm\n" \
  ".macro G1RDB g, r0, r1, ob\n .set g1i, 0\n .rept 8\n  G1BK g1i, \\g, \\r0, \\r1, \\ob\n  .set g1i, g1i+1\n .endr\n.endm\n" \
  ".macro G1RDA a0, a1, oa\n .set g1i, 0\n .rept 8\n  G1AK (g1i>>1)^2, g1i&1, \\a0, \\a1, \\oa\n  .set g1i, g1i+1\n .endr\n.endm\n" \
    \
  ".macro G1Q g, rb, q, i, z\n G1M \\rb, 2*(\\g)+((((\\i)>>1)^(\\q))&1), (((\\i)+1)>>1)&1, (\\z)&&(((\\i)&1)==0)\n.endm\n" \
    \
  ".macro G1SG g, z, rd, a0, a1, r0, r1, o\n" \
  " .set g1i, 0\n .rept 16\n" \
  "  .if ((\\g) == 0) && (g1i == 8) && (\\rd)\n   s_waitcnt lgkmcnt(7)\n   s_nop 0\n  .endif\n" \
  "  .if ((\\g) == 0) && (g1i == 15)\n   s_waitcnt lgkmcnt(0)\n  .endif\n" \
  "  G1Q \\g, (g1i>>2)^(2-((\\g)&1)), g1i>>2, g1i&3, \\z\n" \
  "  .if \\rd\n" \
  "   .if (\\g) == 0\n    .if g1i < 7\n     G1BK g1i, 2, \\r0, \\r1, \\o\n    .elseif g1i == 8\n     G1BK 7, 2, \\r0, \\r1, \\o\n    .endif\n" \
  "   .elseif (\\g) == 1\n    .if g1i < 8\n     G1BK g1i, 0, \\r0, \\r1, \\o\n    .endif\n" \
  "   .elseif (g1i & 2) == 0\n    G1BK (g1i&1)+2*(g1i>>2), 1, \\r0, \\r1, \\o\n" \
  "   .elseif (\\rd) == 1\n    G1AK (g1i>>2)^2, 1-(g1i&1), \\a0, \\a1, \\o\n   .endif\n" \
  "  .endif\n" \
  "  .set g1i, g1i+1\n .endr\n" \
  ".endm\n" \
    \
    \
  ".macro G1END h, i0, n, l, xs, r0, r1\n" \
  " .set g1i, \\i0\n .rept \\n\n" \
  "  .if (\\l) && (g1i-(\\i0) < 12)\n   .set g1x, ((\\l)>>(4*(g1i-(\\i0))))&15\n" \
  "   .if g1x < 15\n    ds_write_b128 \\xs, v[208+4*g1x:211+4*g1x] offset:(g1x+2*%[hh]*(g1x/6))*1024\n   .endif\n  .endif\n" \
  "  G1Q g1i>>3, 2*(\\h)+(((g1i>>2)^(g1i>>3))&1), g1i>>2, g1i&3, 0\n" \
  "  .if ((\\l) == 0) && (g1i < 8)\n   G1BK g1i, 2, \\r0, \\r1, 0\n  .endif\n" \
  "  .set g1i, g1i+1\n .endr\n" \
  ".endm\n"
#define G1_PURGE \
  ".purgem G1M\n .purgem G1RA\n .purgem G1RB\n .purgem G1BK\n .purgem G1AK\n .purgem G1RDB\n .purgem G1RDA\n .purgem G1Q\n .purgem G1SG\n .purgem G1END\n"
#define G1_CLOBBER \
  "v80", "v81", "v82", "v83", "v84", "v85", "v86", "v87", "v88", "v89", "v90", "v91", "v92", "v93", "v94", "v95", "v96", "v97", "v98", "v99", "v100", \
  "v101", "v102", "v103", "v104", "v105", "v106", "v107", "v108", "v109", "v110", "v111", "v112", "v113", "v114", "v115", "v116", "v117", "v118", \
  "v119", "v120", "v121", "v122", "v123", "v124", "v125", "v126", "v127", "v128", "v129", "v130", "v131", "v132", "v133", "v134", "v135", "v136", \
  "v137", "v138", "v139", "v140", "v141", "v142", "v143", "v144", "v145", "v146", "v147", "v148", "v149", "v150", "v151", "v152", "v153", "v154", \
  "v155", "v156", "v157", "v158", "v159", "v208", "v209", "v210", "v211", "v212", "v213", "v214", "v215", "v216", "v217", "v218", "v219", "v220", \
  "v221", "v222", "v223", "v232", "v233", "v234", "v235", "v236", "v237", "v238", "v239", "v240", "v241", "v242", "v243", "v244", "v245", "v246", "v247"

template <int MODE>
__device__ __forceinline__ void g1_phase(char* smem, char* ws, unsigned& P, u64& E, int w, f32x16& xq, int layer, const u16* __restrict__ x0, const u16* __restrict__ W,
                                         const u16* __restrict__ wo, const u16* __restrict__ wphi,
                                         const u16* __restrict__ wn1, const u16* __restrict__ wn2) {
  constexpr bool HH = (MODE == 1);
  constexpr int ABYTES = 128 * 128;
  constexpr int STAGE = ABYTES + 3 * 8192;
  constexpr int OFF_KS = 147456;
  constexpr int OFF_SP = 3 * STAGE;
  constexpr int OFF_RS = !HH ? OFF_SP : OFF_SP + 16384;
  constexpr int OFF_CS = OFF_RS + 1152;
  constexpr int OFF_WP = OFF_CS + 1536;
  constexpr int OFF_JK = LDS_TOTAL - 1024;
  constexpr int OFF_PK = 0;
  constexpr int OFF_VF = OFF_WP + 4096;
  constexpr int OFF_KV = 16384;
  constexpr int OFF_R2 = 135168;
  const int XS = HH ? 98304 + ((w >> 1) & 1) * 16384 + (w & 1) * 4096 : (w & 3) * 12288;
  constexpr int PSTR = 144;
  IDS
  W = uni(W); wphi = uni(wphi); wn1 = uni(wn1); wn2 = uni(wn2);
  const int kg = w >> 2, wm = (w >> 1) & 1, wn = w & 1;
  const bool first = !HH && layer == 0;
  const bf16x8 ones = {0x3F80, 0x3F80, 0x3F80, 0x3F80, 0x3F80, 0x3F80, 0x3F80, 0x3F80};
  f32x4 acc[2][6];
  u32x4 pw[8];
  ST(layer * 24 + 0)
  const int R0 = wm * 64 + (HH ? kg : kg ^ 1) * 32;
  u32x16 pa;
#define pcs pa[0]
#define prs pa[1]
#define opo pa[2]
#define ogo pa[3]
#define pkw pa[5]
#define hof pa[7]
#define dm pa[8]
#define dm2 pa[9]
#define pcv pa[4]
#define pwf pa[5]
#define prv pa[6]
#define pvf pa[7]
#define ppk pa[10]
#define ppa pa[11]
#define pvr pa[12]
#define gkv pa[13]
#define gkz pa[14]
  f32x4 bias[3];
  const unsigned sb = lds_addr(smem);
  auto prep = [&]() {
    pcs = sb + OFF_CS + wn * 128 + g * 16;
    prs = sb + OFF_RS + (R0 + fr) * 8;
    opo = (unsigned)(wn * 8192 + R0 * 64 + lane * 16);
    ogo = opo + WS_O + layer * (SEQ * DM * 2) + SX + (unsigned)(wn * 8192 + hp * 32768);
    opo += sb;
    pcv = sb + OFF_CS + (wn * 32 + 128 + fr) * 4;
    prv = sb + OFF_R2 + (R0 + 4 * g) * 4;
    if (layer < 2) {
      constexpr unsigned kvn = WS_KV + MODE * KVLB;
      dm = kvn + (unsigned)((rt * 16 + hp) * 16384 + (w >> 2) * 8192 + (tid & 255) * 16);
      dm2 = w >= 4 ? WS_HL + (unsigned)((1 << 20) + ((rt & 7) * 8 + (hp >> 1)) * 16384 + (rt >> 3) * 8192 + (hp & 1) * 4096 + (tid - 256) * 16)
          : tid < 16 ? kvn + (unsigned)((rt * 16 + hp) * 16384 + (tid >> 3) * 8192 + 4096 + (tid & 7) * 16)
          : tid < 18 ? WS_KF + MODE * KFLB + (unsigned)(16 * 16384 + (tid & 1) * 8192 + hp * 256 + rt * 16)
          : tid >= 64 && tid < 128 ? WS_KF + MODE * KFLB + (unsigned)(hp * 16384 + (tid & 32) * 256 + rt * 512 + (tid & 31) * 16) : 0x7fff0000u;
    }
    if constexpr (!HH) {
      pkw = sb + OFF_KS - 4096 + R0 * 128 + wn * 2048 + lane * 16;
      const int rr = R0 ? rt : rt - 1;
      hof = m0 + R0 == 0 ? 0x7fff0000u : WS_HL + (unsigned)(layer * 524288 + ((rr & 7) * 8 + (hp >> 1)) * 16384 + (hp & 1) * 4096 + wn * 2048 + lane * 16) + ((((rr + 1) >> 3 ^ 1) & 1) * 8192 ^ P);
      const unsigned out = ~((0x5555u << 2 << fr) >> (4 * g));
#pragma unroll
      for (int i = 0; i < 8; ++i) {
        unsigned t;
        asm("v_bfe_i32 %0, %1, %2, 1" : "=v"(t) : "v"(out), "n"((i >> 2) * 16 + (i & 3)));
        bias[i >> 2][i & 3] = __uint_as_float(t & 0xff61b1e6u);
      }
      bias[2] = m0 + R0 == 0 ? f32x4{-3.0e38f, -3.0e38f, -3.0e38f, -3.0e38f} : bias[0];
    } else {
      const int u = wm * 2 + kg;
      pwf = sb + OFF_WP + wn * 2048 + (4 * g + q4) * 64 + p4 * 8;
      pvf = sb + OFF_VF + w * 2048 + lane * 16;
      ppk = sb + OFF_PK + w * (32 * PSTR) + fr * PSTR + g * 8;
      ppa = sb + OFF_PK + wn * (32 * PSTR) + (4 * g + q4) * PSTR + u * 32 + p4 * 8;
      pvr = sb + OFF_VF + wn * 2048 + lane * 16;
      const unsigned doff = WS_KV + (layer >> 1) * KVLB + (unsigned)((rt * 16 + hp) * 16384) + (((wn ^ (rt >> 3) ^ 1) & 1) * 8192 ^ P);
      gkv = doff + u * 1024 + lane * 16;
      gkz = fr == 0 ? doff + 4096 + u * 32 + g * 8 : 0x7fff0000u;
    }
    asm volatile("" : "+{v[48:63]}"(pa), "+{v[36:39]}"(bias[0]), "+{v[40:43]}"(bias[1]), "+{v[44:47]}"(bias[2]));
  };

  if (kg == 1) {
    const int lw = w & 3;
    const int kB = 16 * lw + (lane >> 3);
    const int fB = (kB >> 1) & 1;
    const int cB0 = ((((lane & 7) >> 1) ^ fB) << 1) | (lane & 1), cB1 = ((((lane & 7) >> 1) ^ (fB | 2)) << 1) | (lane & 1);
    const unsigned vB0 = (unsigned)((kB * 3072 + hp * 64 + cB0 * 8) * 2);
    const unsigned vB1 = (unsigned)(((kB + 8) * 3072 + hp * 64 + cB1 * 8) * 2) - 1024u;

    unsigned vW0, vW1;
    u64 ko2;
    auto run = [&](auto fst) {
      constexpr bool FIRST = decltype(fst)::value;
      constexpr int N = 10;
      const unsigned vA = FIRST ? (unsigned)(((m0 + lw * 32 + (lane >> 3)) * DM + ((lane & 7) ^ (lane >> 3)) * 8) * 2)
                                : SX + (unsigned)((lw >> 1) * 16384 + (lw & 1) * 4096 + lane * 16);
      const char* xa = FIRST ? (const char*)x0 : ws + WS_X + (layer - 1) * (SEQ * DM * 2);
      constexpr int KS = FIRST ? 128 : 32768;
      const u64 kord = FIRST ? korder0(rt) : korder(kv1(bid), hp);
      auto ktile = [&](int ks) { return knib(kord, ks); };
      auto stageA = [&](int ks, int boff) {
        const int kt = ktile(ks);
        const unsigned sA = lds_addr(smem) + boff;
        if (FIRST) glds4v(xa + kt * KS, vA, vA + 15360u, vA + 2 * 15360u, vA + 3 * 15360u, sA + lw * 4096);
        else glds4c(xa + kt * KS, vA, sA + lw * 4096);
      };
      auto stageB = [&](const u16* Wp, int ks, int boff) {
        const int kt = ktile(ks);
        const unsigned sB = lds_addr(smem) + boff + ABYTES;
        const u16* b = Wp + (size_t)kt * 64 * 3072;
        glds4v(b, vB0, vB1, vB0, vB1, sB + lw * 6144);
        glds2v(b + 2048, vB0, vB1, sB + lw * 6144 + 4096);
      };
      bf16x8 sel[3];
      unsigned cso[3], cst;
      if (FIRST) {
        stageA(1, STAGE); stageB(W, 1, STAGE);
        __builtin_amdgcn_sched_barrier(0);
        const int fl = (q4 >> 1) | ((g & 1) << 1), j = 3 * wm + (fr < 3 ? fr : 0);
#pragma unroll
        for (int i = 0; i < 3; ++i) {
          const unsigned o = fr == i ? 0x3F803F80u : 0u;
          sel[i] = __builtin_bit_cast(bf16x8, u32x4{o, o, o, o});
          cso[i] = (unsigned)(2 * STAGE + ABYTES + (g >> 1) * 6144 + (g & 1) * 1024 + q4 * 128 + p4 * 8 + (((wn * 2 + ((wm + i) & 1)) ^ fl) << 5) + ((3 * wm + i) >> 1) * 2048);
          asm volatile("" : "+v"(sel[i]), "+v"(cso[i]));
        }
        cst = fr < 3 ? WS_CPA + (unsigned)(((hp * 16 + rt) * 192 + (j >> 1) * 64 + wn * 32 + (j & 1) * 16 + 4 * g) * 4) : 0x7fff0000u;
        asm volatile("" : "+v"(cst));
        WAITVN(N);
        BAR;
        asm volatile("" ::: "memory");
        const u32x2 ev = *(const u32x2*)(smem + OFF_E);
        stageA(2, 2 * STAGE); stageB(W, 2, 2 * STAGE);
        E = ((u64)__builtin_amdgcn_readfirstlane(ev[1]) << 32) | __builtin_amdgcn_readfirstlane(ev[0]);
      }
      if (!FIRST) {
        stageB(W, 2, 2 * STAGE);
        const char* xa1 = xa + ktile(1) * KS;
        const char* xa2 = xa + ktile(2) * KS;
        unsigned sA = lds_addr(smem) + STAGE + lw * 4096;
        asm volatile("" : "+s"(xa1), "+s"(xa2), "+s"(sA));
        asm volatile("" :: "v"(vA));
        if (lw == 0) {
          const u64 m = kfirst(kord);
          poll_eq(ws, ((m >> lane) & 1) ? (WS_FLAGS + (unsigned)(256 + rt * 16 + lane) * FLS) ^ P : 0u, E + (unsigned)layer, m, ~0ull);
        }
        BAR;
        glds4c(xa1, vA, sA); glds4c(xa2, vA, sA + STAGE);
        __builtin_amdgcn_sched_barrier(0);
      }
      u64 fall, fal2;
      unsigned fao, fa2, vP, vS, vC, vX, dS, dC, dR;
      auto ld_pw = [&](int i0) {
#pragma unroll
        for (int i = i0; i < i0 + 4; ++i) gld16(pw[i], vP, wn1 + i * 8 * 3072);
      };
      auto setup = [&]() {
        fao = (unsigned)((256 + rt * 16 + (lane & 15)) * FLS) ^ P;
        fa2 = (unsigned)(((FIRST ? 512 : 256) + (lane & 15) * 16 + (FIRST ? hp : (hp - ((1 << (lane >> 4)) >> 1)) & 15)) * FLS) ^ P;
        vP = (unsigned)((rt * 64 + (lane >> 3)) * 6144 + lw * 2048 + ((hp + (1 << lw)) & 15) * 128 + (lane & 7) * 16);
        vS = FIRST ? (unsigned)(WS_G0 + (m0 + lw * 32 + (lane & 31)) * 16)
                   : WS_ST + (unsigned)((layer - 1) * (SEQ * 128) + (rt & 7) * 32768 + g * 1024 + lw * 256 + fr * 16) + ((rt >> 3) * 8192 ^ P);
        vC = (FIRST ? WS_CPA : WS_CSP) + (unsigned)(hp * 12288 + g * 768 + lw * 192 + (fr < 12 ? fr : 0) * 16);
        if (!FIRST) vX = HH ? (unsigned)(hp * 4096 + lw * 1024 + lane * 16) : (unsigned)((rt * 16 + hp) * 1024 + lane * 16);
        vW0 = (unsigned)((kB * DM + hp * 64 + ((lane & 7) ^ ((lane >> 4) << 1)) * 8) * 2); vW1 = vW0 + 8 * DM * 2 - 1024u;
        dS = OFF_RS + lw * 256 + (FIRST ? (lane & 31) * 8 : fr * 16 + (g & 1) * 8); dC = OFF_CS + lw * 192 + (fr < 12 ? fr : 0) * 16 + g * 4;
        ko2 = korder(bid & 3, hp);
        dR = OFF_R2 + lw * 128 + (FIRST ? (lane & 31) * 4 : fr * 8 + (g & 1) * 4);
        asm volatile("" : "+v"(dR));
        asm volatile("" : "+v"(vS), "+v"(vX), "+v"(vW0), "+v"(vW1), "+v"(dS));
        asm volatile("" : "+s"(ko2));
        if (FIRST || HH) asm volatile("" : "+v"(vC), "+v"(dC), "+v"(fa2));
        if (FIRST) asm volatile("" : "+v"(vP)); else asm volatile("" : "+v"(fao));
      };
      if (!FIRST) { setup(); prep(); }
      bf16x8 cf[6];
      f32x4 csum = {0, 0, 0, 0};
      if (FIRST) { fall = E + 1u; vX = (unsigned)(bid * 8192 + lw * 2048 + lane * 16); asm volatile("" : "+v"(fall), "+v"(vX)); }
#pragma unroll
      for (int kt = 0; kt < 13; ++kt) {
        const int cur = (kt % 3) * STAGE;
        if (!FIRST && kt == 0) { WAITV(4); }
        else if (FIRST && kt == 4 && lw == 1) { WAITVN(N + 2); }
        else if ((!FIRST && kt == 1 && lw == 3) || ((FIRST || HH) && kt == 10 && lw == 3) || (FIRST && (kt == 2 || kt == 4 || kt == 5))) { WAITVN(N + 1); }
        else { WAITVN(N); }
        if (!FIRST && kt == 2 && lw == 3) {
          asm volatile("" : "+v"(fall));
          if (__any(fall != E + (unsigned)layer)) poll_flags(ws, P, E, 1, rt, hp, lane, layer, 0xffffull);
        }
        if (HH && kt == 11 && lw == 3) {
          asm volatile("" : "+v"(fal2));
          if (__any(fal2 != E + (unsigned)layer)) {
            _Pragma("unroll 1") for (int j = 0; j < 4; ++j) poll_flags(ws, P, E, 1, rt, (hp - ((1 << j) >> 1)) & 15, lane, layer, 0xffff00000000ull);
          }
        }
        if (FIRST && kt == 11 && lw == 3) {
          asm volatile("" : "+v"(fal2));
          if (__any(fal2 != E + 1u)) poll_flags(ws, P, E, 2, rt, hp, lane, 1u, 0xffff00000000ull);
        }
        BAR;
        if (FIRST && kt == 1) {
          asm volatile("" ::: "memory");
#pragma unroll
          for (int i = 0; i < 6; ++i) { const char* t = smem + cso[i >> 1] + (i & 1) * 12288; cf[i] = cat8(tr_read(t), tr_read(t + 512)); }
        }
        if (FIRST && kt == 2 && lw == 2 && bid < 64 && (bid & 7) == ((bid >> 3) & 3)) {
          u64 t0, t1; unsigned d;
          asm volatile("s_memtime %0\n\ts_load_dword %2, %3, 0x0\n\ts_waitcnt lgkmcnt(0)\n\ts_memtime %1\n\ts_waitcnt lgkmcnt(0)"
                       : "=&s"(t0), "=&s"(t1), "=&s"(d) : "s"(ws + WS_PL + (bid >> 5) * 8192 + (bid & 31) * 256) : "memory");
          d = (unsigned)(t1 - t0);
          const u64 v = ((E + 1u) << 16) | (d < 65535u ? d : 65535u);
          __builtin_amdgcn_raw_buffer_store_b64(__builtin_bit_cast(u32x2, v), rsW, lane == 0 ? WS_PR + (unsigned)(bid & 56) : 0x7fff0000u, 0, 16);
        }
        if (FIRST && kt == 12 && lw == 2) asm volatile("global_load_dwordx2 %0, %1, %2 sc1" : "=v"(fal2) : "v"((lane & 7) * 8), "s"(ws + WS_PR) : "memory");
        if (FIRST && kt >= 2 && kt < 6 && lw == kt - 2) setup();
        if (FIRST && kt >= 6 && kt < 10 && lw == kt - 6) prep();
        stageA(kt + 3, cur);
        if (FIRST && kt == 1) {
          asm volatile("" : "+v"(sel[0]));
#pragma unroll
          for (int i = 0; i < 6; ++i) csum = MFMA(cf[i], sel[i >> 1], csum);
          asm volatile("" : "+v"(csum));
        }
        stageB(W, kt + 3, cur);
        if (FIRST) {
          if (kt == 1) __builtin_amdgcn_raw_buffer_store_b128(__builtin_bit_cast(u32x4, csum), rsW, cst, 0, 16);
          if (kt == 3 && tid == 320) __builtin_amdgcn_raw_buffer_store_b64(__builtin_bit_cast(u32x2, fall), rsW, (WS_FLAGS + (unsigned)(512 + rt * 16 + hp) * FLS) ^ P, 0, 16);
          if (kt == 3 || kt == 4) glds16s(wo + (kt - 3) * 512, vX, lds_addr(smem) + OFF_JK);
        }
        if (!FIRST && kt == 0 && lw == 3) asm volatile("global_load_dwordx2 %0, %1, %2 sc1" : "=v"(fall) : "v"(fao), "s"(ws + WS_FLAGS) : "memory");
        if ((FIRST || HH) && kt == 9 && lw == 3) asm volatile("global_load_dwordx2 %0, %1, %2 sc1" : "=v"(fal2) : "v"(fa2), "s"(ws + WS_FLAGS) : "memory");
      }
      if (FIRST && lw < 3) { ld_pw(0); WAITVN(N + 4); } else { WAITVN(N); }
      BAR;
      u32x4 sv[4], cv[4];
      auto ld_c = [&](u32x4& d, unsigned v, int so) { asm volatile("buffer_load_dwordx4 %0, %1, %2, %3 offen sc1" : "=v"(d) : "v"(v), "s"(rsW), "s"(so) : "memory"); };
      auto wo_tile = [&](int t) { glds2v(wo + (size_t)knib(ko2, t) * 64 * DM, vW0, vW1, lds_addr(smem) + OFF_WO + t * 8192 + lw * 2048); };
#pragma unroll
      for (int i = 0; i < (FIRST ? 1 : 4); ++i) ld_c(sv[i], vS, (i & 1) * 4096 + (i >> 1) * 16384);
      if (FIRST || HH) {
#pragma unroll
        for (int i = 0; i < 4; ++i) ld_c(cv[i], vC, i * 3072);
      }
      if (HH) glds16s(wphi, vX, lds_addr(smem) + OFF_WP + lw * 1024);
      else if (!FIRST) glds16c(ws + WS_CSA, vX, lds_addr(smem) + OFF_CS);
      wo_tile(0); wo_tile(1);
      if (HH || (FIRST && lw < 3)) { WAITV(13); } else { WAITV(9); }
      BAR;
      wo_tile(2); wo_tile(3); wo_tile(4); wo_tile(5);
      if (FIRST) {
        glds16s(wn2, vX, lds_addr(smem) + OFF_JK); glds16s(wn2 + 512, vX, lds_addr(smem) + OFF_JK);
        WAITV(14);
        if (lw < 3) ld_pw(4);
      } else {
        WAITV(12);
      }
      auto fin = [&]() {
        if (FIRST) {
          const unsigned tag = (unsigned)E + 1u;
          asm volatile("" : "+v"(sv[0]));
          _Pragma("unroll 1") for (unsigned spins = 0; spins < (1u << 18); ++spins) {
            if (__all(sv[0][1] == tag && sv[0][3] == tag)) break;
            __builtin_amdgcn_s_sleep(1);
            asm volatile("buffer_load_dwordx4 %0, %1, %2, 0 offen sc1\n\ts_waitcnt vmcnt(0)" : "=v"(sv[0]) : "v"(vS), "s"(rsW) : "memory");
          }
          *(u32x2*)(smem + dS) = u32x2{sv[0][0], sv[0][2]};
          { unsigned* p = (unsigned*)(smem + dR); p[0] = sv[0][0]; p[128] = sv[0][2]; }
        } else {
          asm volatile("" : "+v"(sv[0]), "+v"(sv[1]), "+v"(sv[2]), "+v"(sv[3]));
          const f32x4 t = (__builtin_bit_cast(f32x4, sv[0]) + __builtin_bit_cast(f32x4, sv[1])) + (__builtin_bit_cast(f32x4, sv[2]) + __builtin_bit_cast(f32x4, sv[3]));
          const unsigned u = xs4(t[0], t[2], t[1], t[3]);
          auto h = __builtin_amdgcn_permlane32_swap(u, u, false, false);
          const float mean = __uint_as_float(h[0]) * (1.0f / 1024.0f);
          const float var = fmaxf(__uint_as_float(h[1]) * (1.0f / 1024.0f) - mean * mean, 0.f);
          const float rs = __builtin_amdgcn_rsqf(var + 1e-5f);
          *(float2*)(smem + dS) = make_float2(-mean, rs);
          { float* p = (float*)(smem + dR); p[0] = -mean; p[128] = rs; }
        }
      };
      auto cfin = [&]() {
        if (FIRST || HH) {
          asm volatile("" : "+v"(cv[0]), "+v"(cv[1]), "+v"(cv[2]), "+v"(cv[3]));
          const f32x4 t = (__builtin_bit_cast(f32x4, cv[0]) + __builtin_bit_cast(f32x4, cv[1])) + (__builtin_bit_cast(f32x4, cv[2]) + __builtin_bit_cast(f32x4, cv[3]));
          *(unsigned*)(smem + dC) = xs4(t[0], t[1], t[2], t[3]);
        }
      };
      cfin();
      if (FIRST && lw == 2) {
        asm volatile("" : "+v"(fal2));
        _Pragma("unroll 1") for (unsigned spins = 0; spins < (1u << 18); ++spins) {
          if (__all(((fal2 ^ ((E + 1u) << 16)) >> 16) == 0)) break;
          __builtin_amdgcn_s_sleep(1);
          asm volatile("global_load_dwordx2 %0, %1, %2 sc1\n\ts_waitcnt vmcnt(0)" : "=v"(fal2) : "v"((lane & 7) * 8), "s"(ws + WS_PR) : "memory");
        }
        unsigned d = (unsigned)fal2 & 0xffffu;
        d = min(d, (unsigned)__builtin_amdgcn_update_dpp(0u, d, 0xb1, 0xf, 0xf, false));
        d = min(d, (unsigned)__builtin_amdgcn_update_dpp(0u, d, 0x4e, 0xf, 0xf, false));
        *(unsigned*)(smem + OFF_E + 256) = __builtin_amdgcn_readlane(d, 4) < __builtin_amdgcn_readlane(d, 0) ? 8192u : 0u;
      }
      if (!HH) BAR;
      fin();
      WAITL0;
    };
    typedef std::integral_constant<bool, true> T_;
    typedef std::integral_constant<bool, false> F_;
    __builtin_amdgcn_s_setprio(3);
    if (first) run(T_{}); else run(F_{});
    __builtin_amdgcn_s_setprio(0);
    BAR;
    {
      const char* xr = smem + XS + lane * 16;
#pragma unroll
      for (int i = 0; i < 2; ++i)
#pragma unroll
        for (int j = HH ? 2 : 0; j < (HH ? 4 : 6); ++j) acc[i][j] = *(const f32x4*)(xr + (i * (HH ? 8 : 6) + j) * 1024);
      if (HH) asm volatile("" : "=v"(acc[0][0]), "=v"(acc[0][1]), "=v"(acc[1][0]), "=v"(acc[1][1]));
    }
    asm volatile("" : "={v[64:79]}"(xq));
  } else {
    prep();
    const int fl = (q4 >> 1) | ((g & 1) << 1);
    const unsigned bo = lds_addr(smem) + ABYTES + (g >> 1) * 6144 + (g & 1) * 1024 + q4 * 128 + p4 * 8;
    const unsigned bo0 = bo + (((wn * 2 + 0) ^ fl) << 5), bo1 = bo + (((wn * 2 + 1) ^ fl) << 5);
    const unsigned ar = lds_addr(smem) + (wm * 64 + fr) * 128;
    const unsigned aK0 = ar + ((g ^ (fr & 7)) << 4), aK1 = ar + (((4 + g) ^ (fr & 7)) << 4);
    if (first) {
      const int row = m0 + hp * 8 + w * 2 + (lane >> 5);
      static_assert(HH || (OFF_SP == 122880 && OFF_KS + 12288 <= OFF_JK), "addresses used at the top of the kernel");
      asm volatile("" :: "v"(aK0), "v"(aK1), "v"(bo0), "v"(bo1));
      unsigned go = (lane & 31) == 0 ? (unsigned)(WS_G0 + row * 16) : 0x7fff0000u;
      unsigned ho = WS_HL + (unsigned)(((rt & 7) * 8 + (hp >> 1)) * 16384 + (hp & 1) * 4096 + tid * 16) + ((((rt + 1) >> 3 ^ 1) & 1) * 8192 ^ P);
      asm volatile("" : "+v"(go), "+v"(ho));
      WAITV(0);
      u32x4 xs[4];
      const u32x2 ev = *(const u32x2*)(smem + OFF_E);
#pragma unroll
      for (int i = 0; i < 4; ++i) xs[i] = *(const u32x4*)(smem + OFF_SP + w * 4096 + (lane >> 5) * 2048 + (lane & 31) * 16 + i * 512);
      typedef __attribute__((ext_vector_type(2))) __bf16 bf2;
      const bf2 one2 = __builtin_bit_cast(bf2, 0x3F803F80u);
      float sa[2] = {0.f, 0.f}, sq[2] = {0.f, 0.f};
#pragma unroll
      for (int i = 0; i < 16; ++i) {
        const unsigned u = xs[i >> 2][i & 3];
        const bf2 t = __builtin_bit_cast(bf2, u);
        sa[i & 1] = __builtin_amdgcn_fdot2_f32_bf16(t, one2, sa[i & 1], false);
        sq[i & 1] = __builtin_amdgcn_fdot2_f32_bf16(t, t, sq[i & 1], false);
      }
      float sm = r16_sum(sa[0] + sa[1]), m2 = r16_sum(sq[0] + sq[1]);
      {
        auto r = __builtin_amdgcn_permlane16_swap(__float_as_uint(sm), __float_as_uint(sm), false, false);
        auto q = __builtin_amdgcn_permlane16_swap(__float_as_uint(m2), __float_as_uint(m2), false, false);
        sm = __uint_as_float(r[0]) + __uint_as_float(r[1]); m2 = __uint_as_float(q[0]) + __uint_as_float(q[1]);
      }
      const float mean = sm * (1.0f / 1024.0f);
      m2 = fmaxf(m2 - sm * mean, 0.f);
      const unsigned tag = ev[0] + 1u;
      u32x4 gw = {__float_as_uint(-mean), tag, __float_as_uint(__builtin_amdgcn_rsqf(m2 * (1.0f / 1024.0f) + 1e-5f)), tag};
      __builtin_amdgcn_raw_buffer_store_b128(gw, rsW, go, 0, 16);
      __builtin_amdgcn_raw_buffer_store_b128(empty4(), rsW, ho, 0, 16);
      if (bid < 64 && tid == 0) __builtin_amdgcn_raw_buffer_store_b32(0u, rsW, WS_PL + (unsigned)((bid >> 5) * 8192 + (bid & 31) * 256), 0, 16);
      E = ((u64)__builtin_amdgcn_readfirstlane(ev[1]) << 32) | __builtin_amdgcn_readfirstlane(ev[0]);
    }
    f32x32 ac0;
    f32x16 ac1;
    f32x8 av[2];
    asm volatile("" :: "v"(aK0), "v"(aK1), "v"(bo0), "v"(bo1));
    if (first) BAR;
    else publish(rsW, tid, 256 + rt * 16 + hp, E + (unsigned)layer, P);
#define G1_OUT
#define G1_T(a)
#define G1_TEND
#define G1_TCL
    asm volatile(G1_MACROS
        ".p2align 3\n"
        "G1RDB 0, %[b0], %[b1], 0\n G1RDA %[a0], %[a1], 0\n G1RDB 1, %[b0], %[b1], 0\n"
        "ds_read_b128 v[64:67], %[xa]\n ds_read_b128 v[68:71], %[xa] offset:2048\n"
        "ds_read_b128 v[72:75], %[xa] offset:4096\n ds_read_b128 v[76:79], %[xa] offset:6144\n"
        "s_cmp_eq_u32 %[lay], 0\n s_waitcnt lgkmcnt(12)\n"
        "G1SG 0, 1, 1, %[a0], %[a1], %[b0], %[b1], 0\n"
        "s_mov_b32 vcc_lo, 16\n"
        "G1SG 1, 1, 1, %[a0], %[a1], %[b0], %[b1], 40960\n"
        "G1SG 2, 1, 2, %[a0], %[a1], %[b0], %[b1], 40960\n"
        "s_cbranch_scc1 .Lg1a%=\n"
        "s_barrier\n"
        G1_T(88)
        "s_barrier\n"
        G1_T(92)
        "G1RDA %[a0], %[a1], 40960\n"
        "s_waitcnt lgkmcnt(4)\n"
        "s_branch .Lg1b%=\n"
        ".Lg1a%=:\n"
        G1_T(88)
        "s_barrier\n"
        G1_T(92)
        "G1RDB 0, %[b0], %[b1], 40960\n G1RDA %[a0], %[a1], 40960\n G1RDB 1, %[b0], %[b1], 40960\n"
        ".p2align 3\n s_nop 0\n"
        ".Lg1b%=:\n"
        "s_waitcnt lgkmcnt(8)\n"
        "G1SG 0, 0, 1, %[a0], %[a1], %[b0], %[b1], 40960\n"
        "s_barrier\n"
        "G1SG 1, 0, 1, %[a2], %[a3], %[b2], %[b3], 0\n"
        "G1SG 2, 0, 1, %[a2], %[a3], %[b2], %[b3], 0\n"
        "s_lshr_b32 vcc_lo, vcc_lo, 1\n s_waitcnt vmcnt(0) lgkmcnt(8)\n"
        "G1SG 0, 0, 1, %[a2], %[a3], %[b2], %[b3], 0\n"
        "s_barrier\n"
        "G1SG 1, 0, 1, %[a0], %[a1], %[b0], %[b1], 0\n"
        "G1SG 2, 0, 1, %[a0], %[a1], %[b0], %[b1], 0\n"
        "s_cbranch_scc0 .Lg1c%=\n s_waitcnt lgkmcnt(8)\n"
        "G1SG 0, 0, 1, %[a0], %[a1], %[b0], %[b1], 0\n"
        "s_barrier\n"
        "G1SG 1, 0, 1, %[a0], %[a1], %[b0], %[b1], 40960\n"
        "G1SG 2, 0, 1, %[a0], %[a1], %[b0], %[b1], 40960\n"
        "s_branch .Lg1b%=\n"
        ".Lg1c%=:\n"
        "s_waitcnt lgkmcnt(8)\n"
        "G1END 1, 0, 8, 0, %[xs], %[b0], %[b1]\n"
        "s_waitcnt lgkmcnt(8)\n s_nop 0\n"
        "G1END 1, 8, 8, 0, %[xs], %[b0], %[b1]\n"
        "s_waitcnt lgkmcnt(0)\n s_nop 0\n"
        "G1END 1, 16, 8, 0, %[xs], %[b0], %[b1]\n"
        ".if %[hh] == 0\n s_nop 0\n s_barrier\n .endif\n"
        G1_T(96)
        ".if %[hh]\n G1END 0, 0, 20, 0xffff76103298, %[xs], %[b0], %[b1]\n .else\n G1END 0, 0, 18, 0xba5498327610, %[xs], %[b0], %[b1]\n .endif\n"
        "s_waitcnt lgkmcnt(0)\n s_barrier\n"
        ".if %[hh]\n G1END 0, 20, 24-20, 0xffffffffffff, %[xs], %[b0], %[b1]\n .else\n G1END 0, 18, 24-18, 0xffffffffffff, %[xs], %[b0], %[b1]\n .endif\n"
        "s_nop 7\n"
        G1_TEND G1_PURGE
        : "=&{v[160:191]}"(ac0), "=&{v[192:207]}"(ac1), "=&{v[64:79]}"(xq), "=&{v[224:231]}"(av[0]), "=&{v[248:255]}"(av[1]),
          "+{v[48:63]}"(pa), "+{v[36:39]}"(bias[0]), "+{v[40:43]}"(bias[1]), "+{v[44:47]}"(bias[2]) G1_OUT
        : [a0] "v"(aK0), [a1] "v"(aK1), [a2] "v"(aK0 + 2 * STAGE), [a3] "v"(aK1 + 2 * STAGE), [b0] "v"(bo0), [b1] "v"(bo1),
          [b2] "v"(bo0 + 2 * STAGE), [b3] "v"(bo1 + 2 * STAGE), [xs] "v"(lds_addr(smem) + XS + lane * 16), [xa] "v"(wn ? aK1 : aK0),
          [lay] "s"(layer), [hh] "n"(MODE)
        : "memory", "scc", "vcc", G1_TCL G1_CLOBBER);
    acc[0][0] = quad<0>(ac0); acc[0][1] = quad<1>(ac0); acc[0][2] = quad<2>(ac0); acc[0][3] = quad<3>(ac0); acc[0][4] = quad<4>(ac0); acc[0][5] = quad<5>(ac0);
    acc[1][0] = quad<6>(ac0); acc[1][1] = quad<7>(ac0); acc[1][2] = quad<0>(ac1); acc[1][3] = quad<1>(ac1); acc[1][4] = quad<2>(ac1); acc[1][5] = quad<3>(ac1);
    if constexpr (HH) {
      const float cs[2] = {lds<float>(pcv), lds<float>(pcv + 64)};
      f32x4 rm[4], rr[4];
#pragma unroll
      for (int rb = 0; rb < 4; ++rb) { rm[rb] = lds<f32x4>(prv + rb * 64); rr[rb] = lds<f32x4>(prv + 512 + rb * 64); }
#pragma unroll
      for (int h = 0; h < 2; ++h) {
        f32x2 f[8];
#pragma unroll
        for (int i = 0; i < 8; ++i) {
          const int c = i >> 2, rb = (i >> 1) & 1, e = (i & 1) * 2;
          const f32x4 a = h == 0 ? acc[rb][4 + c] : c ? quad<1>(av[rb]) : quad<0>(av[rb]);
          f[i] = __builtin_elementwise_fma(f32x2{rm[2 * h + rb][e], rm[2 * h + rb][e + 1]}, f32x2{cs[c], cs[c]}, f32x2{a[e], a[e + 1]});
        }
        asm volatile("" : "+v"(f[0]), "+v"(f[1]), "+v"(f[2]), "+v"(f[3]), "+v"(f[4]), "+v"(f[5]), "+v"(f[6]), "+v"(f[7]));
#pragma unroll
        for (int i = 0; i < 8; ++i) f[i] *= f32x2{rr[2 * h + ((i >> 1) & 1)][(i & 1) * 2], rr[2 * h + ((i >> 1) & 1)][(i & 1) * 2 + 1]};
        asm volatile("" : "+v"(f[0]), "+v"(f[1]), "+v"(f[2]), "+v"(f[3]), "+v"(f[4]), "+v"(f[5]), "+v"(f[6]), "+v"(f[7]));
#pragma unroll
        for (int c = 0; c < 2; ++c) lds<u32x4>(pvf + h * 8192 + c * 1024) = u32x4{pk2(f[4 * c][0], f[4 * c][1]), pk2(f[4 * c + 1][0], f[4 * c + 1][1]), pk2(f[4 * c + 2][0], f[4 * c + 2][1]), pk2(f[4 * c + 3][0], f[4 * c + 3][1])};
      }
    }
  }
  ST(layer * 24 + 4)
  {
  const int lane = lane_id(), tid = w * 64 + lane, fr = lane & 15, g = lane >> 4;
  (void)fr; (void)g;
  auto o_put = [&](int rb, const f32x4& a, const f32x4& b) {
    const u32x4 v = {pk2(a[0], a[1]), pk2(a[2], a[3]), pk2(b[0], b[1]), pk2(b[2], b[3])};
    __builtin_amdgcn_raw_buffer_store_b128(v, rsW, ogo + rb * 1024, 0, 16);
    return v;
  };
  auto marks = [&]() {
    const u32x4 pz = empty4();
    __builtin_amdgcn_raw_buffer_store_b128(pz, rsW, dm, 0, 16);
    if (w < 2 || (!HH && w >= 4)) __builtin_amdgcn_raw_buffer_store_b128(pz, rsW, dm2, 0, 16);
  };
  if (first) { P = __builtin_amdgcn_readfirstlane(*(const unsigned*)(smem + OFF_E + 256)); ogo ^= P; }
  f32x4 csq[2], csk[2], rm[2], rr[2];
  float csv[2];
  f32x2 st2[2] = {lds<f32x2>(prs), lds<f32x2>(prs + 128)};
#pragma unroll
  for (int c = 0; c < 2; ++c) {
    csk[c] = lds<f32x4>(pcs + 256 + c * 64);
    if (!HH) { csv[c] = lds<float>(pcv + c * 64); rm[1 - c] = lds<f32x4>(prv + (1 - c) * 64); rr[1 - c] = lds<f32x4>(prv + 512 + (1 - c) * 64); }
  }
#pragma unroll
  for (int c = 0; c < 2; ++c) csq[c] = lds<f32x4>(pcs + c * 64);
  if constexpr (!HH) {
    auto fold = [&](const f32x4& a, const f32x4& cs, const f32x2& st, bool scale) {
      f32x2 lo = __builtin_elementwise_fma(f32x2{cs[0], cs[1]}, f32x2{st[0], st[0]}, f32x2{a[0], a[1]});
      f32x2 hi = __builtin_elementwise_fma(f32x2{cs[2], cs[3]}, f32x2{st[0], st[0]}, f32x2{a[2], a[3]});
      if (scale) { lo *= f32x2{st[1], st[1]}; hi *= f32x2{st[1], st[1]}; }
      return u32x2{pk2(lo[0], lo[1]), pk2(hi[0], hi[1])};
    };
    auto cat2 = [](u32x2 a, u32x2 b) { return __builtin_bit_cast(bf16x8, u32x4{a[0], a[1], b[0], b[1]}); };
    auto foldv = [&](int rb, int c) {
      const f32x4& a = acc[rb][4 + c];
      const f32x2 lo = __builtin_elementwise_fma(f32x2{rm[rb][0], rm[rb][1]}, f32x2{csv[c], csv[c]}, f32x2{a[0], a[1]}) * f32x2{rr[rb][0], rr[rb][1]};
      const f32x2 hi = __builtin_elementwise_fma(f32x2{rm[rb][2], rm[rb][3]}, f32x2{csv[c], csv[c]}, f32x2{a[2], a[3]}) * f32x2{rr[rb][2], rr[rb][3]};
      return u32x2{pk2(lo[0], lo[1]), pk2(hi[0], hi[1])};
    };
    bf16x8 qf[2], kf[2];
    u32x2 vt[3][2];
#pragma unroll
    for (int rb = 1; rb >= 0; --rb) {
      kf[rb] = cat2(fold(acc[rb][2], csk[0], st2[rb], true), fold(acc[rb][3], csk[1], st2[rb], true));
      const u32x2 va0 = vt[rb + 1][0] = foldv(rb, 0), va1 = vt[rb + 1][1] = foldv(rb, 1);
      if (rb == 1) {
        if (R0 == 96) {
          u32x4 hk = __builtin_bit_cast(u32x4, kf[1]), hv = {va0[0], va0[1], va1[0], va1[1]};
#pragma unroll
          for (int e = 0; e < 4; ++e) { hk[e] = hk[e] < EMPTY - 1u ? hk[e] : EMPTY - 1u; hv[e] = hv[e] < EMPTY - 1u ? hv[e] : EMPTY - 1u; }
          __builtin_amdgcn_raw_buffer_store_b128(hk, rsW, hof, 0, 16);
          __builtin_amdgcn_raw_buffer_store_b128(hv, rsW, hof + 1024, 0, 16);
        } else {
          lds<bf16x8>(pkw + 4096) = kf[1];
          lds<u32x2>(pkw + 5120) = va0; lds<u32x2>(pkw + 5128) = va1;
        }
        if (first && R0 != 0) marks();
        asm volatile("" : "+v"(st2[0]), "+v"(st2[1]), "+v"(csv[0]), "+v"(csv[1]));
      }
    }
#pragma unroll
    for (int rb = 0; rb < 2; ++rb) qf[rb] = cat2(fold(acc[rb][0], csq[0], st2[rb], false), fold(acc[rb][1], csq[1], st2[rb], false));
    f32x4 sc[2][2];
    sc[1][0] = MFMA(kf[0], qf[1], bias[0]);
    sc[1][1] = MFMA(kf[1], qf[1], bias[1]);
    sc[0][1] = MFMA(kf[0], qf[0], bias[1]);
    constexpr float SCL = 0.17677669529663687f * 1.4426950408889634f;
    auto attn = [&](int rb) {
      const f32x4 z4 = {0, 0, 0, 0};
      const float scl = st2[rb][1] * SCL;
      asm volatile("s_nop 7" : "+v"(sc[rb][0]), "+v"(sc[rb][1]));
      unsigned m = __float_as_uint(vmax3f_n(vmax3f(sc[rb][0][0], sc[rb][0][1], sc[rb][0][2]), vmax3f(sc[rb][0][3], sc[rb][1][0], sc[rb][1][1]), vmaxf(sc[rb][1][2], sc[rb][1][3])));
      auto r = __builtin_amdgcn_permlane16_swap(m, m, false, false);
      m = __float_as_uint(vmaxf_n(__uint_as_float(r[0]), __uint_as_float(r[1])));
      auto r2 = __builtin_amdgcn_permlane32_swap(m, m, false, false);
      const float mxs = -vmaxf(__uint_as_float(r2[0]), __uint_as_float(r2[1])) * scl;
      u32x4 pv;
#pragma unroll
      for (int i = 0; i < 4; ++i) {
        const f32x2 t = __builtin_elementwise_fma(f32x2{sc[rb][i >> 1][(i & 1) * 2], sc[rb][i >> 1][(i & 1) * 2 + 1]}, f32x2{scl, scl}, f32x2{mxs, mxs});
        pv[i] = pk2(__builtin_amdgcn_exp2f(t[0]), __builtin_amdgcn_exp2f(t[1]));
      }
      const bf16x8 pf = __builtin_bit_cast(bf16x8, pv);
      const f32x4 den = MFMA(ones, pf, z4);
      const f32x4 o0 = MFMA(cat2(vt[rb][0], vt[rb + 1][0]), pf, z4), o1 = MFMA(cat2(vt[rb][1], vt[rb + 1][1]), pf, z4);
      const float inv = __builtin_amdgcn_rcpf(den[0]);
      return o_put(rb, o0 * inv, o1 * inv);
    };
    u32x4 hk, hv;
    auto ld_h = [&]() {
      asm volatile("buffer_load_dwordx4 %0, %2, %3, 0 offen sc1\n\tbuffer_load_dwordx4 %1, %2, %3, 0 offen offset:1024 sc1" : "=&v"(hk), "=&v"(hv) : "v"(hof), "s"(rsW) : "memory");
    };
    asm volatile("" : "+v"(sc[0][1]));
    const u32x4 ov = attn(1);
    if (R0 == 0) {
      ld_h();
      WAITL0; BAR;
      _Pragma("unroll 1") for (unsigned spins = 0; spins < (1u << 18); ++spins) {
        ARRIVED("+v"(hk), "+v"(hv));
        unsigned mxw = 0;
#pragma unroll
        for (int e = 0; e < 4; ++e) { mxw = hk[e] > mxw ? hk[e] : mxw; mxw = hv[e] > mxw ? hv[e] : mxw; }
        if (__all(mxw != EMPTY)) break;
        ld_h();
      }
      ARRIVED("+v"(hk), "+v"(hv));
      if (first) marks();
    } else {
      if (first && tid >= 128 && tid < 176) __builtin_amdgcn_raw_buffer_store_b128(*(const u32x4*)(smem + OFF_CS + (tid - 128) * 16), rsW, WS_CSA + (unsigned)((rt * 16 + hp) * 1024 + (tid - 128) * 16), 0, 16);
      WAITL0; BAR;
      asm volatile("" ::: "memory");
      hk = lds<u32x4>(pkw); hv = lds<u32x4>(pkw + 1024);
    }
    ST(layer * 24 + 5)
    asm volatile("" ::: "memory");
    vt[0][0] = u32x2{hv[0], hv[1]}; vt[0][1] = u32x2{hv[2], hv[3]};
    sc[0][0] = MFMA(__builtin_bit_cast(bf16x8, hk), qf[0], bias[2]);
    lds<u32x4>(opo + 1024) = ov;
    lds<u32x4>(opo) = attn(0);
  } else {
    bf16x8 wf[2];
#pragma unroll
    for (int fb = 0; fb < 2; ++fb) wf[fb] = cat8(tr_read(pwf + fb * 32), tr_read(pwf + fb * 32 + 16 * 64));
    __builtin_amdgcn_sched_barrier(0);
    if (layer == 1) marks();
    const f32x4 z4 = {0, 0, 0, 0};
    constexpr float L2E = 1.4426950408889634f;
    const unsigned kvo = WS_KV + (layer >> 1) * KVLB, kfo = WS_KF + (layer >> 1) * KFLB + hp * 16384, kzo = WS_KF + (layer >> 1) * KFLB + 16 * 16384 + hp * 256;
    bf16x8 qp[2][2];
    auto phi2 = [&](auto qktag) {
      constexpr int QK = decltype(qktag)::value & 1;
      constexpr int RB0 = (decltype(qktag)::value >> 1) & 1, RB1 = (decltype(qktag)::value & 4) ? RB0 + 1 : 2;
      float pv[2][8];
      f32x4 pm[2][2];
      f32x2 f[2][4];
#pragma unroll
      for (int rb = RB0; rb < RB1; ++rb) {
        asm volatile("" : "+v"(st2[rb]));
        const f32x2 nm = {st2[rb][0], st2[rb][0]};
#pragma unroll
        for (int i = 0; i < 4; ++i) {
          const f32x4& a = acc[rb][2 * QK + (i >> 1)];
          const f32x4& cs = QK ? csk[i >> 1] : csq[i >> 1];
          f[rb][i] = __builtin_elementwise_fma(f32x2{cs[(i & 1) * 2], cs[(i & 1) * 2 + 1]}, nm, f32x2{a[(i & 1) * 2], a[(i & 1) * 2 + 1]});
        }
      }
#pragma unroll
      for (int rb = RB0; rb < RB1; ++rb) asm volatile("" : "+v"(f[rb][0]), "+v"(f[rb][1]), "+v"(f[rb][2]), "+v"(f[rb][3]));
#pragma unroll
      for (int rb = RB0; rb < RB1; ++rb) {
        const bf16x8 xf = __builtin_bit_cast(bf16x8, u32x4{pk2(f[rb][0][0], f[rb][0][1]), pk2(f[rb][1][0], f[rb][1][1]), pk2(f[rb][2][0], f[rb][2][1]), pk2(f[rb][3][0], f[rb][3][1])});
        pm[rb][0] = MFMA(wf[0], xf, z4); pm[rb][1] = MFMA(wf[1], xf, z4);
      }
      if (QK) asm volatile("s_nop 7" : "+v"(pm[0][0]), "+v"(pm[0][1]), "+v"(pm[1][0]), "+v"(pm[1][1]));
      else asm volatile("s_nop 7" : "+v"(pm[RB0][0]), "+v"(pm[RB0][1]));
#pragma unroll
      for (int rb = RB0; rb < RB1; ++rb)
#pragma unroll
        for (int i = 0; i < 4; ++i) { pv[rb][i] = pm[rb][0][i]; pv[rb][4 + i] = pm[rb][1][i]; }
      float mx[2], mn[2];
#pragma unroll
      for (int u = RB0; u < RB1; ++u) max8pn(pm[u][0], pm[u][1], mx[u], mn[u]);
      if (RB1 - RB0 == 2) xr4<true>(mx[0], mn[0], mx[1], mn[1]); else xr2<true>(mx[RB0], mn[RB0]);
      float ep[2][8], en[2][8], sp[2], sn[2];
#pragma unroll
      for (int u = RB0; u < RB1; ++u) {
        const float c = st2[u][1] * L2E, mxs = -mx[u] * c, mns = -mn[u] * c;
#pragma unroll
        for (int i = 0; i < 4; ++i) {
          const f32x2 p2 = {pv[u][2 * i], pv[u][2 * i + 1]};
          const f32x2 tp = __builtin_elementwise_fma(p2, f32x2{c, c}, f32x2{mxs, mxs});
          const f32x2 tn = __builtin_elementwise_fma(-p2, f32x2{c, c}, f32x2{mns, mns});
          ep[u][2 * i] = __builtin_amdgcn_exp2f(tp[0]); ep[u][2 * i + 1] = __builtin_amdgcn_exp2f(tp[1]);
          en[u][2 * i] = __builtin_amdgcn_exp2f(tn[0]); en[u][2 * i + 1] = __builtin_amdgcn_exp2f(tn[1]);
        }
        const f32x2 a2 = (f32x2{ep[u][0], ep[u][1]} + f32x2{ep[u][2], ep[u][3]}) + (f32x2{ep[u][4], ep[u][5]} + f32x2{ep[u][6], ep[u][7]});
        const f32x2 b2 = (f32x2{en[u][0], en[u][1]} + f32x2{en[u][2], en[u][3]}) + (f32x2{en[u][4], en[u][5]} + f32x2{en[u][6], en[u][7]});
        sp[u] = a2[0] + a2[1]; sn[u] = b2[0] + b2[1];
      }
      if (RB1 - RB0 == 2) xr4<false>(sp[0], sn[0], sp[1], sn[1]); else xr2<false>(sp[RB0], sn[RB0]);
#pragma unroll
      for (int rb = RB0; rb < RB1; ++rb) {
        float ip = __builtin_amdgcn_rcpf(sp[rb]), in = __builtin_amdgcn_rcpf(sn[rb]);
        u32x2 o0, o1, o2, o3;
        o0[0] = pk2(ep[rb][0] * ip, ep[rb][1] * ip); o0[1] = pk2(ep[rb][2] * ip, ep[rb][3] * ip);
        o1[0] = pk2(ep[rb][4] * ip, ep[rb][5] * ip); o1[1] = pk2(ep[rb][6] * ip, ep[rb][7] * ip);
        o2[0] = pk2(en[rb][0] * in, en[rb][1] * in); o2[1] = pk2(en[rb][2] * in, en[rb][3] * in);
        o3[0] = pk2(en[rb][4] * in, en[rb][5] * in); o3[1] = pk2(en[rb][6] * in, en[rb][7] * in);
        if (QK == 0) {
          qp[rb][0] = __builtin_bit_cast(bf16x8, (u32x4){o0[0], o0[1], o1[0], o1[1]});
          qp[rb][1] = __builtin_bit_cast(bf16x8, (u32x4){o2[0], o2[1], o3[0], o3[1]});
          asm volatile("" : "+v"(qp[rb][0]), "+v"(qp[rb][1]));
        } else {
          const unsigned dst = ppk + rb * 16 * PSTR;
          lds<u32x2>(dst) = o0; lds<u32x2>(dst + 32) = o1; lds<u32x2>(dst + 64) = o2; lds<u32x2>(dst + 96) = o3;
        }
      }
    };
    phi2(std::integral_constant<int, 1>{});
    bf16x8 one8 = ones;
    asm volatile("" : "+v"(one8));
    WAITL0; BAR;
    ST(layer * 24 + 5)
    {
      bf16x8 af[4], v0[4], v1[4];
      asm volatile("" ::: "memory");
#pragma unroll
      for (int sw = 0; sw < 4; ++sw) {
        af[sw] = cat8(tr_read(ppa + sw * (64 * PSTR)), tr_read(ppa + sw * (64 * PSTR) + 16 * PSTR));
        v0[sw] = lds<bf16x8>(pvr + sw * 4096);
        v1[sw] = lds<bf16x8>(pvr + sw * 4096 + 1024);
      }
      if (kg) {
#pragma unroll
        for (int i = 0; i < 4; ++i) acc[i >> 1][i & 1] = lds<f32x4>(sb + XS + lane * 16 + ((i >> 1) * 8 + (i & 1)) * 1024);
      }
      __builtin_amdgcn_sched_barrier(0);
      f32x4 kv[3] = {z4, z4, z4};
#pragma unroll
      for (int sw = 0; sw < 4; ++sw) {
        kv[0] = MFMA(af[sw], v0[sw], kv[0]);
        kv[1] = MFMA(af[sw], v1[sw], kv[1]);
        kv[2] = MFMA(af[sw], one8, kv[2]);
      }
      auto pc = [](float x, float y) { const unsigned t = pk2(x, y); return t < EMPTY - 1u ? t : EMPTY - 1u; };
      __builtin_amdgcn_raw_buffer_store_b128(u32x4{pc(kv[0][0], kv[0][1]), pc(kv[0][2], kv[0][3]), pc(kv[1][0], kv[1][1]), pc(kv[1][2], kv[1][3])}, rsW, gkv, 0, 16);
      __builtin_amdgcn_raw_buffer_store_b64(u32x2{pc(kv[2][0], kv[2][1]), pc(kv[2][2], kv[2][3])}, rsW, gkz, 0, 16);
    }
    ST(layer * 24 + 6)
    u32x4 l1[8];
    const bool red = w == 7 || (w == 6 && (rt & 7) == 0);
    unsigned lo = kvo + (unsigned)((hp + (lane >> 5) * 128) * 16384) + ((((rt >> 3) ^ (lane >> 5) ^ 1) & 1) * 8192 ^ P) +
                  (w == 7 ? (unsigned)((rt & 4) * 512 + (g & 1) * 1024 + (rt & 3) * 256 + fr * 16) : (unsigned)(4096 + (lane & 7) * 16));
    unsigned fs = w == 7 ? kfo + (unsigned)((rt >> 2) * 2048 + (lane >> 5) * 1024 + (rt & 3) * 256 + fr * 16 + (g & 1) * 8)
                : (lane & 24) ? 0x7fff0000u : kzo + (unsigned)((rt >> 3) * 128 + (lane & 4) * 16 + (lane & 1) * 32 + (lane >> 5) * 16 + (lane & 2) * 4);
    auto l1_load = [&]() {
      asm volatile("" ::: "memory");
#pragma unroll
      for (int p = 0; p < 8; ++p) l1[p] = __builtin_amdgcn_raw_buffer_load_b128(rsW, lo, p * (16 * 16384), 16);
      __builtin_amdgcn_sched_barrier(0);
    };
    if (red) asm volatile("" : "+v"(lo), "+v"(fs));
    if (red) { __builtin_amdgcn_s_sleep(13); l1_load(); }
    phi2(std::integral_constant<int, 4>{});
    ST(layer * 24 + 7)
    if (!red) {
      phi2(std::integral_constant<int, 6>{});
    } else {
      typedef __attribute__((ext_vector_type(2))) __bf16 bf2;
      unsigned c0 = 0x00003F80u, c1 = 0x3F800000u;
      asm volatile("" : "+v"(c0), "+v"(c1));
      float s4[4];
      _Pragma("unroll 1") for (unsigned spins = 0; spins < (1u << 18); ++spins) {
        float a[8] = {0.f, 0.f, 0.f, 0.f, 0.f, 0.f, 0.f, 0.f};
#pragma unroll
        for (int p = 0; p < 8; ++p)
#pragma unroll
          for (int e = 0; e < 4; ++e) {
            const unsigned u = l1[p][e];
            a[2 * e] = __builtin_amdgcn_fdot2_f32_bf16(__builtin_bit_cast(bf2, u), __builtin_bit_cast(bf2, c0), a[2 * e], false);
            a[2 * e + 1] = __builtin_amdgcn_fdot2_f32_bf16(__builtin_bit_cast(bf2, u), __builtin_bit_cast(bf2, c1), a[2 * e + 1], false);
          }
#pragma unroll
        for (int i = 0; i < 4; ++i) {
          auto r = __builtin_amdgcn_permlane32_swap(__float_as_uint(a[i]), __float_as_uint(a[i + 4]), false, false);
          s4[i] = __uint_as_float(r[0]) + __uint_as_float(r[1]);
        }
        const float t = s4[0] + s4[2];
        if (__all(t == t)) break;
        unsigned mxw = 0;
#pragma unroll
        for (int p = 0; p < 8; ++p)
#pragma unroll
          for (int e = 0; e < 4; ++e) mxw = l1[p][e] > mxw ? l1[p][e] : mxw;
        if (__all(mxw != EMPTY)) break;
        l1_load();
      }
      unsigned o0 = pk2(s4[0], s4[1]), o1 = pk2(s4[2], s4[3]);
      o0 = o0 < EMPTY - 1u ? o0 : EMPTY - 1u; o1 = o1 < EMPTY - 1u ? o1 : EMPTY - 1u;
      __builtin_amdgcn_raw_buffer_store_b64(u32x2{o0, o1}, rsW, fs, 0, 16);
      __builtin_amdgcn_raw_buffer_store_b64(u32x2{o0, o1}, rsW, fs, 8192, 16);
    }
    unsigned vW0 = (unsigned)(((16 * w + (lane >> 3)) * DM + hp * 64 + ((lane & 7) ^ ((lane >> 4) << 1)) * 8) * 2);
    u64 ko = korder(bid & 3, hp);
    if (!red) { asm volatile("" : "+v"(vW0)); asm volatile("" : "+s"(ko)); }
    ST(layer * 24 + 8)
    WAITL0;
    BAR;
    ST(layer * 24 + 9)
    {
      u32x4 fa, fb = {0u, 0u, 0u, 0u};
      unsigned pfo = OFF_KV + wn * 4096 + lane * 16, pzo = OFF_KV + 8192 + wn * 128 + g * 16;
      asm volatile("" : "+v"(pfo), "+v"(pzo));
      const unsigned fo = kfo + (unsigned)(tid * 16) + ((rt >> 3) * 8192 ^ P), fo2 = kzo + (unsigned)((lane & 15) * 16) + ((rt >> 3) * 8192 ^ P);
      if (red) {
        phi2(std::integral_constant<int, 6>{});
      } else if (w < 4) {
#pragma unroll
        for (int t = 6; t < 12; ++t) glds2v(wo + (size_t)knib(ko, t) * 64 * DM, vW0, vW0 + 8 * DM * 2 - 1024u, sb + OFF_WO + t * 8192 + w * 2048);
      } else {
        __builtin_amdgcn_s_sleep(13);
      }
      _Pragma("unroll 1") for (unsigned spins = 0; spins < (1u << 18); ++spins) {
        asm volatile("" ::: "memory");
        fa = __builtin_amdgcn_raw_buffer_load_b128(rsW, fo, 0, 16);
        if (w == 0) fb = __builtin_amdgcn_raw_buffer_load_b128(rsW, fo2, 0, 16);
        unsigned mxw = 0;
#pragma unroll
        for (int e = 0; e < 4; ++e) { mxw = fa[e] > mxw ? fa[e] : mxw; mxw = fb[e] > mxw ? fb[e] : mxw; }
        if (__all(mxw != EMPTY)) break;
        __builtin_amdgcn_s_sleep(1);
      }
      *(u32x4*)(smem + OFF_KV + tid * 16) = fa;
      if (tid < 16) *(u32x4*)(smem + OFF_KV + 8192 + tid * 16) = fb;
      ST(layer * 24 + 10)
      WAITL0; BAR;
      ST(layer * 24 + 11)
      bf16x8 kvf[3][2];
#pragma unroll
      for (int ks = 0; ks < 2; ++ks) {
        kvf[0][ks] = *(const bf16x8*)(smem + pfo + ks * 2048);
        kvf[1][ks] = *(const bf16x8*)(smem + pfo + ks * 2048 + 1024);
        kvf[2][ks] = *(const bf16x8*)(smem + pzo + ks * 64);
      }
#pragma unroll
      for (int rb = 0; rb < 2; ++rb) {
        f32x4 oa[3];
#pragma unroll
        for (int nb = 2; nb >= 0; --nb) {
          oa[nb] = MFMA(kvf[nb][0], qp[rb][0], z4);
          oa[nb] = MFMA(kvf[nb][1], qp[rb][1], oa[nb]);
        }
        float den = oa[2][0];
        den += 1e-6f;
        float inv = __builtin_amdgcn_rcpf(den);
        lds<u32x4>(opo + rb * 1024) = o_put(rb, oa[0] * inv, oa[1] * inv);
      }
    }
  }
  ST(layer * 24 + 12)
  if (!HH && w >= 6) {
    const unsigned v0 = (unsigned)((((w & 1) * 32 + (lane >> 3)) * DM + hp * 64 + ((lane & 7) ^ ((lane >> 4) << 1)) * 8) * 2);
    const u64 ko = korder(bid & 3, hp);
#pragma unroll
    for (int t = 6; t < 12; ++t) glds4v(wo + (size_t)knib(ko, t) * 64 * DM, v0, v0 + 15360u, v0 + 2 * 15360u, v0 + 3 * 15360u, sb + OFF_WO + t * 8192 + (w & 1) * 4096);
    WAITV(24); WAITL0; BAR;
  } else if (w >= 4) { WAITV(0); WAITL0; BAR; }
  if (first && w >= 4 && w < 7) {
    typedef __attribute__((ext_vector_type(2))) __bf16 bf2;
    unsigned c0 = 0x00003F80u, c1 = 0x3F800000u;
    asm volatile("" : "+v"(c0), "+v"(c1));
    const bf2 lo1 = __builtin_bit_cast(bf2, c0), hi1 = __builtin_bit_cast(bf2, c1);
    float a[8] = {0.f, 0.f, 0.f, 0.f, 0.f, 0.f, 0.f, 0.f};
#pragma unroll
    for (int i = 0; i < 8; ++i) {
      asm volatile("" : "+v"(pw[i]));
#pragma unroll
      for (int e = 0; e < 4; ++e) {
        const unsigned u = pw[i][e];
        a[2 * e] = __builtin_amdgcn_fdot2_f32_bf16(__builtin_bit_cast(bf2, u), lo1, a[2 * e], false);
        a[2 * e + 1] = __builtin_amdgcn_fdot2_f32_bf16(__builtin_bit_cast(bf2, u), hi1, a[2 * e + 1], false);
      }
    }
#pragma unroll
    for (int i = 0; i < 4; ++i) {
      auto r = __builtin_amdgcn_permlane32_swap(__float_as_uint(a[i]), __float_as_uint(a[i + 4]), false, false);
      a[i] = __uint_as_float(r[0]) + __uint_as_float(r[1]);
    }
#pragma unroll
    for (int i = 0; i < 2; ++i) {
      auto r = __builtin_amdgcn_permlane16_swap(__float_as_uint(a[i]), __float_as_uint(a[i + 2]), false, false);
      a[i] = __uint_as_float(r[0]) + __uint_as_float(r[1]);
      a[i] += dpp_ror<8>(a[i]);
    }
    if (!(lane & 8))
      __builtin_amdgcn_raw_buffer_store_b64(u32x2{__float_as_uint(a[0]), __float_as_uint(a[1])}, rsW,
          (unsigned)(WS_CSP + ((((hp + (1 << (w - 4))) & 15) * 16 + rt) * 192 + (w - 4) * 64 + (lane & 7) * 8 + (lane >> 5) * 4 + ((lane >> 4) & 1) * 2) * 4), 0, 16);
  }
  }
}

#undef pcs
#undef prs
#undef opo
#undef ogo
#undef pkw
#undef hof
#undef dm
#undef dm2
#undef pcv
#undef pwf
#undef prv
#undef pvf
#undef ppk
#undef ppa
#undef pvr
#undef gkv
#undef gkz

__device__ __forceinline__ void g2_phase(char* smem, char* ws, unsigned P, u64 E, int w, f32x16& xq, int layer, const u16* __restrict__ x0,
                                         const u16* __restrict__ wo, const u16* __restrict__ wnx, u16* __restrict__ out) {
  IDS
  wnx = uni(wnx);
  const unsigned sb = lds_addr(smem);
  const int lw = w & 3, k2 = 16 * lw + (lane >> 3);
  const unsigned vW0 = (unsigned)((k2 * DM + hp * 64 + ((lane & 7) ^ ((lane >> 4) << 1)) * 8) * 2), vW1 = vW0 + 8 * DM * 2 - 1024u;
  const int r0 = w * 16 + (lane >> 3);
  unsigned pxr = sb + r0 * 128 + (lane & 7) * 16;
  const unsigned rmo = (unsigned)(((m0 + r0) * DM + hp * 64 + ((lane & 7) ^ (lane >> 3)) * 8) * 2);
  const unsigned imo = SX + (unsigned)(hp * 32768 + (w >> 2) * 8192 + r0 * 128 + (lane & 7) * 16);
  constexpr int S0 = 0, S1 = 16384, S2 = 32768, S3 = 147456, S4 = OFF_WO, S5 = 98304, S6 = 114688;
  constexpr int AS[16] = {S0, S1, S2, S3, S0, S1, S4, S2, S3, S0, S1, S4, S5, S3, S0, S6};
  constexpr int OFF_C = 0;
  auto bs = [](int t) { return OFF_WO + (t < 12 ? t : t - 10) * 8192; };
  if (w >= 4) {
    constexpr int WT[15] = {0, 8, 12, 14, 16, 18, 20, 18, 16, 14, 12, 12, 10, 10, 8};
    const char* o = ws + WS_O + layer * (SEQ * DM * 2) + SX;
    const unsigned vA = (unsigned)((lw >> 1) * 16384 + (lw & 1) * 4096 + lane * 16);
    const u64 kord = korder(bid & 3, hp);
    auto ldA = [&](int s) { glds4c(o + knib(kord, s) * 32768, vA, sb + AS[s] + lw * 4096); };
    auto ldW = [&](int t) { glds2v(wo + (size_t)knib(kord, t) * 64 * DM, vW0, vW1, sb + bs(t) + lw * 2048); };
    const int fB = (k2 >> 1) & 1;
    unsigned vB0 = (unsigned)((k2 * 3072 + hp * 64 + (((((lane & 7) >> 1) ^ fB) << 1) | (lane & 1)) * 8) * 2);
    unsigned vB1 = (unsigned)(((k2 + 8) * 3072 + hp * 64 + (((((lane & 7) >> 1) ^ (fB | 2)) << 1) | (lane & 1)) * 8) * 2) - 1024u;
    int kn1 = knib(korder(kv1(bid), hp), 1);
    asm volatile("" : "+v"(vB0), "+v"(vB1));
    asm volatile("" : "+s"(kn1));
    auto ldB = [&](int s, int part) {
      const u16* b = wnx + (size_t)(s ? kn1 : hp) * 64 * 3072;
      if (part) glds4v(b, vB0, vB1, vB0, vB1, sb + s * 40960 + 16384 + lw * 6144);
      else glds2v(b + 2048, vB0, vB1, sb + s * 40960 + 16384 + lw * 6144 + 4096);
    };
    BAR;
    ldA(1); ldA(2); ldA(3); ldA(4);
    WAITV(12);
    BAR;
    auto step = [&](auto ktc) {
      constexpr int kt = decltype(ktc)::value;
      WAITVN(WT[kt]);
      BAR;
      if constexpr (kt == 1) { ldA(5); ldA(6); }
      if constexpr (kt >= 2 && kt <= 10) ldA(kt + 5);
      if constexpr (kt >= 2 && kt <= 5) ldW(kt + 10);
      if constexpr (kt >= 11) ldB((kt - 11) >> 1, (kt - 11) & 1);
      if constexpr (kt >= 6) { __builtin_amdgcn_s_sleep(kt >= 11 ? 3 : 2); if (layer == 1) __builtin_amdgcn_s_sleep(1); }
    };
    step(IC<1>{}); step(IC<2>{}); step(IC<3>{}); step(IC<4>{}); step(IC<5>{}); step(IC<6>{}); step(IC<7>{});
    step(IC<8>{}); step(IC<9>{}); step(IC<10>{}); step(IC<11>{}); step(IC<12>{}); step(IC<13>{}); step(IC<14>{});
  } else {
    const int wm = w >> 1, wn = w & 1;
    f32x4 acc[4][2];
    unsigned co[2];
#pragma unroll
    for (int cb = 0; cb < 2; ++cb) co[cb] = sb + OFF_C + (unsigned)((wm * 64 + fr) * 128 + (((wn * 4 + cb * 2 + (g >> 1)) ^ (fr & 7)) << 4) + (g & 1) * 8);
    const int fl = (q4 >> 1) | ((g & 1) << 1);
    const int bb0 = (4 * g + q4) * 128 + p4 * 8 + (((wn * 2) ^ fl) << 5), bb1 = (4 * g + q4) * 128 + p4 * 8 + (((wn * 2 + 1) ^ fl) << 5);
    const int abase = wm * 4096 + lane * 16;
    bf16x8 fa[2][2][4], fb[2][2][2];
    unsigned ra[3], rb0[2], rb1[2];
#pragma unroll
    for (int i = 0; i < 3; ++i) { ra[i] = abase + i * 65536; asm volatile("" : "+v"(ra[i])); }
#pragma unroll
    for (int i = 0; i < 2; ++i) { rb0[i] = bb0 + OFF_WO + i * 49152; rb1[i] = bb1 + OFF_WO + i * 49152; asm volatile("" : "+v"(rb0[i]), "+v"(rb1[i])); }
    auto loadf = [&](int kt, bf16x8 (&a)[2][4], bf16x8 (&b)[2][2]) {
#pragma unroll
      for (int s = 0; s < 2; ++s) {
        const char* sA = smem + ra[AS[kt] >> 16] + (AS[kt] & 65535) + s * 8192;
        const int hi = bs(kt) >= OFF_WO + 49152, ob = bs(kt) - OFF_WO - hi * 49152 + s * 4096;
        b[s][0] = cat8(tr_read(smem + rb0[hi] + ob), tr_read(smem + rb0[hi] + ob + 2048));
        b[s][1] = cat8(tr_read(smem + rb1[hi] + ob), tr_read(smem + rb1[hi] + ob + 2048));
#pragma unroll
        for (int rb = 0; rb < 4; ++rb) a[s][rb] = *(const bf16x8*)(sA + rb * 1024);
      }
    };
    auto mma = [&](bf16x8 (&a)[2][4], bf16x8 (&b)[2][2]) {
#pragma unroll
      for (int i = 0; i < 16; ++i) {
        const int rb = i >> 2, cb = ((i >> 1) ^ rb) & 1, s = ((i + 1) >> 1) & 1;
        acc[rb][cb] = MFMA(b[s][cb], a[s][rb], acc[rb][cb]);
      }
    };
    unsigned fo = lane < 16 && lane != hp ? (WS_FLAGS + (unsigned)(rt * 16 + lane) * FLS) ^ P : 0u;
    const u64 ko = korder(bid & 3, hp);
    unsigned kf = kfirst(ko), kf2 = kfirst(ko >> 16) | (1u << knib(ko, 9));
    unsigned jo = (unsigned)((((rt & 7) * 128 + (w - 1) * 64 + lane) * 3072 + (hp & 3) * 1024 + (hp | 3) * 64) * 2);
    unsigned fo1 = ((kf >> (lane & 31)) & 1) ? fo : 0u, fo2 = ((kf2 >> (lane & 31)) & 1) ? fo : 0u;
    fo = (((kf | kf2) >> (lane & 31)) & 1) ? 0u : fo;
    asm volatile("" : "+v"(fo), "+v"(fo1), "+v"(fo2));
    asm volatile("" : "+s"(kf), "+s"(kf2));
    publish(rsW, tid, rt * 16 + hp, E + (unsigned)(layer + 1), P);
    ST(layer * 24 + 13)
    u64 fl1 = 0;
    if (w == 0) asm volatile("global_load_dwordx2 %0, %1, %2 sc1" : "+v"(fl1) : "v"(fo1), "s"(ws) : "memory");
    if (layer == 0) {
      const unsigned xo = (unsigned)(((m0 + wm * 64 + fr) * DM + hp * 64 + wn * 32 + g * 8) * 2);
      asm volatile("global_load_dwordx4 v[64:67], %1, %3\n\tglobal_load_dwordx4 v[68:71], %1, %4\n\tglobal_load_dwordx4 v[72:75], %2, %3\n\tglobal_load_dwordx4 v[76:79], %2, %4"
                   : "+{v[64:79]}"(xq) : "v"(xo), "v"(xo + 32 * DM * 2), "s"(x0), "s"(x0 + 16 * DM) : "memory");
    }
    loadf(0, fa[0], fb[0]);
    unsigned junk = 0;
    if (layer < 3 && (hp & 7) < 3 && (w == 1 || w == 2))
      asm volatile("global_load_dword %0, %1, %2" : "+v"(junk) : "v"(jo), "s"(wnx) : "memory");
    __builtin_amdgcn_sched_barrier(0);
    bf16x8 id[2];
#pragma unroll
    for (int cb = 0; cb < 2; ++cb) {
      const unsigned one = (g >> 1) == cb && (g & 1) == (fr >> 3) ? 0x3F80u << ((fr & 1) * 16) : 0u;
      u32x4 t;
#pragma unroll
      for (int j = 0; j < 4; ++j) t[j] = ((fr >> 1) & 3) == j ? one : 0u;
      id[cb] = __builtin_bit_cast(bf16x8, t);
      asm volatile("" : "+v"(id[cb]));
    }
    asm volatile("" : "+v"(co[0]), "+v"(co[1]));
#pragma unroll
    for (int rb = 0; rb < 4; ++rb) { acc[rb][0] = f32x4{0, 0, 0, 0}; acc[rb][1] = f32x4{0, 0, 0, 0}; }
    mma(fa[0], fb[0]);
    __builtin_amdgcn_sched_barrier(0);
    WAITL0;
    u64 miss = 0;
    if (w == 0) { ARRIVED("+v"(fl1)); if (__ballot(fl1 != E + (unsigned)(layer + 1)) & kf) poll_eq(ws, fo1, E + (unsigned)(layer + 1), kf, kf); miss = 0xffffu & ~(kf | kf2 | (1u << hp)); }
    ST(layer * 24 + 14)
    __builtin_amdgcn_sched_barrier(0);
    BAR;
    __builtin_amdgcn_sched_barrier(0);
    asm volatile("" ::: "memory");
    if (layer == 0) WAITV(0);
    __builtin_amdgcn_sched_barrier(0);
    asm volatile("" : "+{v[64:79]}"(xq));
    const bf16x8 xf[4] = {__builtin_bit_cast(bf16x8, quad<0>(xq)), __builtin_bit_cast(bf16x8, quad<1>(xq)), __builtin_bit_cast(bf16x8, quad<2>(xq)), __builtin_bit_cast(bf16x8, quad<3>(xq))};
#pragma unroll
    for (int rb = 0; rb < 4; ++rb) { acc[rb][0] = MFMA(id[0], xf[rb], acc[rb][0]); acc[rb][1] = MFMA(id[1], xf[rb], acc[rb][1]); }
    if (w == 0) poll_eq(ws, fo2, E + (unsigned)(layer + 1), kf2, kf2);
    u64 fl2 = 0;
    auto step = [&](auto ktc) {
      constexpr int kt = decltype(ktc)::value;
      __builtin_amdgcn_sched_barrier(0);
      if (kt == 5 && miss) {
        ARRIVED("+v"(fl2));
        if (__ballot(fl2 != E + (unsigned)(layer + 1)) & miss) poll_eq(ws, fo, E + (unsigned)(layer + 1), miss, ~0ull);
      }
      WAITL0;
      BAR;
      if (kt == 2 && miss) asm volatile("global_load_dwordx2 %0, %1, %2 sc1" : "+v"(fl2) : "v"(fo), "s"(ws) : "memory");
      if (kt == 0) { ST(layer * 24 + 15) }
      __builtin_amdgcn_sched_barrier(0);
      loadf(kt + 1, fa[(kt + 1) & 1], fb[(kt + 1) & 1]);
      if (kt == 0) return;
      mma(fa[kt & 1], fb[kt & 1]);
#pragma unroll
      for (int i = 0; i < 4; ++i) {
        __builtin_amdgcn_sched_group_barrier(0x008, 1, 0);
        __builtin_amdgcn_sched_group_barrier(0x100, 1, 0);
        __builtin_amdgcn_sched_group_barrier(0x008, 1, 0);
        __builtin_amdgcn_sched_group_barrier(0x100, 3, 0);
      }
#pragma unroll
      for (int i = 0; i < 8; ++i) __builtin_amdgcn_sched_group_barrier(0x008, 1, 0);
    };
    step(IC<0>{}); step(IC<1>{}); step(IC<2>{}); step(IC<3>{}); step(IC<4>{}); step(IC<5>{}); step(IC<6>{}); step(IC<7>{});
    step(IC<8>{}); step(IC<9>{}); step(IC<10>{}); step(IC<11>{}); step(IC<12>{}); step(IC<13>{}); step(IC<14>{});
    WAITV(0);
    asm volatile("" :: "v"(junk));
    __builtin_amdgcn_sched_barrier(0);
    auto put = [&](int rb) {
#pragma unroll
      for (int cb = 0; cb < 2; ++cb) lds<u32x2>(co[cb] + rb * 2048) = u32x2{pk2(acc[rb][cb][0], acc[rb][cb][1]), pk2(acc[rb][cb][2], acc[rb][cb][3])};
    };
#pragma unroll
    for (int rb = 0; rb < 4; ++rb)
#pragma unroll
      for (int cb = 0; cb < 2; ++cb) acc[rb][cb] = MFMA(fb[1][0][cb], fa[1][0][rb], acc[rb][cb]);
#pragma unroll
    for (int rb = 0; rb < 4; ++rb) {
      __builtin_amdgcn_sched_barrier(0);
#pragma unroll
      for (int cb = 0; cb < 2; ++cb) acc[rb][cb] = MFMA(fb[1][1][cb], fa[1][1][rb], acc[rb][cb]);
      __builtin_amdgcn_sched_barrier(0);
      if (rb >= 2) put(rb - 2);
    }
    __builtin_amdgcn_sched_barrier(0);
    ST(layer * 24 + 16)
    put(2); put(3);
  }
  WAITL0; BAR;
  asm volatile("" ::: "memory");
  u32x4 xn[2];
#pragma unroll
  for (int i = 0; i < 2; ++i) xn[i] = lds<u32x4>(pxr + OFF_C + i * 1024);
  if (layer == 3) {
    const __amdgpu_buffer_rsrc_t rsO = __builtin_amdgcn_make_buffer_rsrc((void*)uni(out), (short)0, SEQ * DM * 2, 0x00020000);
    __builtin_amdgcn_raw_buffer_store_b128(xn[0], rsO, rmo, 0, 16);
    __builtin_amdgcn_raw_buffer_store_b128(xn[1], rsO, rmo + 8 * DM * 2, 0, 16);
  } else {
    __builtin_amdgcn_raw_buffer_store_b128(xn[0], rsW, WS_X + layer * (SEQ * DM * 2) + imo, 0, 16);
    __builtin_amdgcn_raw_buffer_store_b128(xn[1], rsW, WS_X + layer * (SEQ * DM * 2) + imo + 1024, 0, 16);
    float mean[2], m2[2];
#pragma unroll
    for (int i = 0; i < 2; ++i) {
      typedef __attribute__((ext_vector_type(2))) __bf16 bf2;
      const bf2 one2 = __builtin_bit_cast(bf2, 0x3F803F80u);
      float sa = 0.f, sq = 0.f;
#pragma unroll
      for (int e = 0; e < 4; ++e) {
        const unsigned u = xn[i][e];
        const bf2 t = __builtin_bit_cast(bf2, u);
        sa = __builtin_amdgcn_fdot2_f32_bf16(t, one2, sa, false);
        sq = __builtin_amdgcn_fdot2_f32_bf16(t, t, sq, false);
      }
      mean[i] = sa;
      m2[i] = sq;
    }
#define DPA(c) "v_add_f32_dpp %0, %0, %0 " c "\n\tv_add_f32_dpp %1, %1, %1 " c "\n\tv_add_f32_dpp %2, %2, %2 " c "\n\tv_add_f32_dpp %3, %3, %3 " c "\n\t"
    asm volatile("s_nop 3\n\t" DPA("quad_perm:[1,0,3,2] row_mask:0xf bank_mask:0xf") DPA("quad_perm:[2,3,0,1] row_mask:0xf bank_mask:0xf")
                 DPA("row_half_mirror row_mask:0xf bank_mask:0xf") "s_nop 0" : "+v"(mean[0]), "+v"(m2[0]), "+v"(mean[1]), "+v"(m2[1]));
    if ((lane & 7) < 2) {
      const int i = lane & 1;
      const u32x2 sv = {__float_as_uint(i ? mean[1] : mean[0]), __float_as_uint(i ? m2[1] : m2[0])};
      __builtin_amdgcn_raw_buffer_store_b64(sv, rsW, WS_ST + layer * (SEQ * 128) + (unsigned)((rt & 7) * 32768 + (hp >> 3) * 16384 + (hp & 7) * 1024 + (r0 + i * 8) * 8) + ((rt >> 3) * 8192 ^ P), 0, 16);
    }
  }
  ST(layer * 24 + 17)
  if (layer < 3 && w >= 4) { WAITV(0); BAR; }
  ST(layer * 24 + 18)
}

__global__ __launch_bounds__(512, 2) void mega_kernel(const u16* __restrict__ x0, const u16* __restrict__ wqkvA,
                                                      const u16* __restrict__ woA, const u16* __restrict__ wqkvB,
                                                      const u16* __restrict__ wphi, const u16* __restrict__ woB,
                                                      u16* __restrict__ out, char* __restrict__ ws) {
  unsigned P = 0;
  extern __shared__ __attribute__((aligned(16))) char smem[];
  const int w = __builtin_amdgcn_readfirstlane(threadIdx.x >> 6);
  u64 E = 0;
  {
    const int bid = blockIdx.x, lane = threadIdx.x & 63;
    const int rt = 2 * (bid & 4) + ((bid >> 3) & 7), hp = 4 * (bid & 3) + (bid >> 6), m0 = rt * 128;
    if (w < 4) {
      if (w == 3) {
        unsigned long long pc;
        unsigned len;
        asm volatile("s_getpc_b64 %0\n.Lmk_pc:\n\ts_mov_b32 %1, .Lmk_end-.Lmk_pc" : "=s"(pc), "=s"(len));
        const unsigned per = ((len >> 10) + 1) * 128, off = ((bid >> 3) & 7) * per + (lane * 128 < per ? lane * 128 : 0);
        asm volatile("s_mov_b32 m0, %2\n\ts_nop 0\n\tglobal_load_lds_dword %0, %1" :: "v"(off < len - 4 ? off : 0u), "s"(pc), "s"(lds_addr(smem) + LDS_TOTAL - 1024) : "memory", "m0");
      }
      const unsigned xo = (unsigned)((m0 + hp * 8 + w * 2) * (DM * 2) + lane * 16);
      glds4v(x0, xo, xo, xo, xo, lds_addr(smem) + 122880 + w * 4096);
      asm volatile("s_mov_b32 m0, %2\n\ts_nop 0\n\tglobal_load_lds_dword %0, %1" :: "v"(lane * 4), "s"(ws), "s"(lds_addr(smem) + OFF_E) : "memory", "m0");
    } else {
      const int lw = w & 3;
      const int kB = 16 * lw + (lane >> 3), fB = (kB >> 1) & 1;
      const unsigned vB0 = (unsigned)((kB * 3072 + hp * 64 + (((((lane & 7) >> 1) ^ fB) << 1) | (lane & 1)) * 8) * 2);
      const unsigned vB1 = (unsigned)(((kB + 8) * 3072 + hp * 64 + (((((lane & 7) >> 1) ^ (fB | 2)) << 1) | (lane & 1)) * 8) * 2) - 1024u;
      const unsigned vA = (unsigned)(((m0 + lw * 32 + (lane >> 3)) * DM + ((lane & 7) ^ (lane >> 3)) * 8) * 2);
#pragma unroll
      for (int s = 0; s < 1; ++s) {
        const int kt = (rt & 8) + s;
        const unsigned sA = lds_addr(smem) + s * 40960, sB = sA + 16384;
        glds4v(x0 + kt * 64, vA, vA + 15360u, vA + 2 * 15360u, vA + 3 * 15360u, sA + lw * 4096);
        const u16* b = wqkvA + (size_t)kt * 64 * 3072;
        glds4v(b, vB0, vB1, vB0, vB1, sB + lw * 6144);
        glds2v(b + 2048, vB0, vB1, sB + lw * 6144 + 4096);
      }
    }
  }
  f32x16 xq;
#pragma unroll 1
  for (int layer = 0; layer < 4; ++layer) {
    if (layer & 1) g1_phase<1>(smem, ws, P, E, w, xq, layer, x0, wqkvB, woB, wphi, nullptr, nullptr);
    else g1_phase<0>(smem, ws, P, E, w, xq, layer, x0, wqkvA, woA, nullptr, wqkvB, woB);
    g2_phase(smem, ws, P, E, w, xq, layer, x0, (layer & 1) ? woB : woA, (layer & 1) ? wqkvA : wqkvB, out);
  }
  asm volatile(".Lmk_end:");
  WAITV(0);
  if (blockIdx.x == 0 && w == 0 && lane_id() == 0) __hip_atomic_store((gu64*)ws, E + 16u, __ATOMIC_RELAXED, __HIP_MEMORY_SCOPE_AGENT);
}

extern "C" void kernel_launch(void* const* d_in, const int* in_sizes, int n_in,
                              void* d_out, int out_size, void* d_ws, size_t ws_size,
                              hipStream_t stream) {
  hipFuncSetAttribute((const void*)mega_kernel, hipFuncAttributeMaxDynamicSharedMemorySize, LDS_TOTAL);
  mega_kernel<<<256, 512, LDS_TOTAL, stream>>>((const u16*)d_in[0], (const u16*)d_in[1], (const u16*)d_in[2], (const u16*)d_in[4],
                                               (const u16*)d_in[5], (const u16*)d_in[6], (u16*)d_out, (char*)d_ws);
}
```

```cpp
#include <hip/hip_runtime.h>
#include <hip/hip_bf16.h>
#include <stdint.h>
#include <type_traits>
#pragma clang diagnostic ignored "-Winline-asm"
#pragma clang diagnostic ignored "-Wunused-value"

typedef unsigned short u16;
typedef __attribute__((ext_vector_type(8))) short bf16x8;
typedef __attribute__((ext_vector_type(4))) short bf16x4;
typedef __attribute__((ext_vector_type(4))) float f32x4;
typedef __attribute__((ext_vector_type(4))) unsigned u32x4;
typedef __attribute__((ext_vector_type(2))) unsigned u32x2;
typedef __attribute__((ext_vector_type(8))) float f32x8;
typedef __attribute__((ext_vector_type(16))) unsigned u32x16;
typedef __attribute__((ext_vector_type(16))) float f32x16;
typedef __attribute__((ext_vector_type(32))) float f32x32;
template <int I, typename T> __device__ __forceinline__ f32x4 quad(const T& v) { return __builtin_shufflevector(v, v, 4 * I, 4 * I + 1, 4 * I + 2, 4 * I + 3); }

constexpr int SEQ = 2048, DM = 1024, NH = 32;
#define ST(i)
constexpr int KVP = 64 * 32 + 64;

typedef __attribute__((ext_vector_type(2))) float f32x2;
typedef __attribute__((ext_vector_type(2))) __bf16 bf16x2_t;
__device__ __forceinline__ unsigned pk2(float a, float b) {
  f32x2 v = {a, b};
  bf16x2_t r = __builtin_convertvector(v, bf16x2_t);
  return __builtin_bit_cast(unsigned, r);
}

__device__ __forceinline__ bf16x4 tr_read(const char* p) {
  return __builtin_amdgcn_ds_read_tr16_b64_v4i16((__attribute__((address_space(3))) bf16x4*)p);
}
template <typename T> __device__ __forceinline__ __attribute__((address_space(3))) T& lds(unsigned a) { return *(__attribute__((address_space(3))) T*)(size_t)a; }
__device__ __forceinline__ bf16x4 tr_read(unsigned a) { return __builtin_amdgcn_ds_read_tr16_b64_v4i16(&lds<bf16x4>(a)); }
__device__ __forceinline__ bf16x8 cat8(bf16x4 a, bf16x4 b) {
  bf16x8 f = {a[0], a[1], a[2], a[3], b[0], b[1], b[2], b[3]};
  return f;
}
__device__ __forceinline__ void glds16s(const void* sbase, unsigned voff, unsigned lds_base) {
  asm volatile("s_mov_b32 m0, %2\n\ts_nop 0\n\tglobal_load_lds_dwordx4 %0, %1" :: "v"(voff), "s"(sbase), "s"(lds_base) : "memory", "m0");
}
__device__ __forceinline__ void glds16c(const void* sbase, unsigned voff, unsigned lds_base) {
  asm volatile("s_mov_b32 m0, %2\n\ts_nop 0\n\tglobal_load_lds_dwordx4 %0, %1 sc1" :: "v"(voff), "s"(sbase), "s"(lds_base) : "memory", "m0");
}
#define GL_ "\n\tglobal_load_lds_dwordx4 "
__device__ __forceinline__ void glds4c(const void* sbase, unsigned v, unsigned lds_base) {
  asm volatile("s_mov_b32 m0, %2\n\ts_nop 0" GL_ "%0, %1 sc1" GL_ "%0, %1 offset:1024 sc1" GL_ "%0, %1 offset:2048 sc1" GL_ "%0, %1 offset:3072 sc1"
               :: "v"(v), "s"(sbase), "s"(lds_base) : "memory", "m0");
}
__device__ __forceinline__ void glds4v(const void* sbase, unsigned v0, unsigned v1, unsigned v2, unsigned v3, unsigned lds_base) {
  asm volatile("s_mov_b32 m0, %5\n\ts_nop 0" GL_ "%0, %4" GL_ "%1, %4 offset:1024" GL_ "%2, %4 offset:2048" GL_ "%3, %4 offset:3072"
               :: "v"(v0), "v"(v1), "v"(v2), "v"(v3), "s"(sbase), "s"(lds_base) : "memory", "m0");
}
__device__ __forceinline__ void glds2v(const void* sbase, unsigned v0, unsigned v1, unsigned lds_base) {
  asm volatile("s_mov_b32 m0, %3\n\ts_nop 0" GL_ "%0, %2" GL_ "%1, %2 offset:1024" :: "v"(v0), "v"(v1), "s"(sbase), "s"(lds_base) : "memory", "m0");
}
__device__ __forceinline__ void gld16(u32x4& d, unsigned voff, const void* sbase) {
  asm volatile("global_load_dwordx4 %0, %1, %2" : "=v"(d) : "v"(voff), "s"(sbase) : "memory");
}
__device__ __forceinline__ unsigned lds_addr(const void* p) { return (unsigned)(size_t)p; }
#define WAITV(n) asm volatile("s_waitcnt vmcnt(" #n ")" ::: "memory")
#define WAITVN(n) asm volatile("s_waitcnt vmcnt(%0)" :: "n"(n) : "memory")
#define WAITL0 do { asm volatile("" ::: "memory"); __builtin_amdgcn_s_waitcnt(0xC07F); asm volatile("" ::: "memory"); } while (0)
#define BAR __builtin_amdgcn_s_barrier()
#define ARRIVED(...) do { WAITV(0); __builtin_amdgcn_sched_barrier(0); asm volatile("" : __VA_ARGS__); } while (0)

__device__ __forceinline__ float vmaxf(float a, float b) { float r; asm("v_max_f32 %0, %1, %2" : "=v"(r) : "v"(a), "v"(b)); return r; }
template <int N> __device__ __forceinline__ float dpp_ror(float x) {
  return __uint_as_float(__builtin_amdgcn_update_dpp(0u, __float_as_uint(x), 0x120 + N, 0xf, 0xf, false));
}
__device__ __forceinline__ float r16_sum(float x) { x += dpp_ror<8>(x); x += dpp_ror<4>(x); x += dpp_ror<2>(x); return x + dpp_ror<1>(x); }
__device__ __forceinline__ float vmax3f(float a, float b, float c) { float r; asm("v_max3_f32 %0, %1, %2, %3" : "=v"(r) : "v"(a), "v"(b), "v"(c)); return r; }
__device__ __forceinline__ void max8pn(const f32x4& a, const f32x4& b, float& mx, float& mn) {
  asm("v_max3_f32 %0, %2, %3, %4\n\tv_max3_f32 %1, -%2, -%3, -%4\n\tv_max3_f32 %0, %0, %5, %6\n\tv_max3_f32 %1, %1, -%5, -%6\n\t"
      "v_max3_f32 %0, %0, %7, %8\n\tv_max3_f32 %1, %1, -%7, -%8\n\tv_max_f32 %0, %0, %9\n\tv_max_f32 %1, %1, -%9\n\ts_nop 0"
      : "=&v"(mx), "=&v"(mn) : "v"(a[0]), "v"(a[1]), "v"(a[2]), "v"(a[3]), "v"(b[0]), "v"(b[1]), "v"(b[2]), "v"(b[3]));
}
__device__ __forceinline__ float vmaxf_n(float a, float b) { float r; asm("v_max_f32 %0, %1, %2\n\ts_nop 0" : "=v"(r) : "v"(a), "v"(b)); return r; }
__device__ __forceinline__ float vmax3f_n(float a, float b, float c) { float r; asm("v_max3_f32 %0, %1, %2, %3\n\ts_nop 0" : "=v"(r) : "v"(a), "v"(b), "v"(c)); return r; }
template <bool MAXOP> __device__ __forceinline__ float rop(float a, float b) { return MAXOP ? vmaxf_n(a, b) : a + b; }
template <bool MAXOP> __device__ __forceinline__ void xr2(float& a, float& b) {
  auto r = __builtin_amdgcn_permlane16_swap(__float_as_uint(a), __float_as_uint(b), false, false);
  unsigned u = __float_as_uint(rop<MAXOP>(__uint_as_float(r[0]), __uint_as_float(r[1])));
  auto r2 = __builtin_amdgcn_permlane32_swap(u, u, false, false);
  u = __float_as_uint(rop<MAXOP>(__uint_as_float(r2[0]), __uint_as_float(r2[1])));
  auto r3 = __builtin_amdgcn_permlane16_swap(u, u, false, false);
  a = __uint_as_float(r3[0]); b = __uint_as_float(r3[1]);
}
template <bool MAXOP> __device__ __forceinline__ void xr4(float& a, float& b, float& c, float& d) {
  auto r = __builtin_amdgcn_permlane16_swap(__float_as_uint(a), __float_as_uint(b), false, false);
  auto q = __builtin_amdgcn_permlane16_swap(__float_as_uint(c), __float_as_uint(d), false, false);
  const unsigned ab = __float_as_uint(rop<MAXOP>(__uint_as_float(r[0]), __uint_as_float(r[1])));
  const unsigned cd = __float_as_uint(rop<MAXOP>(__uint_as_float(q[0]), __uint_as_float(q[1])));
  auto t = __builtin_amdgcn_permlane32_swap(ab, cd, false, false);
  const unsigned u = __float_as_uint(rop<MAXOP>(__uint_as_float(t[0]), __uint_as_float(t[1])));
  auto h = __builtin_amdgcn_permlane32_swap(u, u, false, false);
  auto p0 = __builtin_amdgcn_permlane16_swap(h[0], h[0], false, false);
  auto p1 = __builtin_amdgcn_permlane16_swap(h[1], h[1], false, false);
  a = __uint_as_float(p0[0]); b = __uint_as_float(p0[1]); c = __uint_as_float(p1[0]); d = __uint_as_float(p1[1]);
}

__device__ __forceinline__ unsigned xs4(float a, float b, float c, float d) {
  auto r = __builtin_amdgcn_permlane16_swap(__float_as_uint(a), __float_as_uint(b), false, false);
  auto q = __builtin_amdgcn_permlane16_swap(__float_as_uint(c), __float_as_uint(d), false, false);
  auto t = __builtin_amdgcn_permlane32_swap(__float_as_uint(__uint_as_float(r[0]) + __uint_as_float(r[1])), __float_as_uint(__uint_as_float(q[0]) + __uint_as_float(q[1])), false, false);
  return __float_as_uint(__uint_as_float(t[0]) + __uint_as_float(t[1]));
}

#define MFMA(a, b, c) __builtin_amdgcn_mfma_f32_16x16x32_bf16(a, b, c, 0, 0, 0)

typedef unsigned long long u64;
typedef __attribute__((address_space(1))) unsigned long long gu64;
constexpr unsigned WS_PL = 0x1a0000, WS_PR = 1024;
constexpr unsigned WS_FLAGS = 16384;
constexpr int FLS = 64;
constexpr unsigned WS_G0 = 65536;
constexpr unsigned WS_CSP = WS_G0 + SEQ * 16;
constexpr unsigned WS_ST = WS_CSP + 16 * 16 * 768;
constexpr unsigned WS_X = 1u << 21;
constexpr unsigned WS_O = WS_X + 3u * SEQ * DM * 2;
constexpr unsigned KVPB = KVP * 2, KVFB = KVP * 4;
constexpr unsigned EMPTY = 0xffffffffu;
constexpr unsigned WS_KV = WS_O + 4u * SEQ * DM * 2;
constexpr unsigned KVLB = 256u * 16384;
constexpr unsigned WS_KF = 0x110000, KFLB = 17u * 16384;
constexpr unsigned WS_HL = WS_KV + 2u * KVLB;
static_assert(WS_ST % 16384 == 0 && WS_ST + 3u * SEQ * 128 <= WS_KF && WS_KF + 2 * KFLB <= WS_PL && WS_PL + 16384 <= WS_X && (WS_X >> 21 & 1) && (WS_O >> 21 & 1) && (WS_KV >> 21 & 1) && (WS_HL >> 21 & 1) && WS_HL % (1u << 21) == 0, "");
constexpr unsigned WS_CSA = WS_HL + 512u * 4096;
constexpr unsigned WS_CPA = WS_CSA + 256u * 1024;
constexpr unsigned WS_END = WS_CPA + 16 * 16 * 768;
constexpr int LDS_TOTAL = 163840;
constexpr int OFF_E = 139264;
constexpr int OFF_WO = 49152;
template <int V> using IC = std::integral_constant<int, V>;
__device__ __forceinline__ u32x4 empty4() { unsigned e = EMPTY; asm volatile("" : "+v"(e)); return u32x4{e, e, e, e}; }
__device__ __forceinline__ u64 korder(int v, int own) {
  const u64 t = v == 0 ? 0x3bf7210edca98654ull : v == 1 ? 0xb37f654210edca98ull : v == 2 ? 0x3bf7a98654210edcull : 0xb37fedca98654210ull;
  const u64 inv = v == 0 ? 0xd876e543c210fba9ull : v == 1 ? 0xc543f210dba9e876ull : v == 2 ? 0xd210eba9c876f543ull : 0xcba9f876d543e210ull;
  const u64 low = (1ull << (4 * ((inv >> (4 * own)) & 15))) - 1ull;
  return (u64)own | ((t & low) << 4) | (t & ~((low << 4) | 15ull));
}
__device__ __forceinline__ u64 korder0(int rt) {
  const int r = rt & 7;
  const u64 own = (0x765432765432ull >> (8 * ((0x4924 >> (2 * r)) & 3))) & 0xffffffull, oth = (0x7654321076543210ull >> (4 * r)) & 0xffffffffull;
  const u64 t = 0x10ull | (oth << 8) | (own << 40);
  return t | (rt < 8 ? 0x0000008888888800ull : 0x8888880000000088ull);
}
__device__ __forceinline__ int knib(u64 t, int s) { return (int)(t >> (4 * s)) & 15; }
__device__ __forceinline__ unsigned kfirst(u64 t) { return (1u << knib(t, 1)) | (1u << knib(t, 2)) | (1u << knib(t, 3)) | (1u << knib(t, 4)); }
__device__ __forceinline__ int kv1(int bid) { return (bid + (0x4924 >> ((bid >> 2) & 14))) & 3; }

template <typename T> __device__ __forceinline__ T* uni(T* p) {
  const unsigned long long v = (unsigned long long)p;
  const unsigned lo = __builtin_amdgcn_readfirstlane((unsigned)v), hi = __builtin_amdgcn_readfirstlane((unsigned)(v >> 32));
  return (T*)(((unsigned long long)hi << 32) | lo);
}
__device__ __forceinline__ u64 uni2(unsigned lo, unsigned hi) {
  lo = __builtin_amdgcn_readfirstlane(lo); hi = __builtin_amdgcn_readfirstlane(hi);
  return ((u64)hi << 32) | lo;
}
__device__ __forceinline__ int lane_id() {
  int l;
  asm volatile("v_mbcnt_lo_u32_b32 %0, -1, 0\n\tv_mbcnt_hi_u32_b32 %0, -1, %0" : "=v"(l));
  __builtin_assume(l >= 0 && l < 64);
  return l;
}
#define CODE_TOUCH(n, from, to) \
  unsigned long long pc; unsigned v; \
  { unsigned o, len; \
    asm volatile("s_getpc_b64 %0\n.Lmk_t" #n ":\n\ts_mov_b32 %1, " from "-.Lmk_t" #n "+0x40000\n\ts_mov_b32 %2, " to "-" from : "=s"(pc), "=s"(o), "=s"(len)); \
    pc -= 0x40000; v = (unsigned)(((bid >> 3) & 7) + lane * 8) * 128; v = o + ((int)v < (int)len ? v : 0u); }
#define IDS \
  const int lane = lane_id(); \
  asm volatile("" : "+s"(w)); \
  const int tid = w * 64 + lane; \
  int bid = blockIdx.x; asm volatile("" : "+s"(bid)); \
  ws = uni(ws); asm volatile("" : "+s"(ws)); \
  layer = __builtin_amdgcn_readfirstlane(layer); x0 = uni(x0); wo = uni(wo); \
    \
  const int rt = 2 * (bid & 4) + ((bid >> 3) & 7); \
  const int hp = 4 * (bid & 3) + (bid >> 6); \
  const int m0 = rt * 128; \
  P = __builtin_amdgcn_readfirstlane(P); \
  const unsigned SX = (unsigned)((rt & 7) * 524288) + ((((rt >> 3) ^ (rt >> 2) ^ 1) & 1) * 8192 ^ P); (void)SX; \
  const int fr = lane & 15, g = lane >> 4; \
  const int q4 = (lane & 15) >> 2, p4 = lane & 3; \
  const __amdgpu_buffer_rsrc_t rsW = __builtin_amdgcn_make_buffer_rsrc((void*)ws, (short)0, (int)WS_END, 0x00020000); \
  (void)m0; (void)fr; (void)q4; (void)p4; (void)rsW; (void)g;

__device__ __forceinline__ void publish(__amdgpu_buffer_rsrc_t rsW, int tid, unsigned idx, u64 v, unsigned P) {
  unsigned off = (WS_FLAGS + idx * FLS) ^ P;
  asm volatile("" : "+v"(off), "+v"(v));
  WAITV(0);
  WAITL0; BAR;
  if (tid == 192) __builtin_amdgcn_raw_buffer_store_b64(__builtin_bit_cast(u32x2, v), rsW, off, 0, 16);
  asm volatile("" ::: "memory");
}
__device__ __forceinline__ u64 poll_flags(char* ws, unsigned P, u64 E, int set, int rt, int hp, int lane, unsigned n, u64 mask, u64 now = ~0ull) {
  u64 miss = 0;
  const int l = lane & 15, g = lane >> 4;
  const int fi = set * 256 + (g == 2 ? l * 16 + hp : rt * 16 + l);
  const unsigned need = ((mask >> lane) & 1) ? n : 0u;
  gu64* fp = (gu64*)(ws + (need ? (WS_FLAGS + fi * FLS) ^ P : 0));
  _Pragma("unroll 1") for (unsigned spins = 0; spins < (1u << 18); ++spins) {
    const u64 d = __hip_atomic_load(fp, __ATOMIC_RELAXED, __HIP_MEMORY_SCOPE_AGENT) - E;
    const bool ok = (need == 0u) || (d >= (u64)need && d <= 16ull);
    miss = __ballot(!ok);
    if (!(miss & now)) break;
    __builtin_amdgcn_s_sleep(1);
  }
  asm volatile("s_waitcnt vmcnt(0)" ::: "memory");
  return miss;
}

__device__ __forceinline__ u64 poll_eq(const char* ws, unsigned off, u64 want, u64 mask, u64 now) {
  u64 miss;
  _Pragma("unroll 1") for (unsigned spins = 0; spins < (1u << 18); ++spins) {
    u64 d;
    asm volatile("global_load_dwordx2 %0, %1, %2 sc1\n\ts_waitcnt vmcnt(0)" : "=v"(d) : "v"(off), "s"(ws) : "memory");
    miss = __ballot(d != want) & mask;
    if (!(miss & now)) break;
    __builtin_amdgcn_s_sleep(1);
  }
  return miss;
}

#define G1_MACROS \
  ".macro G1M rb, j, s, z\n" \
  " .set g1d, 160+24*(\\rb)+4*(\\j)\n .set g1a, 80+40*(\\s)+4*(\\rb)\n .set g1b, 96+40*(\\s)+4*(\\j)\n" \
  " .if (\\j) >= 4\n  .set g1t, g1a\n  .set g1a, g1b\n  .set g1b, g1t\n .endif\n" \
  " .if \\z\n  v_mfma_f32_16x16x32_bf16 v[g1d:g1d+3], v[g1b:g1b+3], v[g1a:g1a+3], 0\n" \
  " .else\n  v_mfma_f32_16x16x32_bf16 v[g1d:g1d+3], v[g1b:g1b+3], v[g1a:g1a+3], v[g1d:g1d+3]\n .endif\n" \
  ".endm\n" \
  ".macro G1RA rb, s, ra, oa\n" \
  " ds_read_b128 v[80+40*(\\s)+4*(\\rb):83+40*(\\s)+4*(\\rb)], \\ra offset:(\\oa)+(((\\rb)*2048)^(4096*(1-%[hh])))\n" \
  ".endm\n" \
  ".macro G1RB k, s, r0, r1, ob\n" \
  " .if (\\k) & 2\n  ds_read_b64_tr_b16 v[96+40*(\\s)+2*(\\k):97+40*(\\s)+2*(\\k)], \\r1 offset:(\\ob)+((\\k)>>2)*2048+((\\k)&1)*512\n" \
  " .else\n  ds_read_b64_tr_b16 v[96+40*(\\s)+2*(\\k):97+40*(\\s)+2*(\\k)], \\r0 offset:(\\ob)+((\\k)>>2)*2048+((\\k)&1)*512\n .endif\n" \
  ".endm\n" \
    \
  ".macro G1BK i, g, r0, r1, ob\n G1RB 4*(\\g)+((\\i)&3), (\\i)>>2, \\r0, \\r1, (\\ob)+((\\i)>>2)*12288\n.endm\n" \
  ".macro G1AK rb, s, a0, a1, oa\n .if \\s\n  G1RA \\rb, 1, \\a1, \\oa\n .else\n  G1RA \\rb, 0, \\a0, \\oa\n .endif\n.endm\n" \
  ".macro G1RDB g, r0, r1, ob\n .set g1i, 0\n .rept 8\n  G1BK g1i, \\g, \\r0, \\r1, \\ob\n  .set g1i, g1i+1\n .endr\n.endm\n" \
  ".macro G1RDA a0, a1, oa\n .set g1i, 0\n .rept 8\n  G1AK (g1i>>1)^2, g1i&1, \\a0, \\a1, \\oa\n  .set g1i, g1i+1\n .endr\n.endm\n" \
    \
  ".macro G1Q g, rb, q, i, z\n G1M \\rb, 2*(\\g)+((((\\i)>>1)^(\\q))&1), (((\\i)+1)>>1)&1, (\\z)&&(((\\i)&1)==0)\n.endm\n" \
    \
  ".macro G1SG g, z, rd, a0, a1, r0, r1, o\n" \
  " .set g1i, 0\n .rept 16\n" \
  "  .if ((\\g) == 0) && (g1i == 8) && (\\rd)\n   s_waitcnt lgkmcnt(7)\n   s_nop 0\n  .endif\n" \
  "  .if ((\\g) == 0) && (g1i == 15)\n   s_waitcnt lgkmcnt(0)\n  .endif\n" \
  "  G1Q \\g, (g1i>>2)^(2-((\\g)&1)), g1i>>2, g1i&3, \\z\n" \
  "  .if \\rd\n" \
  "   .if (\\g) == 0\n    .if g1i < 7\n     G1BK g1i, 2, \\r0, \\r1, \\o\n    .elseif g1i == 8\n     G1BK 7, 2, \\r0, \\r1, \\o\n    .endif\n" \
  "   .elseif (\\g) == 1\n    .if g1i < 8\n     G1BK g1i, 0, \\r0, \\r1, \\o\n    .endif\n" \
  "   .elseif (g1i & 2) == 0\n    G1BK (g1i&1)+2*(g1i>>2), 1, \\r0, \\r1, \\o\n" \
  "   .elseif (\\rd) == 1\n    G1AK (g1i>>2)^2, 1-(g1i&1), \\a0, \\a1, \\o\n   .endif\n" \
  "  .endif\n" \
  "  .set g1i, g1i+1\n .endr\n" \
  ".endm\n" \
    \
    \
  ".macro G1END h, i0, n, l, xs, r0, r1\n" \
  " .set g1i, \\i0\n .rept \\n\n" \
  "  .if (\\l) && (g1i-(\\i0) < 12)\n   .set g1x, ((\\l)>>(4*(g1i-(\\i0))))&15\n" \
  "   .if g1x < 15\n    ds_write_b128 \\xs, v[208+4*g1x:211+4*g1x] offset:(g1x+2*%[hh]*(g1x/6))*1024\n   .endif\n  .endif\n" \
  "  G1Q g1i>>3, 2*(\\h)+(((g1i>>2)^(g1i>>3))&1), g1i>>2, g1i&3, 0\n" \
  "  .if ((\\l) == 0) && (g1i < 8)\n   G1BK g1i, 2, \\r0, \\r1, 0\n  .endif\n" \
  "  .set g1i, g1i+1\n .endr\n" \
  ".endm\n"
#define G1_PURGE \
  ".purgem G1M\n .purgem G1RA\n .purgem G1RB\n .purgem G1BK\n .purgem G1AK\n .purgem G1RDB\n .purgem G1RDA\n .purgem G1Q\n .purgem G1SG\n .purgem G1END\n"
#define G1_CLOBBER \
  "v80", "v81", "v82", "v83", "v84", "v85", "v86", "v87", "v88", "v89", "v90", "v91", "v92", "v93", "v94", "v95", "v96", "v97", "v98", "v99", "v100", \
  "v101", "v102", "v103", "v104", "v105", "v106", "v107", "v108", "v109", "v110", "v111", "v112", "v113", "v114", "v115", "v116", "v117", "v118", \
  "v119", "v120", "v121", "v122", "v123", "v124", "v125", "v126", "v127", "v128", "v129", "v130", "v131", "v132", "v133", "v134", "v135", "v136", \
  "v137", "v138", "v139", "v140", "v141", "v142", "v143", "v144", "v145", "v146", "v147", "v148", "v149", "v150", "v151", "v152", "v153", "v154", \
  "v155", "v156", "v157", "v158", "v159", "v208", "v209", "v210", "v211", "v212", "v213", "v214", "v215", "v216", "v217", "v218", "v219", "v220", \
  "v221", "v222", "v223", "v232", "v233", "v234", "v235", "v236", "v237", "v238", "v239", "v240", "v241", "v242", "v243", "v244", "v245", "v246", "v247"

template <int MODE>
__device__ __forceinline__ void g1_phase(char* smem, char* ws, unsigned& P, u64& E, int w, f32x16& xq, int layer, const u16* __restrict__ x0, const u16* __restrict__ W,
                                         const u16* __restrict__ wo, const u16* __restrict__ wphi,
                                         const u16* __restrict__ wn1, const u16* __restrict__ wn2) {
  constexpr bool HH = (MODE == 1);
  constexpr int ABYTES = 128 * 128;
  constexpr int STAGE = ABYTES + 3 * 8192;
  constexpr int OFF_KS = 147456;
  constexpr int OFF_SP = 3 * STAGE;
  constexpr int OFF_RS = !HH ? OFF_SP : OFF_SP + 16384;
  constexpr int OFF_CS = OFF_RS + 1152;
  constexpr int OFF_WP = OFF_CS + 1536;
  constexpr int OFF_JK = LDS_TOTAL - 1024;
  constexpr int OFF_PK = 0;
  constexpr int OFF_VF = OFF_WP + 4096;
  constexpr int OFF_KV = 16384;
  constexpr int OFF_R2 = 135168;
  const int XS = HH ? 98304 + ((w >> 1) & 1) * 16384 + (w & 1) * 4096 : (w & 3) * 12288;
  constexpr int PSTR = 144;
  if constexpr (HH) asm volatile(".Lmk_g1b:"); else asm volatile(".Lmk_g1a:");
  IDS
  W = uni(W); wphi = uni(wphi); wn1 = uni(wn1); wn2 = uni(wn2);
  const int kg = w >> 2, wm = (w >> 1) & 1, wn = w & 1;
  const bool first = !HH && layer == 0;
  const bf16x8 ones = {0x3F80, 0x3F80, 0x3F80, 0x3F80, 0x3F80, 0x3F80, 0x3F80, 0x3F80};
  f32x4 acc[2][6];
  u32x4 pw[8];
  ST(layer * 24 + 0)
  const int R0 = wm * 64 + (HH ? kg : kg ^ 1) * 32;
  u32x16 pa;
#define pcs pa[0]
#define prs pa[1]
#define opo pa[2]
#define ogo pa[3]
#define pkw pa[5]
#define hof pa[7]
#define dm pa[8]
#define dm2 pa[9]
#define pcv pa[4]
#define pwf pa[5]
#define prv pa[6]
#define pvf pa[7]
#define ppk pa[10]
#define ppa pa[11]
#define pvr pa[12]
#define gkv pa[13]
#define gkz pa[14]
  f32x4 bias[3];
  const unsigned sb = lds_addr(smem);
  auto prep = [&](int part = 3) {
    if (part & 1) {
    pcs = sb + OFF_CS + wn * 128 + g * 16;
    prs = sb + OFF_RS + (R0 + fr) * 8;
    opo = (unsigned)(wn * 8192 + R0 * 64 + lane * 16);
    ogo = opo + WS_O + layer * (SEQ * DM * 2) + SX + (unsigned)(wn * 8192 + hp * 32768);
    opo += sb;
    pcv = sb + OFF_CS + (wn * 32 + 128 + fr) * 4;
    prv = sb + OFF_R2 + (R0 + 4 * g) * 4;
    if (layer < 2) {
      constexpr unsigned kvn = WS_KV + MODE * KVLB;
      dm = kvn + (unsigned)((rt * 16 + hp) * 16384 + (w >> 2) * 8192 + (tid & 255) * 16);
      dm2 = w >= 4 ? WS_HL + (unsigned)((1 << 20) + ((rt & 7) * 8 + (hp >> 1)) * 16384 + (rt >> 3) * 8192 + (hp & 1) * 4096 + (tid - 256) * 16)
          : tid < 16 ? kvn + (unsigned)((rt * 16 + hp) * 16384 + (tid >> 3) * 8192 + 4096 + (tid & 7) * 16)
          : tid < 18 ? WS_KF + MODE * KFLB + (unsigned)(16 * 16384 + (tid & 1) * 8192 + hp * 256 + rt * 16)
          : tid >= 64 && tid < 128 ? WS_KF + MODE * KFLB + (unsigned)(hp * 16384 + (tid & 32) * 256 + rt * 512 + (tid & 31) * 16) : 0x7fff0000u;
    }
    }
    if (!(part & 2)) {
    } else if constexpr (!HH) {
      pkw = sb + OFF_KS - 4096 + R0 * 128 + wn * 2048 + lane * 16;
      const int rr = R0 ? rt : rt - 1;
      hof = m0 + R0 == 0 ? 0x7fff0000u : WS_HL + (unsigned)(layer * 524288 + ((rr & 7) * 8 + (hp >> 1)) * 16384 + (hp & 1) * 4096 + wn * 2048 + lane * 16) + ((((rr + 1) >> 3 ^ 1) & 1) * 8192 ^ P);
      const unsigned out = ~((0x5555u << 2 << fr) >> (4 * g));
#pragma unroll
      for (int i = 0; i < 8; ++i) {
        unsigned t;
        asm("v_bfe_i32 %0, %1, %2, 1" : "=v"(t) : "v"(out), "n"((i >> 2) * 16 + (i & 3)));
        bias[i >> 2][i & 3] = __uint_as_float(t & 0xff61b1e6u);
      }
      bias[2] = m0 + R0 == 0 ? f32x4{-3.0e38f, -3.0e38f, -3.0e38f, -3.0e38f} : bias[0];
    } else {
      const int u = wm * 2 + kg;
      pwf = sb + OFF_WP + wn * 2048 + (4 * g + q4) * 64 + p4 * 8;
      pvf = sb + OFF_VF + w * 2048 + lane * 16;
      ppk = sb + OFF_PK + w * (32 * PSTR) + fr * PSTR + g * 8;
      ppa = sb + OFF_PK + wn * (32 * PSTR) + (4 * g + q4) * PSTR + u * 32 + p4 * 8;
      pvr = sb + OFF_VF + wn * 2048 + lane * 16;
      const unsigned doff = WS_KV + (layer >> 1) * KVLB + (unsigned)((rt * 16 + hp) * 16384) + (((wn ^ (rt >> 3) ^ 1) & 1) * 8192 ^ P);
      gkv = doff + u * 1024 + lane * 16;
      gkz = fr == 0 ? doff + 4096 + u * 32 + g * 8 : 0x7fff0000u;
    }
    if (part & 2) asm volatile("" : "+{v[48:63]}"(pa), "+{v[36:39]}"(bias[0]), "+{v[40:43]}"(bias[1]), "+{v[44:47]}"(bias[2]));
    else asm volatile("" : "+{v[48:63]}"(pa));
  };

  if (kg == 1) {
    const int lw = w & 3;
    const int kB = 16 * lw + (lane >> 3);
    const int fB = (kB >> 1) & 1;
    const int cB0 = ((((lane & 7) >> 1) ^ fB) << 1) | (lane & 1), cB1 = ((((lane & 7) >> 1) ^ (fB | 2)) << 1) | (lane & 1);
    const unsigned vB0 = (unsigned)((kB * 3072 + hp * 64 + cB0 * 8) * 2);
    const unsigned vB1 = (unsigned)(((kB + 8) * 3072 + hp * 64 + cB1 * 8) * 2) - 1024u;

    unsigned vW0, vW1;
    u64 ko2;
    auto run = [&](auto fst) {
      constexpr bool FIRST = decltype(fst)::value;
      if constexpr (!HH) { if constexpr (FIRST) asm volatile(".Lmk_f1:"); else asm volatile(".Lmk_f0:"); }
      constexpr int N = 10;
      const unsigned vA = FIRST ? (unsigned)(((m0 + lw * 32 + (lane >> 3)) * DM + ((lane & 7) ^ (lane >> 3)) * 8) * 2)
                                : SX + (unsigned)((lw >> 1) * 16384 + (lw & 1) * 4096 + lane * 16);
      const char* xa = FIRST ? (const char*)x0 : ws + WS_X + (layer - 1) * (SEQ * DM * 2);
      constexpr int KS = FIRST ? 128 : 32768;
      const u64 kord = FIRST ? korder0(rt) : korder(kv1(bid), hp);
      auto ktile = [&](int ks) { return knib(kord, ks); };
      auto stageA = [&](int ks, int boff) {
        const int kt = ktile(ks);
        const unsigned sA = lds_addr(smem) + boff;
        if (FIRST) glds4v(xa + kt * KS, vA, vA + 15360u, vA + 2 * 15360u, vA + 3 * 15360u, sA + lw * 4096);
        else glds4c(xa + kt * KS, vA, sA + lw * 4096);
      };
      auto stageB = [&](const u16* Wp, int ks, int boff) {
        const int kt = ktile(ks);
        const unsigned sB = lds_addr(smem) + boff + ABYTES;
        const u16* b = Wp + (size_t)kt * 64 * 3072;
        glds4v(b, vB0, vB1, vB0, vB1, sB + lw * 6144);
        glds2v(b + 2048, vB0, vB1, sB + lw * 6144 + 4096);
      };
      bf16x8 sel[3];
      unsigned cso[3], cst;
      if (FIRST) {
        stageA(1, STAGE); stageB(W, 1, STAGE);
        __builtin_amdgcn_sched_barrier(0);
        WAITVN(N);
        BAR;
        asm volatile("" ::: "memory");
        const u32x2 ev = *(const u32x2*)(smem + OFF_E);
        stageA(2, 2 * STAGE); stageB(W, 2, 2 * STAGE);
        __builtin_amdgcn_sched_barrier(0);
        const int fl = (q4 >> 1) | ((g & 1) << 1), j = 3 * wm + (fr < 3 ? fr : 0);
#pragma unroll
        for (int i = 0; i < 3; ++i) {
          const unsigned o = fr == i ? 0x3F803F80u : 0u;
          sel[i] = __builtin_bit_cast(bf16x8, u32x4{o, o, o, o});
          cso[i] = (unsigned)(2 * STAGE + ABYTES + (g >> 1) * 6144 + (g & 1) * 1024 + q4 * 128 + p4 * 8 + (((wn * 2 + ((wm + i) & 1)) ^ fl) << 5) + ((3 * wm + i) >> 1) * 2048);
          asm volatile("" : "+v"(sel[i]), "+v"(cso[i]));
        }
        cst = fr < 3 ? WS_CPA + (unsigned)(((hp * 16 + rt) * 192 + (j >> 1) * 64 + wn * 32 + (j & 1) * 16 + 4 * g) * 4) : 0x7fff0000u;
        asm volatile("" : "+v"(cst));
        E = uni2(ev[0], ev[1]);
      }
      if (!FIRST) {
        stageB(W, 2, 2 * STAGE);
        const char* xa1 = xa + ktile(1) * KS;
        const char* xa2 = xa + ktile(2) * KS;
        unsigned sA = lds_addr(smem) + STAGE + lw * 4096;
        asm volatile("" : "+s"(xa1), "+s"(xa2), "+s"(sA));
        asm volatile("" :: "v"(vA));
        if (lw == 0) {
          const u64 m = kfirst(kord);
          poll_eq(ws, ((m >> lane) & 1) ? (WS_FLAGS + (unsigned)(256 + rt * 16 + lane) * FLS) ^ P : 0u, E + (unsigned)layer, m, ~0ull);
        }
        BAR;
        glds4c(xa1, vA, sA); glds4c(xa2, vA, sA + STAGE);
        __builtin_amdgcn_sched_barrier(0);
      }
      u64 fall, fal2;
      unsigned fao, fa2, vP, vS, vC, vX, dS, dC, dR;
      auto ld_pw = [&](int i0) {
#pragma unroll
        for (int i = i0; i < i0 + 4; ++i) gld16(pw[i], vP, wn1 + i * 8 * 3072);
      };
      auto setup = [&]() {
        fao = (unsigned)((256 + rt * 16 + (lane & 15)) * FLS) ^ P;
        fa2 = (unsigned)(((FIRST ? 512 : 256) + (lane & 15) * 16 + (FIRST ? hp : (hp - ((1 << (lane >> 4)) >> 1)) & 15)) * FLS) ^ P;
        vP = (unsigned)((rt * 64 + (lane >> 3)) * 6144 + lw * 2048 + ((hp + (1 << lw)) & 15) * 128 + (lane & 7) * 16);
        vS = FIRST ? (unsigned)(WS_G0 + (m0 + lw * 32 + (lane & 31)) * 16)
                   : WS_ST + (unsigned)((layer - 1) * (SEQ * 128) + (rt & 7) * 32768 + g * 1024 + lw * 256 + fr * 16) + ((rt >> 3) * 8192 ^ P);
        vC = (FIRST ? WS_CPA : WS_CSP) + (unsigned)(hp * 12288 + g * 768 + lw * 192 + (fr < 12 ? fr : 0) * 16);
        if (!FIRST) vX = HH ? (unsigned)(hp * 4096 + lw * 1024 + lane * 16) : (unsigned)((rt * 16 + hp) * 1024 + lane * 16);
        vW0 = (unsigned)((kB * DM + hp * 64 + ((lane & 7) ^ ((lane >> 4) << 1)) * 8) * 2); vW1 = vW0 + 8 * DM * 2 - 1024u;
        dS = OFF_RS + lw * 256 + (FIRST ? (lane & 31) * 8 : fr * 16 + (g & 1) * 8); dC = OFF_CS + lw * 192 + (fr < 12 ? fr : 0) * 16 + g * 4;
        ko2 = korder(bid & 3, hp);
        dR = OFF_R2 + lw * 128 + (FIRST ? (lane & 31) * 4 : fr * 8 + (g & 1) * 4);
        asm volatile("" : "+v"(dR));
        asm volatile("" : "+v"(vS), "+v"(vX), "+v"(vW0), "+v"(vW1), "+v"(dS));
        asm volatile("" : "+s"(ko2));
        if (FIRST || HH) asm volatile("" : "+v"(vC), "+v"(dC), "+v"(fa2));
        if (FIRST) asm volatile("" : "+v"(vP)); else asm volatile("" : "+v"(fao));
      };
      if (!FIRST) { setup(); prep(); }
      bf16x8 cf[6];
      f32x4 csum = {0, 0, 0, 0};
      if (FIRST) { fall = E + 1u; vX = (unsigned)(bid * 8192 + lw * 2048 + lane * 16); asm volatile("" : "+v"(fall), "+v"(vX)); }
#pragma unroll
      for (int kt = 0; kt < 13; ++kt) {
        const int cur = (kt % 3) * STAGE;
        if (!FIRST && kt == 0) { WAITV(4); }
        else if (FIRST && kt == 4 && lw == 1) { WAITVN(N + 2); }
        else if ((!FIRST && kt == 1 && lw == 3) || ((FIRST || HH) && kt == 10 && lw == 3) || (FIRST && (kt == 2 || kt == 4 || kt == 5 || (kt == 11 && lw == 0)))) { WAITVN(N + 1); }
        else { WAITVN(N); }
        if (!FIRST && kt == 2 && lw == 3) {
          asm volatile("" : "+v"(fall));
          if (__any(fall != E + (unsigned)layer)) poll_flags(ws, P, E, 1, rt, hp, lane, layer, 0xffffull);
        }
        if (HH && kt == 11 && lw == 3) {
          asm volatile("" : "+v"(fal2));
          if (__any(fal2 != E + (unsigned)layer)) {
            _Pragma("unroll 1") for (int j = 0; j < 4; ++j) poll_flags(ws, P, E, 1, rt, (hp - ((1 << j) >> 1)) & 15, lane, layer, 0xffff00000000ull);
          }
        }
        if (FIRST && kt == 11 && lw == 3) {
          asm volatile("" : "+v"(fal2));
          if (__any(fal2 != E + 1u)) poll_flags(ws, P, E, 2, rt, hp, lane, 1u, 0xffff00000000ull);
        }
        BAR;
        if (FIRST && kt == 1) {
          asm volatile("" ::: "memory");
#pragma unroll
          for (int i = 0; i < 6; ++i) { const char* t = smem + cso[i >> 1] + (i & 1) * 12288; cf[i] = cat8(tr_read(t), tr_read(t + 512)); }
        }
        if (FIRST && kt == 2 && lw == 2 && bid < 64 && (bid & 7) == ((bid >> 3) & 3)) {
          u64 t0, t1; unsigned d;
          asm volatile("s_memtime %0\n\ts_load_dword %2, %3, 0x0\n\ts_waitcnt lgkmcnt(0)\n\ts_memtime %1\n\ts_waitcnt lgkmcnt(0)"
                       : "=&s"(t0), "=&s"(t1), "=&s"(d) : "s"(ws + WS_PL + (bid >> 5) * 8192 + (bid & 31) * 256) : "memory");
          d = (unsigned)(t1 - t0);
          const u64 v = ((E + 1u) << 16) | (d < 65535u ? d : 65535u);
          __builtin_amdgcn_raw_buffer_store_b64(__builtin_bit_cast(u32x2, v), rsW, lane == 0 ? WS_PR + (unsigned)(bid & 56) : 0x7fff0000u, 0, 16);
        }
        if (FIRST && kt == 12 && lw == 2) asm volatile("global_load_dwordx2 %0, %1, %2 sc1" : "=v"(fal2) : "v"((lane & 7) * 8), "s"(ws + WS_PR) : "memory");
        if (FIRST && kt >= 2 && kt < 6 && lw == kt - 2) setup();
        if (FIRST && kt >= 6 && kt < 10 && (lw >> 1) == ((kt - 6) >> 1)) prep(1 + (kt & 1));
        stageA(kt + 3, cur);
        if (FIRST && kt == 1) {
          asm volatile("" : "+v"(sel[0]));
#pragma unroll
          for (int i = 0; i < 6; ++i) csum = MFMA(cf[i], sel[i >> 1], csum);
          asm volatile("" : "+v"(csum));
        }
        stageB(W, kt + 3, cur);
        if (FIRST) {
          if (kt == 1) __builtin_amdgcn_raw_buffer_store_b128(__builtin_bit_cast(u32x4, csum), rsW, cst, 0, 16);
          if (kt == 3 && tid == 320) __builtin_amdgcn_raw_buffer_store_b64(__builtin_bit_cast(u32x2, fall), rsW, (WS_FLAGS + (unsigned)(512 + rt * 16 + hp) * FLS) ^ P, 0, 16);
          if (kt == 3 || kt == 4) glds16s(wo + (kt - 3) * 512, vX, lds_addr(smem) + OFF_JK);
        }
        if constexpr (!HH) if (FIRST && kt == 10 && lw == 0) {
          CODE_TOUCH(1, ".Lmk_ep0", ".Lmk_end")
          asm volatile("s_mov_b32 m0, %2\n\ts_nop 0\n\tglobal_load_lds_dword %0, %1" :: "v"(v), "s"(pc), "s"(lds_addr(smem) + OFF_JK) : "memory", "m0");
        }
        if (!FIRST && kt == 0 && lw == 3) asm volatile("global_load_dwordx2 %0, %1, %2 sc1" : "=v"(fall) : "v"(fao), "s"(ws + WS_FLAGS) : "memory");
        if ((FIRST || HH) && kt == 9 && lw == 3) asm volatile("global_load_dwordx2 %0, %1, %2 sc1" : "=v"(fal2) : "v"(fa2), "s"(ws + WS_FLAGS) : "memory");
      }
      if (FIRST && lw < 3) { ld_pw(0); WAITVN(N + 4); } else { WAITVN(N); }
      BAR;
      u32x4 sv[4], cv[4];
      auto ld_c = [&](u32x4& d, unsigned v, int so) { asm volatile("buffer_load_dwordx4 %0, %1, %2, %3 offen sc1" : "=v"(d) : "v"(v), "s"(rsW), "s"(so) : "memory"); };
      auto wo_tile = [&](int t) { glds2v(wo + (size_t)knib(ko2, t) * 64 * DM, vW0, vW1, lds_addr(smem) + OFF_WO + t * 8192 + lw * 2048); };
#pragma unroll
      for (int i = 0; i < (FIRST ? 1 : 4); ++i) ld_c(sv[i], vS, (i & 1) * 4096 + (i >> 1) * 16384);
      if (FIRST || HH) {
#pragma unroll
        for (int i = 0; i < 4; ++i) ld_c(cv[i], vC, i * 3072);
      }
      if (HH) glds16s(wphi, vX, lds_addr(smem) + OFF_WP + lw * 1024);
      else if (!FIRST) glds16c(ws + WS_CSA, vX, lds_addr(smem) + OFF_CS);
      wo_tile(0); wo_tile(1);
      if (HH || (FIRST && lw < 3)) { WAITV(13); } else { WAITV(9); }
      BAR;
      wo_tile(2); wo_tile(3); wo_tile(4); wo_tile(5);
      if (FIRST) {
        glds16s(wn2, vX, lds_addr(smem) + OFF_JK); glds16s(wn2 + 512, vX, lds_addr(smem) + OFF_JK);
        WAITV(14);
        if (lw < 3) ld_pw(4);
      } else {
        WAITV(12);
      }
      auto fin = [&]() {
        if (FIRST) {
          const unsigned tag = (unsigned)E + 1u;
          asm volatile("" : "+v"(sv[0]));
          _Pragma("unroll 1") for (unsigned spins = 0; spins < (1u << 18); ++spins) {
            if (__all(sv[0][1] == tag && sv[0][3] == tag)) break;
            __builtin_amdgcn_s_sleep(1);
            asm volatile("buffer_load_dwordx4 %0, %1, %2, 0 offen sc1\n\ts_waitcnt vmcnt(0)" : "=v"(sv[0]) : "v"(vS), "s"(rsW) : "memory");
          }
          *(u32x2*)(smem + dS) = u32x2{sv[0][0], sv[0][2]};
          { unsigned* p = (unsigned*)(smem + dR); p[0] = sv[0][0]; p[128] = sv[0][2]; }
        } else {
          asm volatile("" : "+v"(sv[0]), "+v"(sv[1]), "+v"(sv[2]), "+v"(sv[3]));
          const f32x4 t = (__builtin_bit_cast(f32x4, sv[0]) + __builtin_bit_cast(f32x4, sv[1])) + (__builtin_bit_cast(f32x4, sv[2]) + __builtin_bit_cast(f32x4, sv[3]));
          const unsigned u = xs4(t[0], t[2], t[1], t[3]);
          auto h = __builtin_amdgcn_permlane32_swap(u, u, false, false);
          const float mean = __uint_as_float(h[0]) * (1.0f / 1024.0f);
          const float var = fmaxf(__uint_as_float(h[1]) * (1.0f / 1024.0f) - mean * mean, 0.f);
          const float rs = __builtin_amdgcn_rsqf(var + 1e-5f);
          *(float2*)(smem + dS) = make_float2(-mean, rs);
          { float* p = (float*)(smem + dR); p[0] = -mean; p[128] = rs; }
        }
      };
      auto cfin = [&]() {
        if (FIRST || HH) {
          asm volatile("" : "+v"(cv[0]), "+v"(cv[1]), "+v"(cv[2]), "+v"(cv[3]));
          const f32x4 t = (__builtin_bit_cast(f32x4, cv[0]) + __builtin_bit_cast(f32x4, cv[1])) + (__builtin_bit_cast(f32x4, cv[2]) + __builtin_bit_cast(f32x4, cv[3]));
          *(unsigned*)(smem + dC) = xs4(t[0], t[1], t[2], t[3]);
        }
      };
      cfin();
      if (FIRST && lw == 2) {
        asm volatile("" : "+v"(fal2));
        _Pragma("unroll 1") for (unsigned spins = 0; spins < (1u << 18); ++spins) {
          if (__all(((fal2 ^ ((E + 1u) << 16)) >> 16) == 0)) break;
          __builtin_amdgcn_s_sleep(1);
          asm volatile("global_load_dwordx2 %0, %1, %2 sc1\n\ts_waitcnt vmcnt(0)" : "=v"(fal2) : "v"((lane & 7) * 8), "s"(ws + WS_PR) : "memory");
        }
        unsigned d = (unsigned)fal2 & 0xffffu;
        d = min(d, (unsigned)__builtin_amdgcn_update_dpp(0u, d, 0xb1, 0xf, 0xf, false));
        d = min(d, (unsigned)__builtin_amdgcn_update_dpp(0u, d, 0x4e, 0xf, 0xf, false));
        *(unsigned*)(smem + OFF_E + 256) = __builtin_amdgcn_readlane(d, 4) < __builtin_amdgcn_readlane(d, 0) ? 8192u : 0u;
      }
      if (!HH) BAR;
      fin();
      WAITL0;
    };
    typedef std::integral_constant<bool, true> T_;
    typedef std::integral_constant<bool, false> F_;
    __builtin_amdgcn_s_setprio(3);
    if (first) run(T_{}); else run(F_{});
    __builtin_amdgcn_s_setprio(0);
    BAR;
    if constexpr (!HH) asm volatile(".Lmk_ep0:");
    {
      const char* xr = smem + XS + lane * 16;
#pragma unroll
      for (int i = 0; i < 2; ++i)
#pragma unroll
        for (int j = HH ? 2 : 0; j < (HH ? 4 : 6); ++j) acc[i][j] = *(const f32x4*)(xr + (i * (HH ? 8 : 6) + j) * 1024);
      if (HH) asm volatile("" : "=v"(acc[0][0]), "=v"(acc[0][1]), "=v"(acc[1][0]), "=v"(acc[1][1]));
    }
    asm volatile("" : "={v[64:79]}"(xq));
  } else {
    prep();
    const int fl = (q4 >> 1) | ((g & 1) << 1);
    const unsigned bo = lds_addr(smem) + ABYTES + (g >> 1) * 6144 + (g & 1) * 1024 + q4 * 128 + p4 * 8;
    const unsigned bo0 = bo + (((wn * 2 + 0) ^ fl) << 5), bo1 = bo + (((wn * 2 + 1) ^ fl) << 5);
    const unsigned ar = lds_addr(smem) + (wm * 64 + fr) * 128;
    const unsigned aK0 = ar + ((g ^ (fr & 7)) << 4), aK1 = ar + (((4 + g) ^ (fr & 7)) << 4);
    if (first) {
      const int row = m0 + hp * 8 + w * 2 + (lane >> 5);
      static_assert(HH || (OFF_SP == 122880 && OFF_KS + 12288 <= OFF_JK), "addresses used at the top of the kernel");
      asm volatile("" :: "v"(aK0), "v"(aK1), "v"(bo0), "v"(bo1));
      unsigned go = (lane & 31) == 0 ? (unsigned)(WS_G0 + row * 16) : 0x7fff0000u;
      unsigned ho = WS_HL + (unsigned)(((rt & 7) * 8 + (hp >> 1)) * 16384 + (hp & 1) * 4096 + tid * 16) + ((((rt + 1) >> 3 ^ 1) & 1) * 8192 ^ P);
      asm volatile("" : "+v"(go), "+v"(ho));
      WAITV(0);
      u32x4 xs[4];
      const u32x2 ev = *(const u32x2*)(smem + OFF_E);
#pragma unroll
      for (int i = 0; i < 4; ++i) xs[i] = *(const u32x4*)(smem + OFF_SP + w * 4096 + (lane >> 5) * 2048 + (lane & 31) * 16 + i * 512);
      typedef __attribute__((ext_vector_type(2))) __bf16 bf2;
      const bf2 one2 = __builtin_bit_cast(bf2, 0x3F803F80u);
      float sa[2] = {0.f, 0.f}, sq[2] = {0.f, 0.f};
#pragma unroll
      for (int i = 0; i < 16; ++i) {
        const unsigned u = xs[i >> 2][i & 3];
        const bf2 t = __builtin_bit_cast(bf2, u);
        sa[i & 1] = __builtin_amdgcn_fdot2_f32_bf16(t, one2, sa[i & 1], false);
        sq[i & 1] = __builtin_amdgcn_fdot2_f32_bf16(t, t, sq[i & 1], false);
      }
      float sm = r16_sum(sa[0] + sa[1]), m2 = r16_sum(sq[0] + sq[1]);
      {
        auto r = __builtin_amdgcn_permlane16_swap(__float_as_uint(sm), __float_as_uint(sm), false, false);
        auto q = __builtin_amdgcn_permlane16_swap(__float_as_uint(m2), __float_as_uint(m2), false, false);
        sm = __uint_as_float(r[0]) + __uint_as_float(r[1]); m2 = __uint_as_float(q[0]) + __uint_as_float(q[1]);
      }
      const float mean = sm * (1.0f / 1024.0f);
      m2 = fmaxf(m2 - sm * mean, 0.f);
      const unsigned tag = ev[0] + 1u;
      u32x4 gw = {__float_as_uint(-mean), tag, __float_as_uint(__builtin_amdgcn_rsqf(m2 * (1.0f / 1024.0f) + 1e-5f)), tag};
      __builtin_amdgcn_raw_buffer_store_b128(gw, rsW, go, 0, 16);
      __builtin_amdgcn_raw_buffer_store_b128(empty4(), rsW, ho, 0, 16);
      if (bid < 64 && tid == 0) __builtin_amdgcn_raw_buffer_store_b32(0u, rsW, WS_PL + (unsigned)((bid >> 5) * 8192 + (bid & 31) * 256), 0, 16);
      E = uni2(ev[0], ev[1]);
    }
    f32x32 ac0;
    f32x16 ac1;
    f32x8 av[2];
    asm volatile("" :: "v"(aK0), "v"(aK1), "v"(bo0), "v"(bo1));
    if (first) BAR;
    else publish(rsW, tid, 256 + rt * 16 + hp, E + (unsigned)layer, P);
#define G1_OUT
#define G1_T(a)
#define G1_TEND
#define G1_TCL
    asm volatile(G1_MACROS
        ".p2align 3\n"
        "G1RDB 0, %[b0], %[b1], 0\n G1RDA %[a0], %[a1], 0\n G1RDB 1, %[b0], %[b1], 0\n"
        "ds_read_b128 v[64:67], %[xa]\n ds_read_b128 v[68:71], %[xa] offset:2048\n"
        "ds_read_b128 v[72:75], %[xa] offset:4096\n ds_read_b128 v[76:79], %[xa] offset:6144\n"
        "s_cmp_eq_u32 %[lay], 0\n s_waitcnt lgkmcnt(12)\n"
        "G1SG 0, 1, 1, %[a0], %[a1], %[b0], %[b1], 0\n"
        "s_mov_b32 vcc_lo, 16\n"
        "G1SG 1, 1, 1, %[a0], %[a1], %[b0], %[b1], 40960\n"
        "G1SG 2, 1, 2, %[a0], %[a1], %[b0], %[b1], 40960\n"
        "s_cbranch_scc1 .Lg1a%=\n"
        "s_barrier\n"
        G1_T(88)
        "s_barrier\n"
        G1_T(92)
        "G1RDA %[a0], %[a1], 40960\n"
        "s_waitcnt lgkmcnt(4)\n"
        "s_branch .Lg1b%=\n"
        ".Lg1a%=:\n"
        G1_T(88)
        "s_barrier\n"
        G1_T(92)
        "G1RDB 0, %[b0], %[b1], 40960\n G1RDA %[a0], %[a1], 40960\n G1RDB 1, %[b0], %[b1], 40960\n"
        ".p2align 3\n s_nop 0\n"
        ".Lg1b%=:\n"
        "s_waitcnt lgkmcnt(8)\n"
        "G1SG 0, 0, 1, %[a0], %[a1], %[b0], %[b1], 40960\n"
        "s_barrier\n"
        "G1SG 1, 0, 1, %[a2], %[a3], %[b2], %[b3], 0\n"
        "G1SG 2, 0, 1, %[a2], %[a3], %[b2], %[b3], 0\n"
        "s_lshr_b32 vcc_lo, vcc_lo, 1\n s_waitcnt vmcnt(0) lgkmcnt(8)\n"
        "G1SG 0, 0, 1, %[a2], %[a3], %[b2], %[b3], 0\n"
        "s_barrier\n"
        "G1SG 1, 0, 1, %[a0], %[a1], %[b0], %[b1], 0\n"
        "G1SG 2, 0, 1, %[a0], %[a1], %[b0], %[b1], 0\n"
        "s_cbranch_scc0 .Lg1c%=\n s_waitcnt lgkmcnt(8)\n"
        "G1SG 0, 0, 1, %[a0], %[a1], %[b0], %[b1], 0\n"
        "s_barrier\n"
        "G1SG 1, 0, 1, %[a0], %[a1], %[b0], %[b1], 40960\n"
        "G1SG 2, 0, 1, %[a0], %[a1], %[b0], %[b1], 40960\n"
        "s_branch .Lg1b%=\n"
        ".Lg1c%=:\n"
        "s_waitcnt lgkmcnt(8)\n"
        "G1END 1, 0, 8, 0, %[xs], %[b0], %[b1]\n"
        "s_waitcnt lgkmcnt(8)\n s_nop 0\n"
        "G1END 1, 8, 8, 0, %[xs], %[b0], %[b1]\n"
        "s_waitcnt lgkmcnt(0)\n s_nop 0\n"
        "G1END 1, 16, 8, 0, %[xs], %[b0], %[b1]\n"
        ".if %[hh] == 0\n s_nop 0\n s_barrier\n .endif\n"
        G1_T(96)
        ".if %[hh]\n G1END 0, 0, 20, 0xffff76103298, %[xs], %[b0], %[b1]\n .else\n G1END 0, 0, 18, 0xba5498327610, %[xs], %[b0], %[b1]\n .endif\n"
        "s_waitcnt lgkmcnt(0)\n s_barrier\n"
        ".if %[hh]\n G1END 0, 20, 24-20, 0xffffffffffff, %[xs], %[b0], %[b1]\n .else\n G1END 0, 18, 24-18, 0xffffffffffff, %[xs], %[b0], %[b1]\n .endif\n"
        "s_nop 7\n"
        G1_TEND G1_PURGE
        : "=&{v[160:191]}"(ac0), "=&{v[192:207]}"(ac1), "=&{v[64:79]}"(xq), "=&{v[224:231]}"(av[0]), "=&{v[248:255]}"(av[1]),
          "+{v[48:63]}"(pa), "+{v[36:39]}"(bias[0]), "+{v[40:43]}"(bias[1]), "+{v[44:47]}"(bias[2]) G1_OUT
        : [a0] "v"(aK0), [a1] "v"(aK1), [a2] "v"(aK0 + 2 * STAGE), [a3] "v"(aK1 + 2 * STAGE), [b0] "v"(bo0), [b1] "v"(bo1),
          [b2] "v"(bo0 + 2 * STAGE), [b3] "v"(bo1 + 2 * STAGE), [xs] "v"(lds_addr(smem) + XS + lane * 16), [xa] "v"(wn ? aK1 : aK0),
          [lay] "s"(layer), [hh] "n"(MODE)
        : "memory", "scc", "vcc", G1_TCL G1_CLOBBER);
    acc[0][0] = quad<0>(ac0); acc[0][1] = quad<1>(ac0); acc[0][2] = quad<2>(ac0); acc[0][3] = quad<3>(ac0); acc[0][4] = quad<4>(ac0); acc[0][5] = quad<5>(ac0);
    acc[1][0] = quad<6>(ac0); acc[1][1] = quad<7>(ac0); acc[1][2] = quad<0>(ac1); acc[1][3] = quad<1>(ac1); acc[1][4] = quad<2>(ac1); acc[1][5] = quad<3>(ac1);
    if constexpr (HH) {
      const float cs[2] = {lds<float>(pcv), lds<float>(pcv + 64)};
      f32x4 rm[4], rr[4];
#pragma unroll
      for (int rb = 0; rb < 4; ++rb) { rm[rb] = lds<f32x4>(prv + rb * 64); rr[rb] = lds<f32x4>(prv + 512 + rb * 64); }
#pragma unroll
      for (int h = 0; h < 2; ++h) {
        f32x2 f[8];
#pragma unroll
        for (int i = 0; i < 8; ++i) {
          const int c = i >> 2, rb = (i >> 1) & 1, e = (i & 1) * 2;
          const f32x4 a = h == 0 ? acc[rb][4 + c] : c ? quad<1>(av[rb]) : quad<0>(av[rb]);
          f[i] = __builtin_elementwise_fma(f32x2{rm[2 * h + rb][e], rm[2 * h + rb][e + 1]}, f32x2{cs[c], cs[c]}, f32x2{a[e], a[e + 1]});
        }
        asm volatile("" : "+v"(f[0]), "+v"(f[1]), "+v"(f[2]), "+v"(f[3]), "+v"(f[4]), "+v"(f[5]), "+v"(f[6]), "+v"(f[7]));
#pragma unroll
        for (int i = 0; i < 8; ++i) f[i] *= f32x2{rr[2 * h + ((i >> 1) & 1)][(i & 1) * 2], rr[2 * h + ((i >> 1) & 1)][(i & 1) * 2 + 1]};
        asm volatile("" : "+v"(f[0]), "+v"(f[1]), "+v"(f[2]), "+v"(f[3]), "+v"(f[4]), "+v"(f[5]), "+v"(f[6]), "+v"(f[7]));
#pragma unroll
        for (int c = 0; c < 2; ++c) lds<u32x4>(pvf + h * 8192 + c * 1024) = u32x4{pk2(f[4 * c][0], f[4 * c][1]), pk2(f[4 * c + 1][0], f[4 * c + 1][1]), pk2(f[4 * c + 2][0], f[4 * c + 2][1]), pk2(f[4 * c + 3][0], f[4 * c + 3][1])};
      }
    }
  }
  ST(layer * 24 + 4)
  {
  const int lane = lane_id(), tid = w * 64 + lane, fr = lane & 15, g = lane >> 4;
  (void)fr; (void)g;
  auto o_put = [&](int rb, const f32x4& a, const f32x4& b) {
    const u32x4 v = {pk2(a[0], a[1]), pk2(a[2], a[3]), pk2(b[0], b[1]), pk2(b[2], b[3])};
    __builtin_amdgcn_raw_buffer_store_b128(v, rsW, ogo + rb * 1024, 0, 16);
    return v;
  };
  auto marks = [&]() {
    const u32x4 pz = empty4();
    __builtin_amdgcn_raw_buffer_store_b128(pz, rsW, dm, 0, 16);
    if (w < 2 || (!HH && w >= 4)) __builtin_amdgcn_raw_buffer_store_b128(pz, rsW, dm2, 0, 16);
  };
  if (first) { P = __builtin_amdgcn_readfirstlane(*(const unsigned*)(smem + OFF_E + 256)); ogo ^= P; }
  f32x4 csq[2], csk[2], rm[2], rr[2];
  float csv[2];
  f32x2 st2[2] = {lds<f32x2>(prs), lds<f32x2>(prs + 128)};
#pragma unroll
  for (int c = 0; c < 2; ++c) {
    csk[c] = lds<f32x4>(pcs + 256 + c * 64);
    if (!HH) { csv[c] = lds<float>(pcv + c * 64); rm[1 - c] = lds<f32x4>(prv + (1 - c) * 64); rr[1 - c] = lds<f32x4>(prv + 512 + (1 - c) * 64); }
  }
#pragma unroll
  for (int c = 0; c < 2; ++c) csq[c] = lds<f32x4>(pcs + c * 64);
  if constexpr (!HH) {
    auto fold = [&](const f32x4& a, const f32x4& cs, const f32x2& st, bool scale) {
      f32x2 lo = __builtin_elementwise_fma(f32x2{cs[0], cs[1]}, f32x2{st[0], st[0]}, f32x2{a[0], a[1]});
      f32x2 hi = __builtin_elementwise_fma(f32x2{cs[2], cs[3]}, f32x2{st[0], st[0]}, f32x2{a[2], a[3]});
      if (scale) { lo *= f32x2{st[1], st[1]}; hi *= f32x2{st[1], st[1]}; }
      return u32x2{pk2(lo[0], lo[1]), pk2(hi[0], hi[1])};
    };
    auto cat2 = [](u32x2 a, u32x2 b) { return __builtin_bit_cast(bf16x8, u32x4{a[0], a[1], b[0], b[1]}); };
    auto foldv = [&](int rb, int c) {
      const f32x4& a = acc[rb][4 + c];
      const f32x2 lo = __builtin_elementwise_fma(f32x2{rm[rb][0], rm[rb][1]}, f32x2{csv[c], csv[c]}, f32x2{a[0], a[1]}) * f32x2{rr[rb][0], rr[rb][1]};
      const f32x2 hi = __builtin_elementwise_fma(f32x2{rm[rb][2], rm[rb][3]}, f32x2{csv[c], csv[c]}, f32x2{a[2], a[3]}) * f32x2{rr[rb][2], rr[rb][3]};
      return u32x2{pk2(lo[0], lo[1]), pk2(hi[0], hi[1])};
    };
    bf16x8 qf[2], kf[2];
    u32x2 vt[3][2];
#pragma unroll
    for (int rb = 1; rb >= 0; --rb) {
      kf[rb] = cat2(fold(acc[rb][2], csk[0], st2[rb], true), fold(acc[rb][3], csk[1], st2[rb], true));
      const u32x2 va0 = vt[rb + 1][0] = foldv(rb, 0), va1 = vt[rb + 1][1] = foldv(rb, 1);
      if (rb == 1) {
        if (R0 == 96) {
          u32x4 hk = __builtin_bit_cast(u32x4, kf[1]), hv = {va0[0], va0[1], va1[0], va1[1]};
#pragma unroll
          for (int e = 0; e < 4; ++e) { hk[e] = hk[e] < EMPTY - 1u ? hk[e] : EMPTY - 1u; hv[e] = hv[e] < EMPTY - 1u ? hv[e] : EMPTY - 1u; }
          __builtin_amdgcn_raw_buffer_store_b128(hk, rsW, hof, 0, 16);
          __builtin_amdgcn_raw_buffer_store_b128(hv, rsW, hof + 1024, 0, 16);
        } else {
          lds<bf16x8>(pkw + 4096) = kf[1];
          lds<u32x2>(pkw + 5120) = va0; lds<u32x2>(pkw + 5128) = va1;
        }
        if (first && R0 != 0) marks();
        asm volatile("" : "+v"(st2[0]), "+v"(st2[1]), "+v"(csv[0]), "+v"(csv[1]));
      }
    }
#pragma unroll
    for (int rb = 0; rb < 2; ++rb) qf[rb] = cat2(fold(acc[rb][0], csq[0], st2[rb], false), fold(acc[rb][1], csq[1], st2[rb], false));
    f32x4 sc[2][2];
    sc[1][0] = MFMA(kf[0], qf[1], bias[0]);
    sc[1][1] = MFMA(kf[1], qf[1], bias[1]);
    sc[0][1] = MFMA(kf[0], qf[0], bias[1]);
    constexpr float SCL = 0.17677669529663687f * 1.4426950408889634f;
    auto attn = [&](int rb) {
      const f32x4 z4 = {0, 0, 0, 0};
      const float scl = st2[rb][1] * SCL;
      asm volatile("s_nop 7" : "+v"(sc[rb][0]), "+v"(sc[rb][1]));
      unsigned m = __float_as_uint(vmax3f_n(vmax3f(sc[rb][0][0], sc[rb][0][1], sc[rb][0][2]), vmax3f(sc[rb][0][3], sc[rb][1][0], sc[rb][1][1]), vmaxf(sc[rb][1][2], sc[rb][1][3])));
      auto r = __builtin_amdgcn_permlane16_swap(m, m, false, false);
      m = __float_as_uint(vmaxf_n(__uint_as_float(r[0]), __uint_as_float(r[1])));
      auto r2 = __builtin_amdgcn_permlane32_swap(m, m, false, false);
      const float mxs = -vmaxf(__uint_as_float(r2[0]), __uint_as_float(r2[1])) * scl;
      u32x4 pv;
#pragma unroll
      for (int i = 0; i < 4; ++i) {
        const f32x2 t = __builtin_elementwise_fma(f32x2{sc[rb][i >> 1][(i & 1) * 2], sc[rb][i >> 1][(i & 1) * 2 + 1]}, f32x2{scl, scl}, f32x2{mxs, mxs});
        pv[i] = pk2(__builtin_amdgcn_exp2f(t[0]), __builtin_amdgcn_exp2f(t[1]));
      }
      const bf16x8 pf = __builtin_bit_cast(bf16x8, pv);
      const f32x4 den = MFMA(ones, pf, z4);
      const f32x4 o0 = MFMA(cat2(vt[rb][0], vt[rb + 1][0]), pf, z4), o1 = MFMA(cat2(vt[rb][1], vt[rb + 1][1]), pf, z4);
      const float inv = __builtin_amdgcn_rcpf(den[0]);
      return o_put(rb, o0 * inv, o1 * inv);
    };
    u32x4 hk, hv;
    auto ld_h = [&]() {
      asm volatile("buffer_load_dwordx4 %0, %2, %3, 0 offen sc1\n\tbuffer_load_dwordx4 %1, %2, %3, 0 offen offset:1024 sc1" : "=&v"(hk), "=&v"(hv) : "v"(hof), "s"(rsW) : "memory");
    };
    asm volatile("" : "+v"(sc[0][1]));
    const u32x4 ov = attn(1);
    if (R0 == 0) {
      ld_h();
      WAITL0; BAR;
      _Pragma("unroll 1") for (unsigned spins = 0; spins < (1u << 18); ++spins) {
        ARRIVED("+v"(hk), "+v"(hv));
        unsigned mxw = 0;
#pragma unroll
        for (int e = 0; e < 4; ++e) { mxw = hk[e] > mxw ? hk[e] : mxw; mxw = hv[e] > mxw ? hv[e] : mxw; }
        if (__all(mxw != EMPTY)) break;
        ld_h();
      }
      ARRIVED("+v"(hk), "+v"(hv));
      if (first) marks();
    } else {
      if (first && tid >= 128 && tid < 176) __builtin_amdgcn_raw_buffer_store_b128(*(const u32x4*)(smem + OFF_CS + (tid - 128) * 16), rsW, WS_CSA + (unsigned)((rt * 16 + hp) * 1024 + (tid - 128) * 16), 0, 16);
      WAITL0; BAR;
      asm volatile("" ::: "memory");
      hk = lds<u32x4>(pkw); hv = lds<u32x4>(pkw + 1024);
    }
    ST(layer * 24 + 5)
    asm volatile("" ::: "memory");
    vt[0][0] = u32x2{hv[0], hv[1]}; vt[0][1] = u32x2{hv[2], hv[3]};
    sc[0][0] = MFMA(__builtin_bit_cast(bf16x8, hk), qf[0], bias[2]);
    lds<u32x4>(opo + 1024) = ov;
    lds<u32x4>(opo) = attn(0);
  } else {
    bf16x8 wf[2];
#pragma unroll
    for (int fb = 0; fb < 2; ++fb) wf[fb] = cat8(tr_read(pwf + fb * 32), tr_read(pwf + fb * 32 + 16 * 64));
    __builtin_amdgcn_sched_barrier(0);
    if (layer == 1) marks();
    const f32x4 z4 = {0, 0, 0, 0};
    constexpr float L2E = 1.4426950408889634f;
    const unsigned kvo = WS_KV + (layer >> 1) * KVLB, kfo = WS_KF + (layer >> 1) * KFLB + hp * 16384, kzo = WS_KF + (layer >> 1) * KFLB + 16 * 16384 + hp * 256;
    bf16x8 qp[2][2];
    auto phi2 = [&](auto qktag) {
      constexpr int QK = decltype(qktag)::value & 1;
      constexpr int RB0 = (decltype(qktag)::value >> 1) & 1, RB1 = (decltype(qktag)::value & 4) ? RB0 + 1 : 2;
      float pv[2][8];
      f32x4 pm[2][2];
      f32x2 f[2][4];
#pragma unroll
      for (int rb = RB0; rb < RB1; ++rb) {
        asm volatile("" : "+v"(st2[rb]));
        const f32x2 nm = {st2[rb][0], st2[rb][0]};
#pragma unroll
        for (int i = 0; i < 4; ++i) {
          const f32x4& a = acc[rb][2 * QK + (i >> 1)];
          const f32x4& cs = QK ? csk[i >> 1] : csq[i >> 1];
          f[rb][i] = __builtin_elementwise_fma(f32x2{cs[(i & 1) * 2], cs[(i & 1) * 2 + 1]}, nm, f32x2{a[(i & 1) * 2], a[(i & 1) * 2 + 1]});
        }
      }
#pragma unroll
      for (int rb = RB0; rb < RB1; ++rb) asm volatile("" : "+v"(f[rb][0]), "+v"(f[rb][1]), "+v"(f[rb][2]), "+v"(f[rb][3]));
#pragma unroll
      for (int rb = RB0; rb < RB1; ++rb) {
        const bf16x8 xf = __builtin_bit_cast(bf16x8, u32x4{pk2(f[rb][0][0], f[rb][0][1]), pk2(f[rb][1][0], f[rb][1][1]), pk2(f[rb][2][0], f[rb][2][1]), pk2(f[rb][3][0], f[rb][3][1])});
        pm[rb][0] = MFMA(wf[0], xf, z4); pm[rb][1] = MFMA(wf[1], xf, z4);
      }
      if (QK) asm volatile("s_nop 7" : "+v"(pm[0][0]), "+v"(pm[0][1]), "+v"(pm[1][0]), "+v"(pm[1][1]));
      else asm volatile("s_nop 7" : "+v"(pm[RB0][0]), "+v"(pm[RB0][1]));
#pragma unroll
      for (int rb = RB0; rb < RB1; ++rb)
#pragma unroll
        for (int i = 0; i < 4; ++i) { pv[rb][i] = pm[rb][0][i]; pv[rb][4 + i] = pm[rb][1][i]; }
      float mx[2], mn[2];
#pragma unroll
      for (int u = RB0; u < RB1; ++u) max8pn(pm[u][0], pm[u][1], mx[u], mn[u]);
      if (RB1 - RB0 == 2) xr4<true>(mx[0], mn[0], mx[1], mn[1]); else xr2<true>(mx[RB0], mn[RB0]);
      float ep[2][8], en[2][8], sp[2], sn[2];
#pragma unroll
      for (int u = RB0; u < RB1; ++u) {
        const float c = st2[u][1] * L2E, mxs = -mx[u] * c, mns = -mn[u] * c;
#pragma unroll
        for (int i = 0; i < 4; ++i) {
          const f32x2 p2 = {pv[u][2 * i], pv[u][2 * i + 1]};
          const f32x2 tp = __builtin_elementwise_fma(p2, f32x2{c, c}, f32x2{mxs, mxs});
          const f32x2 tn = __builtin_elementwise_fma(-p2, f32x2{c, c}, f32x2{mns, mns});
          ep[u][2 * i] = __builtin_amdgcn_exp2f(tp[0]); ep[u][2 * i + 1] = __builtin_amdgcn_exp2f(tp[1]);
          en[u][2 * i] = __builtin_amdgcn_exp2f(tn[0]); en[u][2 * i + 1] = __builtin_amdgcn_exp2f(tn[1]);
        }
        const f32x2 a2 = (f32x2{ep[u][0], ep[u][1]} + f32x2{ep[u][2], ep[u][3]}) + (f32x2{ep[u][4], ep[u][5]} + f32x2{ep[u][6], ep[u][7]});
        const f32x2 b2 = (f32x2{en[u][0], en[u][1]} + f32x2{en[u][2], en[u][3]}) + (f32x2{en[u][4], en[u][5]} + f32x2{en[u][6], en[u][7]});
        sp[u] = a2[0] + a2[1]; sn[u] = b2[0] + b2[1];
      }
      if (RB1 - RB0 == 2) xr4<false>(sp[0], sn[0], sp[1], sn[1]); else xr2<false>(sp[RB0], sn[RB0]);
#pragma unroll
      for (int rb = RB0; rb < RB1; ++rb) {
        float ip = __builtin_amdgcn_rcpf(sp[rb]), in = __builtin_amdgcn_rcpf(sn[rb]);
        u32x2 o0, o1, o2, o3;
        o0[0] = pk2(ep[rb][0] * ip, ep[rb][1] * ip); o0[1] = pk2(ep[rb][2] * ip, ep[rb][3] * ip);
        o1[0] = pk2(ep[rb][4] * ip, ep[rb][5] * ip); o1[1] = pk2(ep[rb][6] * ip, ep[rb][7] * ip);
        o2[0] = pk2(en[rb][0] * in, en[rb][1] * in); o2[1] = pk2(en[rb][2] * in, en[rb][3] * in);
        o3[0] = pk2(en[rb][4] * in, en[rb][5] * in); o3[1] = pk2(en[rb][6] * in, en[rb][7] * in);
        if (QK == 0) {
          qp[rb][0] = __builtin_bit_cast(bf16x8, (u32x4){o0[0], o0[1], o1[0], o1[1]});
          qp[rb][1] = __builtin_bit_cast(bf16x8, (u32x4){o2[0], o2[1], o3[0], o3[1]});
          asm volatile("" : "+v"(qp[rb][0]), "+v"(qp[rb][1]));
        } else {
          const unsigned dst = ppk + rb * 16 * PSTR;
          lds<u32x2>(dst) = o0; lds<u32x2>(dst + 32) = o1; lds<u32x2>(dst + 64) = o2; lds<u32x2>(dst + 96) = o3;
        }
      }
    };
    phi2(std::integral_constant<int, 1>{});
    bf16x8 one8 = ones;
    asm volatile("" : "+v"(one8));
    WAITL0; BAR;
    ST(layer * 24 + 5)
    {
      bf16x8 af[4], v0[4], v1[4];
      asm volatile("" ::: "memory");
#pragma unroll
      for (int sw = 0; sw < 4; ++sw) {
        af[sw] = cat8(tr_read(ppa + sw * (64 * PSTR)), tr_read(ppa + sw * (64 * PSTR) + 16 * PSTR));
        v0[sw] = lds<bf16x8>(pvr + sw * 4096);
        v1[sw] = lds<bf16x8>(pvr + sw * 4096 + 1024);
      }
      if (kg) {
#pragma unroll
        for (int i = 0; i < 4; ++i) acc[i >> 1][i & 1] = lds<f32x4>(sb + XS + lane * 16 + ((i >> 1) * 8 + (i & 1)) * 1024);
      }
      __builtin_amdgcn_sched_barrier(0);
      f32x4 kv[3] = {z4, z4, z4};
#pragma unroll
      for (int sw = 0; sw < 4; ++sw) {
        kv[0] = MFMA(af[sw], v0[sw], kv[0]);
        kv[1] = MFMA(af[sw], v1[sw], kv[1]);
        kv[2] = MFMA(af[sw], one8, kv[2]);
      }
      auto pc = [](float x, float y) { const unsigned t = pk2(x, y); return t < EMPTY - 1u ? t : EMPTY - 1u; };
      __builtin_amdgcn_raw_buffer_store_b128(u32x4{pc(kv[0][0], kv[0][1]), pc(kv[0][2], kv[0][3]), pc(kv[1][0], kv[1][1]), pc(kv[1][2], kv[1][3])}, rsW, gkv, 0, 16);
      __builtin_amdgcn_raw_buffer_store_b64(u32x2{pc(kv[2][0], kv[2][1]), pc(kv[2][2], kv[2][3])}, rsW, gkz, 0, 16);
    }
    ST(layer * 24 + 6)
    u32x4 l1[8];
    const bool red = w == 7 || (w == 6 && (rt & 7) == 0);
    unsigned lo = kvo + (unsigned)((hp + (lane >> 5) * 128) * 16384) + ((((rt >> 3) ^ (lane >> 5) ^ 1) & 1) * 8192 ^ P) +
                  (w == 7 ? (unsigned)((rt & 4) * 512 + (g & 1) * 1024 + (rt & 3) * 256 + fr * 16) : (unsigned)(4096 + (lane & 7) * 16));
    unsigned fs = w == 7 ? kfo + (unsigned)((rt >> 2) * 2048 + (lane >> 5) * 1024 + (rt & 3) * 256 + fr * 16 + (g & 1) * 8)
                : (lane & 24) ? 0x7fff0000u : kzo + (unsigned)((rt >> 3) * 128 + (lane & 4) * 16 + (lane & 1) * 32 + (lane >> 5) * 16 + (lane & 2) * 4);
    auto l1_load = [&]() {
      asm volatile("" ::: "memory");
#pragma unroll
      for (int p = 0; p < 8; ++p) l1[p] = __builtin_amdgcn_raw_buffer_load_b128(rsW, lo, p * (16 * 16384), 16);
      __builtin_amdgcn_sched_barrier(0);
    };
    if (red) asm volatile("" : "+v"(lo), "+v"(fs));
    if (red) { __builtin_amdgcn_s_sleep(10); l1_load(); }
    phi2(std::integral_constant<int, 4>{});
    ST(layer * 24 + 7)
    if (!red) {
      phi2(std::integral_constant<int, 6>{});
    } else {
      typedef __attribute__((ext_vector_type(2))) __bf16 bf2;
      unsigned c0 = 0x00003F80u, c1 = 0x3F800000u;
      asm volatile("" : "+v"(c0), "+v"(c1));
      float s4[4];
      _Pragma("unroll 1") for (unsigned spins = 0; spins < (1u << 18); ++spins) {
        float a[8] = {0.f, 0.f, 0.f, 0.f, 0.f, 0.f, 0.f, 0.f};
#pragma unroll
        for (int p = 0; p < 8; ++p)
#pragma unroll
          for (int e = 0; e < 4; ++e) {
            const unsigned u = l1[p][e];
            a[2 * e] = __builtin_amdgcn_fdot2_f32_bf16(__builtin_bit_cast(bf2, u), __builtin_bit_cast(bf2, c0), a[2 * e], false);
            a[2 * e + 1] = __builtin_amdgcn_fdot2_f32_bf16(__builtin_bit_cast(bf2, u), __builtin_bit_cast(bf2, c1), a[2 * e + 1], false);
          }
#pragma unroll
        for (int i = 0; i < 4; ++i) {
          auto r = __builtin_amdgcn_permlane32_swap(__float_as_uint(a[i]), __float_as_uint(a[i + 4]), false, false);
          s4[i] = __uint_as_float(r[0]) + __uint_as_float(r[1]);
        }
        const float t = s4[0] + s4[2];
        if (__all(t == t)) break;
        unsigned mxw = 0;
#pragma unroll
        for (int p = 0; p < 8; ++p)
#pragma unroll
          for (int e = 0; e < 4; ++e) mxw = l1[p][e] > mxw ? l1[p][e] : mxw;
        if (__all(mxw != EMPTY)) break;
        l1_load();
      }
      unsigned o0 = pk2(s4[0], s4[1]), o1 = pk2(s4[2], s4[3]);
      o0 = o0 < EMPTY - 1u ? o0 : EMPTY - 1u; o1 = o1 < EMPTY - 1u ? o1 : EMPTY - 1u;
      __builtin_amdgcn_raw_buffer_store_b64(u32x2{o0, o1}, rsW, fs, 0, 16);
      __builtin_amdgcn_raw_buffer_store_b64(u32x2{o0, o1}, rsW, fs, 8192, 16);
    }
    unsigned vW0 = (unsigned)(((16 * w + (lane >> 3)) * DM + hp * 64 + ((lane & 7) ^ ((lane >> 4) << 1)) * 8) * 2);
    u64 ko = korder(bid & 3, hp);
    if (!red) { asm volatile("" : "+v"(vW0)); asm volatile("" : "+s"(ko)); }
    ST(layer * 24 + 8)
    WAITL0;
    BAR;
    ST(layer * 24 + 9)
    {
      u32x4 fa, fb = {0u, 0u, 0u, 0u};
      unsigned pfo = OFF_KV + wn * 4096 + lane * 16, pzo = OFF_KV + 8192 + wn * 128 + g * 16;
      asm volatile("" : "+v"(pfo), "+v"(pzo));
      const unsigned fo = kfo + (unsigned)(tid * 16) + ((rt >> 3) * 8192 ^ P), fo2 = kzo + (unsigned)((lane & 15) * 16) + ((rt >> 3) * 8192 ^ P);
      if (red) {
        phi2(std::integral_constant<int, 6>{});
      } else if (w < 4) {
#pragma unroll
        for (int t = 6; t < 12; ++t) glds2v(wo + (size_t)knib(ko, t) * 64 * DM, vW0, vW0 + 8 * DM * 2 - 1024u, sb + OFF_WO + t * 8192 + w * 2048);
      } else {
        __builtin_amdgcn_s_sleep(11);
      }
      _Pragma("unroll 1") for (unsigned spins = 0; spins < (1u << 18); ++spins) {
        asm volatile("" ::: "memory");
        fa = __builtin_amdgcn_raw_buffer_load_b128(rsW, fo, 0, 16);
        if (w == 0) fb = __builtin_amdgcn_raw_buffer_load_b128(rsW, fo2, 0, 16);
        unsigned mxw = 0;
#pragma unroll
        for (int e = 0; e < 4; ++e) { mxw = fa[e] > mxw ? fa[e] : mxw; mxw = fb[e] > mxw ? fb[e] : mxw; }
        if (__all(mxw != EMPTY)) break;
        __builtin_amdgcn_s_sleep(1);
      }
      *(u32x4*)(smem + OFF_KV + tid * 16) = fa;
      if (tid < 16) *(u32x4*)(smem + OFF_KV + 8192 + tid * 16) = fb;
      ST(layer * 24 + 10)
      WAITL0; BAR;
      ST(layer * 24 + 11)
      bf16x8 kvf[3][2];
#pragma unroll
      for (int ks = 0; ks < 2; ++ks) {
        kvf[0][ks] = *(const bf16x8*)(smem + pfo + ks * 2048);
        kvf[1][ks] = *(const bf16x8*)(smem + pfo + ks * 2048 + 1024);
        kvf[2][ks] = *(const bf16x8*)(smem + pzo + ks * 64);
      }
#pragma unroll
      for (int rb = 0; rb < 2; ++rb) {
        f32x4 oa[3];
#pragma unroll
        for (int nb = 2; nb >= 0; --nb) {
          oa[nb] = MFMA(kvf[nb][0], qp[rb][0], z4);
          oa[nb] = MFMA(kvf[nb][1], qp[rb][1], oa[nb]);
        }
        float den = oa[2][0];
        den += 1e-6f;
        float inv = __builtin_amdgcn_rcpf(den);
        lds<u32x4>(opo + rb * 1024) = o_put(rb, oa[0] * inv, oa[1] * inv);
      }
    }
  }
  ST(layer * 24 + 12)
  if (!HH && w >= 6) {
    const unsigned v0 = (unsigned)((((w & 1) * 32 + (lane >> 3)) * DM + hp * 64 + ((lane & 7) ^ ((lane >> 4) << 1)) * 8) * 2);
    const u64 ko = korder(bid & 3, hp);
#pragma unroll
    for (int t = 6; t < 12; ++t) glds4v(wo + (size_t)knib(ko, t) * 64 * DM, v0, v0 + 15360u, v0 + 2 * 15360u, v0 + 3 * 15360u, sb + OFF_WO + t * 8192 + (w & 1) * 4096);
    WAITV(24); WAITL0; BAR;
  } else if (w >= 4) { WAITV(0); WAITL0; BAR; }
  if (first && w >= 4 && w < 7) {
    typedef __attribute__((ext_vector_type(2))) __bf16 bf2;
    unsigned c0 = 0x00003F80u, c1 = 0x3F800000u;
    asm volatile("" : "+v"(c0), "+v"(c1));
    const bf2 lo1 = __builtin_bit_cast(bf2, c0), hi1 = __builtin_bit_cast(bf2, c1);
    float a[8] = {0.f, 0.f, 0.f, 0.f, 0.f, 0.f, 0.f, 0.f};
#pragma unroll
    for (int i = 0; i < 8; ++i) {
      asm volatile("" : "+v"(pw[i]));
#pragma unroll
      for (int e = 0; e < 4; ++e) {
        const unsigned u = pw[i][e];
        a[2 * e] = __builtin_amdgcn_fdot2_f32_bf16(__builtin_bit_cast(bf2, u), lo1, a[2 * e], false);
        a[2 * e + 1] = __builtin_amdgcn_fdot2_f32_bf16(__builtin_bit_cast(bf2, u), hi1, a[2 * e + 1], false);
      }
    }
#pragma unroll
    for (int i = 0; i < 4; ++i) {
      auto r = __builtin_amdgcn_permlane32_swap(__float_as_uint(a[i]), __float_as_uint(a[i + 4]), false, false);
      a[i] = __uint_as_float(r[0]) + __uint_as_float(r[1]);
    }
#pragma unroll
    for (int i = 0; i < 2; ++i) {
      auto r = __builtin_amdgcn_permlane16_swap(__float_as_uint(a[i]), __float_as_uint(a[i + 2]), false, false);
      a[i] = __uint_as_float(r[0]) + __uint_as_float(r[1]);
      a[i] += dpp_ror<8>(a[i]);
    }
    if (!(lane & 8))
      __builtin_amdgcn_raw_buffer_store_b64(u32x2{__float_as_uint(a[0]), __float_as_uint(a[1])}, rsW,
          (unsigned)(WS_CSP + ((((hp + (1 << (w - 4))) & 15) * 16 + rt) * 192 + (w - 4) * 64 + (lane & 7) * 8 + (lane >> 5) * 4 + ((lane >> 4) & 1) * 2) * 4), 0, 16);
  }
  }
}

#undef pcs
#undef prs
#undef opo
#undef ogo
#undef pkw
#undef hof
#undef dm
#undef dm2
#undef pcv
#undef pwf
#undef prv
#undef pvf
#undef ppk
#undef ppa
#undef pvr
#undef gkv
#undef gkz

__device__ __forceinline__ void g2_phase(char* smem, char* ws, unsigned P, u64 E, int w, f32x16& xq, int layer, const u16* __restrict__ x0,
                                         const u16* __restrict__ wo, const u16* __restrict__ wnx, u16* __restrict__ out) {
  IDS
  wnx = uni(wnx);
  const unsigned sb = lds_addr(smem);
  const int lw = w & 3, k2 = 16 * lw + (lane >> 3);
  const unsigned vW0 = (unsigned)((k2 * DM + hp * 64 + ((lane & 7) ^ ((lane >> 4) << 1)) * 8) * 2), vW1 = vW0 + 8 * DM * 2 - 1024u;
  const int r0 = w * 16 + (lane >> 3);
  unsigned pxr = sb + r0 * 128 + (lane & 7) * 16;
  const unsigned rmo = (unsigned)(((m0 + r0) * DM + hp * 64 + ((lane & 7) ^ (lane >> 3)) * 8) * 2);
  const unsigned imo = SX + (unsigned)(hp * 32768 + (w >> 2) * 8192 + r0 * 128 + (lane & 7) * 16);
  constexpr int S0 = 0, S1 = 16384, S2 = 32768, S3 = 147456, S4 = OFF_WO, S5 = 98304, S6 = 114688;
  constexpr int AS[16] = {S0, S1, S2, S3, S0, S1, S4, S2, S3, S0, S1, S4, S5, S3, S0, S6};
  constexpr int OFF_C = 0;
  auto bs = [](int t) { return OFF_WO + (t < 12 ? t : t - 10) * 8192; };
  if (w >= 4) {
    constexpr int WT[15] = {0, 8, 12, 14, 16, 18, 20, 18, 16, 14, 12, 12, 10, 10, 8};
    const char* o = ws + WS_O + layer * (SEQ * DM * 2) + SX;
    const unsigned vA = (unsigned)((lw >> 1) * 16384 + (lw & 1) * 4096 + lane * 16);
    const u64 kord = korder(bid & 3, hp);
    auto ldA = [&](int s) { glds4c(o + knib(kord, s) * 32768, vA, sb + AS[s] + lw * 4096); };
    auto ldW = [&](int t) { glds2v(wo + (size_t)knib(kord, t) * 64 * DM, vW0, vW1, sb + bs(t) + lw * 2048); };
    const int fB = (k2 >> 1) & 1;
    unsigned vB0 = (unsigned)((k2 * 3072 + hp * 64 + (((((lane & 7) >> 1) ^ fB) << 1) | (lane & 1)) * 8) * 2);
    unsigned vB1 = (unsigned)(((k2 + 8) * 3072 + hp * 64 + (((((lane & 7) >> 1) ^ (fB | 2)) << 1) | (lane & 1)) * 8) * 2) - 1024u;
    int kn1 = knib(korder(kv1(bid), hp), 1);
    asm volatile("" : "+v"(vB0), "+v"(vB1));
    asm volatile("" : "+s"(kn1));
    auto ldB = [&](int s, int part) {
      const u16* b = wnx + (size_t)(s ? kn1 : hp) * 64 * 3072;
      if (part) glds4v(b, vB0, vB1, vB0, vB1, sb + s * 40960 + 16384 + lw * 6144);
      else glds2v(b + 2048, vB0, vB1, sb + s * 40960 + 16384 + lw * 6144 + 4096);
    };
    BAR;
    ldA(1); ldA(2); ldA(3); ldA(4);
    WAITV(12);
    BAR;
    auto step = [&](auto ktc) {
      constexpr int kt = decltype(ktc)::value;
      WAITVN(WT[kt]);
      BAR;
      if constexpr (kt == 1) { ldA(5); ldA(6); }
      if constexpr (kt >= 2 && kt <= 10) ldA(kt + 5);
      if constexpr (kt >= 2 && kt <= 5) ldW(kt + 10);
      if constexpr (kt >= 11) ldB((kt - 11) >> 1, (kt - 11) & 1);
      if constexpr (kt >= 8) { __builtin_amdgcn_s_sleep(kt >= 11 ? 2 : 1); if (layer == 1) __builtin_amdgcn_s_sleep(1); }
    };
    step(IC<1>{}); step(IC<2>{}); step(IC<3>{}); step(IC<4>{}); step(IC<5>{}); step(IC<6>{}); step(IC<7>{});
    step(IC<8>{}); step(IC<9>{}); step(IC<10>{}); step(IC<11>{}); step(IC<12>{}); step(IC<13>{}); step(IC<14>{});
  } else {
    const int wm = w >> 1, wn = w & 1;
    f32x4 acc[4][2];
    unsigned co[2];
#pragma unroll
    for (int cb = 0; cb < 2; ++cb) co[cb] = sb + OFF_C + (unsigned)((wm * 64 + fr) * 128 + (((wn * 4 + cb * 2 + (g >> 1)) ^ (fr & 7)) << 4) + (g & 1) * 8);
    const int fl = (q4 >> 1) | ((g & 1) << 1);
    const int bb0 = (4 * g + q4) * 128 + p4 * 8 + (((wn * 2) ^ fl) << 5), bb1 = (4 * g + q4) * 128 + p4 * 8 + (((wn * 2 + 1) ^ fl) << 5);
    const int abase = wm * 4096 + lane * 16;
    bf16x8 fa[2][2][4], fb[2][2][2];
    unsigned ra[3], rb0[2], rb1[2];
#pragma unroll
    for (int i = 0; i < 3; ++i) { ra[i] = abase + i * 65536; asm volatile("" : "+v"(ra[i])); }
#pragma unroll
    for (int i = 0; i < 2; ++i) { rb0[i] = bb0 + OFF_WO + i * 49152; rb1[i] = bb1 + OFF_WO + i * 49152; asm volatile("" : "+v"(rb0[i]), "+v"(rb1[i])); }
    auto loadf = [&](int kt, bf16x8 (&a)[2][4], bf16x8 (&b)[2][2]) {
#pragma unroll
      for (int s = 0; s < 2; ++s) {
        const char* sA = smem + ra[AS[kt] >> 16] + (AS[kt] & 65535) + s * 8192;
        const int hi = bs(kt) >= OFF_WO + 49152, ob = bs(kt) - OFF_WO - hi * 49152 + s * 4096;
        b[s][0] = cat8(tr_read(smem + rb0[hi] + ob), tr_read(smem + rb0[hi] + ob + 2048));
        b[s][1] = cat8(tr_read(smem + rb1[hi] + ob), tr_read(smem + rb1[hi] + ob + 2048));
#pragma unroll
        for (int rb = 0; rb < 4; ++rb) a[s][rb] = *(const bf16x8*)(sA + rb * 1024);
      }
    };
    auto mma = [&](bf16x8 (&a)[2][4], bf16x8 (&b)[2][2]) {
#pragma unroll
      for (int i = 0; i < 16; ++i) {
        const int rb = i >> 2, cb = ((i >> 1) ^ rb) & 1, s = ((i + 1) >> 1) & 1;
        acc[rb][cb] = MFMA(b[s][cb], a[s][rb], acc[rb][cb]);
      }
    };
    unsigned fo = lane < 16 && lane != hp ? (WS_FLAGS + (unsigned)(rt * 16 + lane) * FLS) ^ P : 0u;
    const u64 ko = korder(bid & 3, hp);
    unsigned kf = kfirst(ko), kf2 = kfirst(ko >> 16) | (1u << knib(ko, 9));
    unsigned jo = (unsigned)((((rt & 7) * 128 + (w - 1) * 64 + lane) * 3072 + (hp & 3) * 1024 + (hp | 3) * 64) * 2);
    unsigned fo1 = ((kf >> (lane & 31)) & 1) ? fo : 0u, fo2 = ((kf2 >> (lane & 31)) & 1) ? fo : 0u;
    fo = (((kf | kf2) >> (lane & 31)) & 1) ? 0u : fo;
    asm volatile("" : "+v"(fo), "+v"(fo1), "+v"(fo2));
    asm volatile("" : "+s"(kf), "+s"(kf2));
    publish(rsW, tid, rt * 16 + hp, E + (unsigned)(layer + 1), P);
    ST(layer * 24 + 13)
    u64 fl1 = 0;
    if (w == 0) asm volatile("global_load_dwordx2 %0, %1, %2 sc1" : "+v"(fl1) : "v"(fo1), "s"(ws) : "memory");
    if (layer == 0) {
      const unsigned xo = (unsigned)(((m0 + wm * 64 + fr) * DM + hp * 64 + wn * 32 + g * 8) * 2);
      asm volatile("global_load_dwordx4 v[64:67], %1, %3\n\tglobal_load_dwordx4 v[68:71], %1, %4\n\tglobal_load_dwordx4 v[72:75], %2, %3\n\tglobal_load_dwordx4 v[76:79], %2, %4"
                   : "+{v[64:79]}"(xq) : "v"(xo), "v"(xo + 32 * DM * 2), "s"(x0), "s"(x0 + 16 * DM) : "memory");
    }
    loadf(0, fa[0], fb[0]);
    unsigned junk = 0;
    if (layer < 3 && (hp & 7) < 3 && (w == 1 || w == 2))
      asm volatile("global_load_dword %0, %1, %2" : "+v"(junk) : "v"(jo), "s"(wnx) : "memory");
    if (layer == 0 && w == 3) {
      CODE_TOUCH(2, ".Lmk_g1b", ".Lmk_g1a")
      asm volatile("global_load_dword %0, %1, %2" : "+v"(junk) : "v"(v), "s"(pc) : "memory");
    }
    if (layer == 1 && w == 3) {
      CODE_TOUCH(3, ".Lmk_f0", ".Lmk_f1")
      asm volatile("global_load_dword %0, %1, %2" : "+v"(junk) : "v"(v), "s"(pc) : "memory");
    }
    __builtin_amdgcn_sched_barrier(0);
    bf16x8 id[2];
#pragma unroll
    for (int cb = 0; cb < 2; ++cb) {
      const unsigned one = (g >> 1) == cb && (g & 1) == (fr >> 3) ? 0x3F80u << ((fr & 1) * 16) : 0u;
      u32x4 t;
#pragma unroll
      for (int j = 0; j < 4; ++j) t[j] = ((fr >> 1) & 3) == j ? one : 0u;
      id[cb] = __builtin_bit_cast(bf16x8, t);
      asm volatile("" : "+v"(id[cb]));
    }
    asm volatile("" : "+v"(co[0]), "+v"(co[1]));
#pragma unroll
    for (int rb = 0; rb < 4; ++rb) { acc[rb][0] = f32x4{0, 0, 0, 0}; acc[rb][1] = f32x4{0, 0, 0, 0}; }
    mma(fa[0], fb[0]);
    __builtin_amdgcn_sched_barrier(0);
    WAITL0;
    u64 miss = 0;
    if (w == 0) { ARRIVED("+v"(fl1)); if (__ballot(fl1 != E + (unsigned)(layer + 1)) & kf) poll_eq(ws, fo1, E + (unsigned)(layer + 1), kf, kf); miss = 0xffffu & ~(kf | kf2 | (1u << hp)); }
    ST(layer * 24 + 14)
    __builtin_amdgcn_sched_barrier(0);
    BAR;
    __builtin_amdgcn_sched_barrier(0);
    asm volatile("" ::: "memory");
    if (layer == 0) WAITV(0);
    __builtin_amdgcn_sched_barrier(0);
    asm volatile("" : "+{v[64:79]}"(xq));
    const bf16x8 xf[4] = {__builtin_bit_cast(bf16x8, quad<0>(xq)), __builtin_bit_cast(bf16x8, quad<1>(xq)), __builtin_bit_cast(bf16x8, quad<2>(xq)), __builtin_bit_cast(bf16x8, quad<3>(xq))};
#pragma unroll
    for (int rb = 0; rb < 4; ++rb) { acc[rb][0] = MFMA(id[0], xf[rb], acc[rb][0]); acc[rb][1] = MFMA(id[1], xf[rb], acc[rb][1]); }
    if (w == 0) poll_eq(ws, fo2, E + (unsigned)(layer + 1), kf2, kf2);
    u64 fl2 = 0;
    auto step = [&](auto ktc) {
      constexpr int kt = decltype(ktc)::value;
      __builtin_amdgcn_sched_barrier(0);
      if (kt == 5 && miss) {
        ARRIVED("+v"(fl2));
        if (__ballot(fl2 != E + (unsigned)(layer + 1)) & miss) poll_eq(ws, fo, E + (unsigned)(layer + 1), miss, ~0ull);
      }
      WAITL0;
      BAR;
      if (kt == 2 && miss) asm volatile("global_load_dwordx2 %0, %1, %2 sc1" : "+v"(fl2) : "v"(fo), "s"(ws) : "memory");
      if (kt == 0) { ST(layer * 24 + 15) }
      __builtin_amdgcn_sched_barrier(0);
      loadf(kt + 1, fa[(kt + 1) & 1], fb[(kt + 1) & 1]);
      if (kt == 0) return;
      mma(fa[kt & 1], fb[kt & 1]);
#pragma unroll
      for (int i = 0; i < 4; ++i) {
        __builtin_amdgcn_sched_group_barrier(0x008, 1, 0);
        __builtin_amdgcn_sched_group_barrier(0x100, 1, 0);
        __builtin_amdgcn_sched_group_barrier(0x008, 1, 0);
        __builtin_amdgcn_sched_group_barrier(0x100, 3, 0);
      }
#pragma unroll
      for (int i = 0; i < 8; ++i) __builtin_amdgcn_sched_group_barrier(0x008, 1, 0);
    };
    step(IC<0>{}); step(IC<1>{}); step(IC<2>{}); step(IC<3>{}); step(IC<4>{}); step(IC<5>{}); step(IC<6>{}); step(IC<7>{});
    step(IC<8>{}); step(IC<9>{}); step(IC<10>{}); step(IC<11>{}); step(IC<12>{}); step(IC<13>{}); step(IC<14>{});
    WAITV(0);
    asm volatile("" :: "v"(junk));
    __builtin_amdgcn_sched_barrier(0);
    auto put = [&](int rb) {
#pragma unroll
      for (int cb = 0; cb < 2; ++cb) lds<u32x2>(co[cb] + rb * 2048) = u32x2{pk2(acc[rb][cb][0], acc[rb][cb][1]), pk2(acc[rb][cb][2], acc[rb][cb][3])};
    };
#pragma unroll
    for (int rb = 0; rb < 4; ++rb)
#pragma unroll
      for (int cb = 0; cb < 2; ++cb) acc[rb][cb] = MFMA(fb[1][0][cb], fa[1][0][rb], acc[rb][cb]);
#pragma unroll
    for (int rb = 0; rb < 4; ++rb) {
      __builtin_amdgcn_sched_barrier(0);
#pragma unroll
      for (int cb = 0; cb < 2; ++cb) acc[rb][cb] = MFMA(fb[1][1][cb], fa[1][1][rb], acc[rb][cb]);
      __builtin_amdgcn_sched_barrier(0);
      if (rb >= 2) put(rb - 2);
    }
    __builtin_amdgcn_sched_barrier(0);
    ST(layer * 24 + 16)
    put(2); put(3);
  }
  WAITL0; BAR;
  asm volatile("" ::: "memory");
  u32x4 xn[2];
#pragma unroll
  for (int i = 0; i < 2; ++i) xn[i] = lds<u32x4>(pxr + OFF_C + i * 1024);
  if (layer == 3) {
    const __amdgpu_buffer_rsrc_t rsO = __builtin_amdgcn_make_buffer_rsrc((void*)uni(out), (short)0, SEQ * DM * 2, 0x00020000);
    __builtin_amdgcn_raw_buffer_store_b128(xn[0], rsO, rmo, 0, 16);
    __builtin_amdgcn_raw_buffer_store_b128(xn[1], rsO, rmo + 8 * DM * 2, 0, 16);
  } else {
    __builtin_amdgcn_raw_buffer_store_b128(xn[0], rsW, WS_X + layer * (SEQ * DM * 2) + imo, 0, 16);
    __builtin_amdgcn_raw_buffer_store_b128(xn[1], rsW, WS_X + layer * (SEQ * DM * 2) + imo + 1024, 0, 16);
    float mean[2], m2[2];
#pragma unroll
    for (int i = 0; i < 2; ++i) {
      typedef __attribute__((ext_vector_type(2))) __bf16 bf2;
      const bf2 one2 = __builtin_bit_cast(bf2, 0x3F803F80u);
      float sa = 0.f, sq = 0.f;
#pragma unroll
      for (int e = 0; e < 4; ++e) {
        const unsigned u = xn[i][e];
        const bf2 t = __builtin_bit_cast(bf2, u);
        sa = __builtin_amdgcn_fdot2_f32_bf16(t, one2, sa, false);
        sq = __builtin_amdgcn_fdot2_f32_bf16(t, t, sq, false);
      }
      mean[i] = sa;
      m2[i] = sq;
    }
#define DPA(c) "v_add_f32_dpp %0, %0, %0 " c "\n\tv_add_f32_dpp %1, %1, %1 " c "\n\tv_add_f32_dpp %2, %2, %2 " c "\n\tv_add_f32_dpp %3, %3, %3 " c "\n\t"
    asm volatile("s_nop 3\n\t" DPA("quad_perm:[1,0,3,2] row_mask:0xf bank_mask:0xf") DPA("quad_perm:[2,3,0,1] row_mask:0xf bank_mask:0xf")
                 DPA("row_half_mirror row_mask:0xf bank_mask:0xf") "s_nop 0" : "+v"(mean[0]), "+v"(m2[0]), "+v"(mean[1]), "+v"(m2[1]));
    if ((lane & 7) < 2) {
      const int i = lane & 1;
      const u32x2 sv = {__float_as_uint(i ? mean[1] : mean[0]), __float_as_uint(i ? m2[1] : m2[0])};
      __builtin_amdgcn_raw_buffer_store_b64(sv, rsW, WS_ST + layer * (SEQ * 128) + (unsigned)((rt & 7) * 32768 + (hp >> 3) * 16384 + (hp & 7) * 1024 + (r0 + i * 8) * 8) + ((rt >> 3) * 8192 ^ P), 0, 16);
    }
  }
  ST(layer * 24 + 17)
  if (layer < 3 && w >= 4) { WAITV(0); BAR; }
  ST(layer * 24 + 18)
}

__global__ __launch_bounds__(512, 2) void mega_kernel(const u16* __restrict__ x0, const u16* __restrict__ wqkvA,
                                                      const u16* __restrict__ woA, const u16* __restrict__ wqkvB,
                                                      const u16* __restrict__ wphi, const u16* __restrict__ woB,
                                                      u16* __restrict__ out, char* __restrict__ ws) {
  unsigned P = 0;
  extern __shared__ __attribute__((aligned(16))) char smem[];
  const int w = __builtin_amdgcn_readfirstlane(threadIdx.x >> 6);
  u64 E = 0;
  {
    const int bid = blockIdx.x, lane = threadIdx.x & 63;
    const int rt = 2 * (bid & 4) + ((bid >> 3) & 7), hp = 4 * (bid & 3) + (bid >> 6), m0 = rt * 128;
    if (w < 4) {
      if (w == 3) {
        unsigned long long pc;
        unsigned len;
        asm volatile("s_getpc_b64 %0\n.Lmk_pc:\n\ts_mov_b32 %1, .Lmk_end-.Lmk_pc" : "=s"(pc), "=s"(len));
        const unsigned per = ((len >> 10) + 1) * 128, off = ((bid >> 3) & 7) * per + (lane * 128 < per ? lane * 128 : 0);
        asm volatile("s_mov_b32 m0, %2\n\ts_nop 0\n\tglobal_load_lds_dword %0, %1" :: "v"(off < len - 4 ? off : 0u), "s"(pc), "s"(lds_addr(smem) + LDS_TOTAL - 1024) : "memory", "m0");
      }
      const unsigned xo = (unsigned)((m0 + hp * 8 + w * 2) * (DM * 2) + lane * 16);
      glds4v(x0, xo, xo, xo, xo, lds_addr(smem) + 122880 + w * 4096);
      asm volatile("s_mov_b32 m0, %2\n\ts_nop 0\n\tglobal_load_lds_dword %0, %1" :: "v"(lane * 4), "s"(ws), "s"(lds_addr(smem) + OFF_E) : "memory", "m0");
    } else {
      const int lw = w & 3;
      const int kB = 16 * lw + (lane >> 3), fB = (kB >> 1) & 1;
      const unsigned vB0 = (unsigned)((kB * 3072 + hp * 64 + (((((lane & 7) >> 1) ^ fB) << 1) | (lane & 1)) * 8) * 2);
      const unsigned vB1 = (unsigned)(((kB + 8) * 3072 + hp * 64 + (((((lane & 7) >> 1) ^ (fB | 2)) << 1) | (lane & 1)) * 8) * 2) - 1024u;
      const unsigned vA = (unsigned)(((m0 + lw * 32 + (lane >> 3)) * DM + ((lane & 7) ^ (lane >> 3)) * 8) * 2);
#pragma unroll
      for (int s = 0; s < 1; ++s) {
        const int kt = (rt & 8) + s;
        const unsigned sA = lds_addr(smem) + s * 40960, sB = sA + 16384;
        glds4v(x0 + kt * 64, vA, vA + 15360u, vA + 2 * 15360u, vA + 3 * 15360u, sA + lw * 4096);
        const u16* b = wqkvA + (size_t)kt * 64 * 3072;
        glds4v(b, vB0, vB1, vB0, vB1, sB + lw * 6144);
        glds2v(b + 2048, vB0, vB1, sB + lw * 6144 + 4096);
      }
    }
  }
  f32x16 xq;
#pragma unroll 1
  for (int layer = 0; layer < 4; ++layer) {
    if (layer & 1) g1_phase<1>(smem, ws, P, E, w, xq, layer, x0, wqkvB, woB, wphi, nullptr, nullptr);
    else g1_phase<0>(smem, ws, P, E, w, xq, layer, x0, wqkvA, woA, nullptr, wqkvB, woB);
    g2_phase(smem, ws, P, E, w, xq, layer, x0, (layer & 1) ? woB : woA, (layer & 1) ? wqkvA : wqkvB, out);
  }
  asm volatile(".Lmk_end:");
  WAITV(0);
  if (blockIdx.x == 0 && w == 0 && lane_id() == 0) __hip_atomic_store((gu64*)ws, E + 16u, __ATOMIC_RELAXED, __HIP_MEMORY_SCOPE_AGENT);
}

extern "C" void kernel_launch(void* const* d_in, const int* in_sizes, int n_in,
                              void* d_out, int out_size, void* d_ws, size_t ws_size,
                              hipStream_t stream) {
  hipFuncSetAttribute((const void*)mega_kernel, hipFuncAttributeMaxDynamicSharedMemorySize, LDS_TOTAL);
  mega_kernel<<<256, 512, LDS_TOTAL, stream>>>((const u16*)d_in[0], (const u16*)d_in[1], (const u16*)d_in[2], (const u16*)d_in[4],
                                               (const u16*)d_in[5], (const u16*)d_in[6], (u16*)d_out, (char*)d_ws);
}
```

```cpp
#include <hip/hip_runtime.h>
#include <hip/hip_bf16.h>
#include <stdint.h>
#include <type_traits>
#pragma clang diagnostic ignored "-Winline-asm"
#pragma clang diagnostic ignored "-Wunused-value"

typedef unsigned short u16;
typedef __attribute__((ext_vector_type(8))) short bf16x8;
typedef __attribute__((ext_vector_type(4))) short bf16x4;
typedef __attribute__((ext_vector_type(4))) float f32x4;
typedef __attribute__((ext_vector_type(4))) unsigned u32x4;
typedef __attribute__((ext_vector_type(2))) unsigned u32x2;
typedef __attribute__((ext_vector_type(8))) float f32x8;
typedef __attribute__((ext_vector_type(16))) unsigned u32x16;
typedef __attribute__((ext_vector_type(16))) float f32x16;
typedef __attribute__((ext_vector_type(32))) float f32x32;
template <int I, typename T> __device__ __forceinline__ f32x4 quad(const T& v) { return __builtin_shufflevector(v, v, 4 * I, 4 * I + 1, 4 * I + 2, 4 * I + 3); }

constexpr int SEQ = 2048, DM = 1024, NH = 32;
#define ST(i)
constexpr int KVP = 64 * 32 + 64;

typedef __attribute__((ext_vector_type(2))) float f32x2;
typedef __attribute__((ext_vector_type(2))) __bf16 bf16x2_t;
__device__ __forceinline__ unsigned pk2(float a, float b) {
  f32x2 v = {a, b};
  bf16x2_t r = __builtin_convertvector(v, bf16x2_t);
  return __builtin_bit_cast(unsigned, r);
}

__device__ __forceinline__ bf16x4 tr_read(const char* p) {
  return __builtin_amdgcn_ds_read_tr16_b64_v4i16((__attribute__((address_space(3))) bf16x4*)p);
}
template <typename T> __device__ __forceinline__ __attribute__((address_space(3))) T& lds(unsigned a) { return *(__attribute__((address_space(3))) T*)(size_t)a; }
__device__ __forceinline__ bf16x4 tr_read(unsigned a) { return __builtin_amdgcn_ds_read_tr16_b64_v4i16(&lds<bf16x4>(a)); }
__device__ __forceinline__ bf16x8 cat8(bf16x4 a, bf16x4 b) {
  bf16x8 f = {a[0], a[1], a[2], a[3], b[0], b[1], b[2], b[3]};
  return f;
}
__device__ __forceinline__ void glds16s(const void* sbase, unsigned voff, unsigned lds_base) {
  asm volatile("s_mov_b32 m0, %2\n\ts_nop 0\n\tglobal_load_lds_dwordx4 %0, %1" :: "v"(voff), "s"(sbase), "s"(lds_base) : "memory", "m0");
}
__device__ __forceinline__ void glds16c(const void* sbase, unsigned voff, unsigned lds_base) {
  asm volatile("s_mov_b32 m0, %2\n\ts_nop 0\n\tglobal_load_lds_dwordx4 %0, %1 sc1" :: "v"(voff), "s"(sbase), "s"(lds_base) : "memory", "m0");
}
#define GL_ "\n\tglobal_load_lds_dwordx4 "
__device__ __forceinline__ void glds4c(const void* sbase, unsigned v, unsigned lds_base) {
  asm volatile("s_mov_b32 m0, %2\n\ts_nop 0" GL_ "%0, %1 sc1" GL_ "%0, %1 offset:1024 sc1" GL_ "%0, %1 offset:2048 sc1" GL_ "%0, %1 offset:3072 sc1"
               :: "v"(v), "s"(sbase), "s"(lds_base) : "memory", "m0");
}
__device__ __forceinline__ void glds4v(const void* sbase, unsigned v0, unsigned v1, unsigned v2, unsigned v3, unsigned lds_base) {
  asm volatile("s_mov_b32 m0, %5\n\ts_nop 0" GL_ "%0, %4" GL_ "%1, %4 offset:1024" GL_ "%2, %4 offset:2048" GL_ "%3, %4 offset:3072"
               :: "v"(v0), "v"(v1), "v"(v2), "v"(v3), "s"(sbase), "s"(lds_base) : "memory", "m0");
}
__device__ __forceinline__ void glds2v(const void* sbase, unsigned v0, unsigned v1, unsigned lds_base) {
  asm volatile("s_mov_b32 m0, %3\n\ts_nop 0" GL_ "%0, %2" GL_ "%1, %2 offset:1024" :: "v"(v0), "v"(v1), "s"(sbase), "s"(lds_base) : "memory", "m0");
}
__device__ __forceinline__ void gld16(u32x4& d, unsigned voff, const void* sbase) {
  asm volatile("global_load_dwordx4 %0, %1, %2" : "=v"(d) : "v"(voff), "s"(sbase) : "memory");
}
__device__ __forceinline__ unsigned lds_addr(const void* p) { return (unsigned)(size_t)p; }
#define WAITV(n) asm volatile("s_waitcnt vmcnt(" #n ")" ::: "memory")
#define WAITVN(n) asm volatile("s_waitcnt vmcnt(%0)" :: "n"(n) : "memory")
#define WAITL0 do { asm volatile("" ::: "memory"); __builtin_amdgcn_s_waitcnt(0xC07F); asm volatile("" ::: "memory"); } while (0)
#define BAR __builtin_amdgcn_s_barrier()
#define ARRIVED(...) do { WAITV(0); __builtin_amdgcn_sched_barrier(0); asm volatile("" : __VA_ARGS__); } while (0)

__device__ __forceinline__ float vmaxf(float a, float b) { float r; asm("v_max_f32 %0, %1, %2" : "=v"(r) : "v"(a), "v"(b)); return r; }
template <int N> __device__ __forceinline__ float dpp_ror(float x) {
  return __uint_as_float(__builtin_amdgcn_update_dpp(0u, __float_as_uint(x), 0x120 + N, 0xf, 0xf, false));
}
__device__ __forceinline__ float r16_sum(float x) { x += dpp_ror<8>(x); x += dpp_ror<4>(x); x += dpp_ror<2>(x); return x + dpp_ror<1>(x); }
__device__ __forceinline__ float vmax3f(float a, float b, float c) { float r; asm("v_max3_f32 %0, %1, %2, %3" : "=v"(r) : "v"(a), "v"(b), "v"(c)); return r; }
__device__ __forceinline__ void max8pn(const f32x4& a, const f32x4& b, float& mx, float& mn) {
  asm("v_max3_f32 %0, %2, %3, %4\n\tv_max3_f32 %1, -%2, -%3, -%4\n\tv_max3_f32 %0, %0, %5, %6\n\tv_max3_f32 %1, %1, -%5, -%6\n\t"
      "v_max3_f32 %0, %0, %7, %8\n\tv_max3_f32 %1, %1, -%7, -%8\n\tv_max_f32 %0, %0, %9\n\tv_max_f32 %1, %1, -%9\n\ts_nop 0"
      : "=&v"(mx), "=&v"(mn) : "v"(a[0]), "v"(a[1]), "v"(a[2]), "v"(a[3]), "v"(b[0]), "v"(b[1]), "v"(b[2]), "v"(b[3]));
}
__device__ __forceinline__ float vmaxf_n(float a, float b) { float r; asm("v_max_f32 %0, %1, %2\n\ts_nop 0" : "=v"(r) : "v"(a), "v"(b)); return r; }
__device__ __forceinline__ float vmax3f_n(float a, float b, float c) { float r; asm("v_max3_f32 %0, %1, %2, %3\n\ts_nop 0" : "=v"(r) : "v"(a), "v"(b), "v"(c)); return r; }
template <bool MAXOP> __device__ __forceinline__ float rop(float a, float b) { return MAXOP ? vmaxf_n(a, b) : a + b; }
template <bool MAXOP> __device__ __forceinline__ void xr2(float& a, float& b) {
  auto r = __builtin_amdgcn_permlane16_swap(__float_as_uint(a), __float_as_uint(b), false, false);
  unsigned u = __float_as_uint(rop<MAXOP>(__uint_as_float(r[0]), __uint_as_float(r[1])));
  auto r2 = __builtin_amdgcn_permlane32_swap(u, u, false, false);
  u = __float_as_uint(rop<MAXOP>(__uint_as_float(r2[0]), __uint_as_float(r2[1])));
  auto r3 = __builtin_amdgcn_permlane16_swap(u, u, false, false);
  a = __uint_as_float(r3[0]); b = __uint_as_float(r3[1]);
}
template <bool MAXOP> __device__ __forceinline__ void xr4(float& a, float& b, float& c, float& d) {
  auto r = __builtin_amdgcn_permlane16_swap(__float_as_uint(a), __float_as_uint(b), false, false);
  auto q = __builtin_amdgcn_permlane16_swap(__float_as_uint(c), __float_as_uint(d), false, false);
  const unsigned ab = __float_as_uint(rop<MAXOP>(__uint_as_float(r[0]), __uint_as_float(r[1])));
  const unsigned cd = __float_as_uint(rop<MAXOP>(__uint_as_float(q[0]), __uint_as_float(q[1])));
  auto t = __builtin_amdgcn_permlane32_swap(ab, cd, false, false);
  const unsigned u = __float_as_uint(rop<MAXOP>(__uint_as_float(t[0]), __uint_as_float(t[1])));
  auto h = __builtin_amdgcn_permlane32_swap(u, u, false, false);
  auto p0 = __builtin_amdgcn_permlane16_swap(h[0], h[0], false, false);
  auto p1 = __builtin_amdgcn_permlane16_swap(h[1], h[1], false, false);
  a = __uint_as_float(p0[0]); b = __uint_as_float(p0[1]); c = __uint_as_float(p1[0]); d = __uint_as_float(p1[1]);
}

__device__ __forceinline__ unsigned xs4(float a, float b, float c, float d) {
  auto r = __builtin_amdgcn_permlane16_swap(__float_as_uint(a), __float_as_uint(b), false, false);
  auto q = __builtin_amdgcn_permlane16_swap(__float_as_uint(c), __float_as_uint(d), false, false);
  auto t = __builtin_amdgcn_permlane32_swap(__float_as_uint(__uint_as_float(r[0]) + __uint_as_float(r[1])), __float_as_uint(__uint_as_float(q[0]) + __uint_as_float(q[1])), false, false);
  return __float_as_uint(__uint_as_float(t[0]) + __uint_as_float(t[1]));
}

#define MFMA(a, b, c) __builtin_amdgcn_mfma_f32_16x16x32_bf16(a, b, c, 0, 0, 0)

typedef unsigned long long u64;
typedef __attribute__((address_space(1))) unsigned long long gu64;
constexpr unsigned WS_PL = 0x1a0000, WS_PR = 1024;
constexpr unsigned WS_FLAGS = 16384;
constexpr int FLS = 64;
constexpr unsigned WS_G0 = 65536;
constexpr unsigned WS_CSP = WS_G0 + SEQ * 16;
constexpr unsigned WS_ST = WS_CSP + 16 * 16 * 768;
constexpr unsigned WS_X = 1u << 21;
constexpr unsigned WS_O = WS_X + 3u * SEQ * DM * 2;
constexpr unsigned KVPB = KVP * 2, KVFB = KVP * 4;
constexpr unsigned EMPTY = 0xffffffffu;
constexpr unsigned WS_KV = WS_O + 4u * SEQ * DM * 2;
constexpr unsigned KVLB = 256u * 16384;
constexpr unsigned WS_KF = 0x110000, KFLB = 17u * 16384;
constexpr unsigned WS_HL = WS_KV + 2u * KVLB;
static_assert(WS_ST % 16384 == 0 && WS_ST + 3u * SEQ * 128 <= WS_KF && WS_KF + 2 * KFLB <= WS_PL && WS_PL + 16384 <= WS_X && (WS_X >> 21 & 1) && (WS_O >> 21 & 1) && (WS_KV >> 21 & 1) && (WS_HL >> 21 & 1) && WS_HL % (1u << 21) == 0, "");
constexpr unsigned WS_CSA = WS_HL + 512u * 4096;
constexpr unsigned WS_CPA = WS_CSA + 256u * 1024;
constexpr unsigned WS_END = WS_CPA + 16 * 16 * 768;
constexpr int LDS_TOTAL = 163840;
constexpr int OFF_E = 139264;
constexpr int OFF_WO = 49152;
template <int V> using IC = std::integral_constant<int, V>;
__device__ __forceinline__ bf16x8 ones8() { unsigned e = 0x3F803F80u; asm volatile("" : "+v"(e)); return __builtin_bit_cast(bf16x8, u32x4{e, e, e, e}); }
__device__ __forceinline__ float zero1() { float z; asm volatile("v_mov_b32 %0, 0" : "=v"(z)); return z; }
__device__ __forceinline__ u32x4 empty4() { unsigned e = EMPTY; asm volatile("" : "+v"(e)); return u32x4{e, e, e, e}; }
__device__ __forceinline__ u64 korder(int v, int own) {
  const u64 t = v == 0 ? 0x3bf7210edca98654ull : v == 1 ? 0xb37f654210edca98ull : v == 2 ? 0x3bf7a98654210edcull : 0xb37fedca98654210ull;
  const u64 inv = v == 0 ? 0xd876e543c210fba9ull : v == 1 ? 0xc543f210dba9e876ull : v == 2 ? 0xd210eba9c876f543ull : 0xcba9f876d543e210ull;
  const u64 low = (1ull << (4 * ((inv >> (4 * own)) & 15))) - 1ull;
  return (u64)own | ((t & low) << 4) | (t & ~((low << 4) | 15ull));
}
__device__ __forceinline__ u64 korder0(int rt) {
  const int r = rt & 7;
  const u64 own = (0x765432765432ull >> (8 * ((0x4924 >> (2 * r)) & 3))) & 0xffffffull, oth = (0x7654321076543210ull >> (4 * r)) & 0xffffffffull;
  const u64 t = 0x10ull | (oth << 8) | (own << 40);
  return t | (rt < 8 ? 0x0000008888888800ull : 0x8888880000000088ull);
}
__device__ __forceinline__ int knib(u64 t, int s) { return (int)(t >> (4 * s)) & 15; }
__device__ __forceinline__ unsigned kfirst(u64 t) { return (1u << knib(t, 1)) | (1u << knib(t, 2)) | (1u << knib(t, 3)) | (1u << knib(t, 4)); }
__device__ __forceinline__ int kv1(int bid) { return (bid + (0x4924 >> ((bid >> 2) & 14))) & 3; }

template <typename T> __device__ __forceinline__ T* uni(T* p) {
  const unsigned long long v = (unsigned long long)p;
  const unsigned lo = __builtin_amdgcn_readfirstlane((unsigned)v), hi = __builtin_amdgcn_readfirstlane((unsigned)(v >> 32));
  return (T*)(((unsigned long long)hi << 32) | lo);
}
__device__ __forceinline__ u64 uni2(unsigned lo, unsigned hi) {
  lo = __builtin_amdgcn_readfirstlane(lo); hi = __builtin_amdgcn_readfirstlane(hi);
  return ((u64)hi << 32) | lo;
}
__device__ __forceinline__ int lane_id() {
  int l;
  asm volatile("v_mbcnt_lo_u32_b32 %0, -1, 0\n\tv_mbcnt_hi_u32_b32 %0, -1, %0" : "=v"(l));
  __builtin_assume(l >= 0 && l < 64);
  return l;
}
#define CODE_TOUCH(n, from, to) \
  unsigned long long pc; unsigned v; \
  { unsigned o, len; \
    asm volatile("s_getpc_b64 %0\n.Lmk_t" #n ":\n\ts_mov_b32 %1, " from "-.Lmk_t" #n "+0x40000\n\ts_mov_b32 %2, " to "-" from : "=s"(pc), "=s"(o), "=s"(len)); \
    pc -= 0x40000; v = (unsigned)(((bid >> 3) & 7) + lane * 8) * 128; v = o + ((int)v < (int)len ? v : 0u); }
#define IDS \
  const int lane = lane_id(); \
  asm volatile("" : "+s"(w)); \
  const int tid = w * 64 + lane; \
  int bid = blockIdx.x; asm volatile("" : "+s"(bid)); \
  ws = uni(ws); asm volatile("" : "+s"(ws)); \
  layer = __builtin_amdgcn_readfirstlane(layer); x0 = uni(x0); wo = uni(wo); \
    \
  const int rt = 2 * (bid & 4) + ((bid >> 3) & 7); \
  const int hp = 4 * (bid & 3) + (bid >> 6); \
  const int m0 = rt * 128; \
  P = __builtin_amdgcn_readfirstlane(P); \
  const unsigned SX = (unsigned)((rt & 7) * 524288) + ((((rt >> 3) ^ (rt >> 2) ^ 1) & 1) * 8192 ^ P); (void)SX; \
  const int fr = lane & 15, g = lane >> 4; \
  const int q4 = (lane & 15) >> 2, p4 = lane & 3; \
  const __amdgpu_buffer_rsrc_t rsW = __builtin_amdgcn_make_buffer_rsrc((void*)ws, (short)0, (int)WS_END, 0x00020000); \
  (void)m0; (void)fr; (void)q4; (void)p4; (void)rsW; (void)g;

__device__ __forceinline__ void publish(__amdgpu_buffer_rsrc_t rsW, int tid, unsigned idx, u64 v, unsigned P) {
  unsigned off = (WS_FLAGS + idx * FLS) ^ P;
  asm volatile("" : "+v"(off), "+v"(v));
  WAITV(0);
  WAITL0; BAR;
  if (tid == 192) __builtin_amdgcn_raw_buffer_store_b64(__builtin_bit_cast(u32x2, v), rsW, off, 0, 16);
  asm volatile("" ::: "memory");
}
__device__ __forceinline__ void poll_flags(char* ws, unsigned P, u64 E, int set, int rt, int hp, int lane, unsigned n, int grp) {
  const int l = lane & 15, g = lane >> 4;
  const int fi = set * 256 + (g == 2 ? l * 16 + hp : rt * 16 + l);
  const unsigned need = g == grp ? n : 0u;
  const unsigned off = need ? (WS_FLAGS + fi * FLS) ^ P : 0u;
  _Pragma("unroll 1") for (unsigned spins = 0; spins < (1u << 18); ++spins) {
    u64 d;
    asm volatile("global_load_dwordx2 %0, %1, %2 sc1\n\ts_waitcnt vmcnt(0)" : "=v"(d) : "v"(off), "s"(ws) : "memory");
    d -= E;
    const bool ok = (need == 0u) || ((unsigned)(d >> 32) == 0u && (unsigned)d >= need && (unsigned)d <= 16u);
    if (__all(ok)) break;
    __builtin_amdgcn_s_sleep(1);
  }
}

__device__ __forceinline__ u64 poll_eq(const char* ws, unsigned off, u64 want, u64 mask, u64 now) {
  u64 miss;
  _Pragma("unroll 1") for (unsigned spins = 0; spins < (1u << 18); ++spins) {
    u64 d;
    asm volatile("global_load_dwordx2 %0, %1, %2 sc1\n\ts_waitcnt vmcnt(0)" : "=v"(d) : "v"(off), "s"(ws) : "memory");
    miss = __ballot(d != want) & mask;
    if (!(miss & now)) break;
    __builtin_amdgcn_s_sleep(1);
  }
  return miss;
}

#define G1_MACROS \
  ".macro G1M rb, j, s, z\n" \
  " .set g1d, 160+24*(\\rb)+4*(\\j)\n .set g1a, 80+40*(\\s)+4*(\\rb)\n .set g1b, 96+40*(\\s)+4*(\\j)\n" \
  " .if (\\j) >= 4\n  .set g1t, g1a\n  .set g1a, g1b\n  .set g1b, g1t\n .endif\n" \
  " .if \\z\n  v_mfma_f32_16x16x32_bf16 v[g1d:g1d+3], v[g1b:g1b+3], v[g1a:g1a+3], 0\n" \
  " .else\n  v_mfma_f32_16x16x32_bf16 v[g1d:g1d+3], v[g1b:g1b+3], v[g1a:g1a+3], v[g1d:g1d+3]\n .endif\n" \
  ".endm\n" \
  ".macro G1RA rb, s, ra, oa\n" \
  " ds_read_b128 v[80+40*(\\s)+4*(\\rb):83+40*(\\s)+4*(\\rb)], \\ra offset:(\\oa)+(((\\rb)*2048)^(4096*(1-%[hh])))\n" \
  ".endm\n" \
  ".macro G1RB k, s, r0, r1, ob\n" \
  " .if (\\k) & 2\n  ds_read_b64_tr_b16 v[96+40*(\\s)+2*(\\k):97+40*(\\s)+2*(\\k)], \\r1 offset:(\\ob)+((\\k)>>2)*2048+((\\k)&1)*512\n" \
  " .else\n  ds_read_b64_tr_b16 v[96+40*(\\s)+2*(\\k):97+40*(\\s)+2*(\\k)], \\r0 offset:(\\ob)+((\\k)>>2)*2048+((\\k)&1)*512\n .endif\n" \
  ".endm\n" \
    \
  ".macro G1BK i, g, r0, r1, ob\n G1RB 4*(\\g)+((\\i)&3), (\\i)>>2, \\r0, \\r1, (\\ob)+((\\i)>>2)*12288\n.endm\n" \
  ".macro G1AK rb, s, a0, a1, oa\n .if \\s\n  G1RA \\rb, 1, \\a1, \\oa\n .else\n  G1RA \\rb, 0, \\a0, \\oa\n .endif\n.endm\n" \
  ".macro G1RDB g, r0, r1, ob\n .set g1i, 0\n .rept 8\n  G1BK g1i, \\g, \\r0, \\r1, \\ob\n  .set g1i, g1i+1\n .endr\n.endm\n" \
  ".macro G1RDA a0, a1, oa\n .set g1i, 0\n .rept 8\n  G1AK (g1i>>1)^2, g1i&1, \\a0, \\a1, \\oa\n  .set g1i, g1i+1\n .endr\n.endm\n" \
    \
  ".macro G1Q g, rb, q, i, z\n G1M \\rb, 2*(\\g)+((((\\i)>>1)^(\\q))&1), (((\\i)+1)>>1)&1, (\\z)&&(((\\i)&1)==0)\n.endm\n" \
    \
  ".macro G1SG g, z, rd, a0, a1, r0, r1, o\n" \
  " .set g1i, 0\n .rept 16\n" \
  "  .if ((\\g) == 0) && (g1i == 8) && (\\rd)\n   s_waitcnt lgkmcnt(7)\n   s_nop 0\n  .endif\n" \
  "  .if ((\\g) == 0) && (g1i == 15)\n   s_waitcnt lgkmcnt(0)\n  .endif\n" \
  "  G1Q \\g, (g1i>>2)^(2-((\\g)&1)), g1i>>2, g1i&3, \\z\n" \
  "  .if \\rd\n" \
  "   .if (\\g) == 0\n    .if g1i < 7\n     G1BK g1i, 2, \\r0, \\r1, \\o\n    .elseif g1i == 8\n     G1BK 7, 2, \\r0, \\r1, \\o\n    .endif\n" \
  "   .elseif (\\g) == 1\n    .if g1i < 8\n     G1BK g1i, 0, \\r0, \\r1, \\o\n    .endif\n" \
  "   .elseif (g1i & 2) == 0\n    G1BK (g1i&1)+2*(g1i>>2), 1, \\r0, \\r1, \\o\n" \
  "   .elseif (\\rd) == 1\n    G1AK (g1i>>2)^2, 1-(g1i&1), \\a0, \\a1, \\o\n   .endif\n" \
  "  .endif\n" \
  "  .set g1i, g1i+1\n .endr\n" \
  ".endm\n" \
    \
    \
  ".macro G1END h, i0, n, l, xs, r0, r1\n" \
  " .set g1i, \\i0\n .rept \\n\n" \
  "  .if (\\l) && (g1i-(\\i0) < 12)\n   .set g1x, ((\\l)>>(4*(g1i-(\\i0))))&15\n" \
  "   .if g1x < 15\n    ds_write_b128 \\xs, v[208+4*g1x:211+4*g1x] offset:(g1x+2*%[hh]*(g1x/6))*1024\n   .endif\n  .endif\n" \
  "  G1Q g1i>>3, 2*(\\h)+(((g1i>>2)^(g1i>>3))&1), g1i>>2, g1i&3, 0\n" \
  "  .if ((\\l) == 0) && (g1i < 8)\n   G1BK g1i, 2, \\r0, \\r1, 0\n  .endif\n" \
  "  .set g1i, g1i+1\n .endr\n" \
  ".endm\n"
#define G1_PURGE \
  ".purgem G1M\n .purgem G1RA\n .purgem G1RB\n .purgem G1BK\n .purgem G1AK\n .purgem G1RDB\n .purgem G1RDA\n .purgem G1Q\n .purgem G1SG\n .purgem G1END\n"
#define G1_CLOBBER \
  "v80", "v81", "v82", "v83", "v84", "v85", "v86", "v87", "v88", "v89", "v90", "v91", "v92", "v93", "v94", "v95", "v96", "v97", "v98", "v99", "v100", \
  "v101", "v102", "v103", "v104", "v105", "v106", "v107", "v108", "v109", "v110", "v111", "v112", "v113", "v114", "v115", "v116", "v117", "v118", \
  "v119", "v120", "v121", "v122", "v123", "v124", "v125", "v126", "v127", "v128", "v129", "v130", "v131", "v132", "v133", "v134", "v135", "v136", \
  "v137", "v138", "v139", "v140", "v141", "v142", "v143", "v144", "v145", "v146", "v147", "v148", "v149", "v150", "v151", "v152", "v153", "v154", \
  "v155", "v156", "v157", "v158", "v159", "v208", "v209", "v210", "v211", "v212", "v213", "v214", "v215", "v216", "v217", "v218", "v219", "v220", \
  "v221", "v222", "v223", "v232", "v233", "v234", "v235", "v236", "v237", "v238", "v239", "v240", "v241", "v242", "v243", "v244", "v245", "v246", "v247"

template <int MODE>
__device__ __forceinline__ void g1_phase(char* smem, char* ws, unsigned& P, const u32x4& nv, u64 nk, u64& E, int w, f32x16& xq, int layer, const u16* __restrict__ x0, const u16* __restrict__ W,
                                         const u16* __restrict__ wo, const u16* __restrict__ wphi,
                                         const u16* __restrict__ wn1, const u16* __restrict__ wn2) {
  constexpr bool HH = (MODE == 1);
  constexpr int ABYTES = 128 * 128;
  constexpr int STAGE = ABYTES + 3 * 8192;
  constexpr int OFF_KS = 147456;
  constexpr int OFF_SP = 3 * STAGE;
  constexpr int OFF_RS = !HH ? OFF_SP : OFF_SP + 16384;
  constexpr int OFF_CS = OFF_RS + 1152;
  constexpr int OFF_WP = OFF_CS + 1536;
  constexpr int OFF_JK = LDS_TOTAL - 1024;
  constexpr int OFF_PK = 0;
  constexpr int OFF_VF = OFF_WP + 4096;
  constexpr int OFF_KV = 16384;
  constexpr int OFF_R2 = 135168;
  const int XS = HH ? 98304 + ((w >> 1) & 1) * 16384 + (w & 1) * 4096 : (w & 3) * 12288;
  constexpr int PSTR = 144;
  if constexpr (HH) asm volatile(".Lmk_g1b:"); else asm volatile(".Lmk_g1a:");
  IDS
  W = uni(W); wphi = uni(wphi); wn1 = uni(wn1); wn2 = uni(wn2);
  const int kg = w >> 2, wm = (w >> 1) & 1, wn = w & 1;
  const bool first = !HH && layer == 0;
  f32x4 acc[2][6];
  u32x4 pw[8];
  ST(layer * 24 + 0)
  const int R0 = wm * 64 + (HH ? kg : kg ^ 1) * 32;
  u32x16 pa;
#define pcs pa[0]
#define prs pa[1]
#define opo pa[2]
#define ogo pa[3]
#define pkw pa[5]
#define hof pa[7]
#define dm pa[8]
#define dm2 pa[9]
#define pcv pa[4]
#define pwf pa[5]
#define prv pa[6]
#define pvf pa[7]
#define ppk pa[10]
#define ppa pa[11]
#define pvr pa[12]
#define gkv pa[13]
#define gkz pa[14]
  f32x4 bias[3];
  const unsigned sb = lds_addr(smem);
  auto prep = [&](int part = 3) {
    if (part & 1) {
    pcs = sb + OFF_CS + wn * 128 + g * 16;
    prs = sb + OFF_RS + (R0 + fr) * 8;
    opo = (unsigned)(wn * 8192 + R0 * 64 + lane * 16);
    ogo = opo + WS_O + layer * (SEQ * DM * 2) + SX + (unsigned)(wn * 8192 + hp * 32768);
    opo += sb;
    pcv = sb + OFF_CS + (wn * 32 + 128 + fr) * 4;
    prv = sb + OFF_R2 + (R0 + 4 * g) * 4;
    if (layer < 2) {
      constexpr unsigned kvn = WS_KV + MODE * KVLB;
      dm = kvn + (unsigned)((rt * 16 + hp) * 16384 + (w >> 2) * 8192 + (tid & 255) * 16);
      dm2 = w >= 4 ? WS_HL + (unsigned)((1 << 20) + ((rt & 7) * 8 + (hp >> 1)) * 16384 + (rt >> 3) * 8192 + (hp & 1) * 4096 + (tid - 256) * 16)
          : tid < 16 ? kvn + (unsigned)((rt * 16 + hp) * 16384 + (tid >> 3) * 8192 + 4096 + (tid & 7) * 16)
          : tid < 18 ? WS_KF + MODE * KFLB + (unsigned)(16 * 16384 + (tid & 1) * 8192 + hp * 256 + rt * 16)
          : tid >= 64 && tid < 128 ? WS_KF + MODE * KFLB + (unsigned)(hp * 16384 + (tid & 32) * 256 + rt * 512 + (tid & 31) * 16) : 0x7fff0000u;
    }
    }
    if (!(part & 2)) {
    } else if constexpr (!HH) {
      pkw = sb + OFF_KS - 4096 + R0 * 128 + wn * 2048 + lane * 16;
      const int rr = R0 ? rt : rt - 1;
      hof = m0 + R0 == 0 ? 0x7fff0000u : WS_HL + (unsigned)(layer * 524288 + ((rr & 7) * 8 + (hp >> 1)) * 16384 + (hp & 1) * 4096 + wn * 2048 + lane * 16) + ((((rr + 1) >> 3 ^ 1) & 1) * 8192 ^ P);
      const unsigned out = ~((0x5555u << 2 << fr) >> (4 * g));
#pragma unroll
      for (int i = 0; i < 8; ++i) {
        unsigned t;
        asm("v_bfe_i32 %0, %1, %2, 1" : "=v"(t) : "v"(out), "n"((i >> 2) * 16 + (i & 3)));
        bias[i >> 2][i & 3] = __uint_as_float(t & 0xff61b1e6u);
      }
      bias[2] = m0 + R0 == 0 ? f32x4{-3.0e38f, -3.0e38f, -3.0e38f, -3.0e38f} : bias[0];
    } else {
      const int u = wm * 2 + kg;
      pwf = sb + OFF_WP + wn * 2048 + (4 * g + q4) * 64 + p4 * 8;
      pvf = sb + OFF_VF + w * 2048 + lane * 16;
      ppk = sb + OFF_PK + w * (32 * PSTR) + fr * PSTR + g * 8;
      ppa = sb + OFF_PK + wn * (32 * PSTR) + (4 * g + q4) * PSTR + u * 32 + p4 * 8;
      pvr = sb + OFF_VF + wn * 2048 + lane * 16;
      const unsigned doff = WS_KV + (layer >> 1) * KVLB + (unsigned)((rt * 16 + hp) * 16384) + (((wn ^ (rt >> 3) ^ 1) & 1) * 8192 ^ P);
      gkv = doff + u * 1024 + lane * 16;
      gkz = fr == 0 ? doff + 4096 + u * 32 + g * 8 : 0x7fff0000u;
    }
    if (part & 2) asm volatile("" : "+{v[48:63]}"(pa), "+{v[36:39]}"(bias[0]), "+{v[40:43]}"(bias[1]), "+{v[44:47]}"(bias[2]));
    else asm volatile("" : "+{v[48:63]}"(pa));
  };

  if (kg == 1) {
    const int lw = w & 3;
    const int kB = 16 * lw + (lane >> 3);
    const int fB = (kB >> 1) & 1;
    const int cB0 = ((((lane & 7) >> 1) ^ fB) << 1) | (lane & 1), cB1 = ((((lane & 7) >> 1) ^ (fB | 2)) << 1) | (lane & 1);

    unsigned vW0, vW1;
    u64 ko2;
    auto run = [&](auto fst) {
      constexpr bool FIRST = decltype(fst)::value;
      if constexpr (!HH) { if constexpr (FIRST) asm volatile(".Lmk_f1:"); else asm volatile(".Lmk_f0:"); }
      constexpr int N = 10;
      const unsigned vB0 = FIRST ? (unsigned)((kB * 3072 + hp * 64 + cB0 * 8) * 2) : nv[0];
      const unsigned vB1 = FIRST ? (unsigned)(((kB + 8) * 3072 + hp * 64 + cB1 * 8) * 2) - 1024u : nv[1];
      const unsigned vA = FIRST ? (unsigned)(((m0 + lw * 32 + (lane >> 3)) * DM + ((lane & 7) ^ (lane >> 3)) * 8) * 2)
                                : nv[2];
      const char* xa = FIRST ? (const char*)x0 : ws + WS_X + (layer - 1) * (SEQ * DM * 2);
      constexpr int KS = FIRST ? 128 : 32768;
      const u64 kord = FIRST ? korder0(rt) : uni2((unsigned)nk, (unsigned)(nk >> 32));
      auto ktile = [&](int ks) { return knib(kord, ks); };
      auto stageA = [&](int ks, int boff) {
        const int kt = ktile(ks);
        const unsigned sA = lds_addr(smem) + boff;
        if (FIRST) glds4v(xa + kt * KS, vA, vA + 15360u, vA + 2 * 15360u, vA + 3 * 15360u, sA + lw * 4096);
        else glds4c(xa + kt * KS, vA, sA + lw * 4096);
      };
      auto stageB = [&](const u16* Wp, int ks, int boff) {
        const int kt = ktile(ks);
        const unsigned sB = lds_addr(smem) + boff + ABYTES;
        const u16* b = Wp + (size_t)kt * 64 * 3072;
        glds4v(b, vB0, vB1, vB0, vB1, sB + lw * 6144);
        glds2v(b + 2048, vB0, vB1, sB + lw * 6144 + 4096);
      };
      bf16x8 sel[3];
      unsigned cso[3], cst;
      if (FIRST) {
        stageA(1, STAGE); stageB(W, 1, STAGE);
        __builtin_amdgcn_sched_barrier(0);
        WAITVN(N);
        BAR;
        asm volatile("" ::: "memory");
        const u32x2 ev = *(const u32x2*)(smem + OFF_E);
        stageA(2, 2 * STAGE); stageB(W, 2, 2 * STAGE);
        __builtin_amdgcn_sched_barrier(0);
        const int fl = (q4 >> 1) | ((g & 1) << 1), j = 3 * wm + (fr < 3 ? fr : 0);
#pragma unroll
        for (int i = 0; i < 3; ++i) {
          const unsigned o = fr == i ? 0x3F803F80u : 0u;
          sel[i] = __builtin_bit_cast(bf16x8, u32x4{o, o, o, o});
          cso[i] = (unsigned)(2 * STAGE + ABYTES + (g >> 1) * 6144 + (g & 1) * 1024 + q4 * 128 + p4 * 8 + (((wn * 2 + ((wm + i) & 1)) ^ fl) << 5) + ((3 * wm + i) >> 1) * 2048);
          asm volatile("" : "+v"(sel[i]), "+v"(cso[i]));
        }
        cst = fr < 3 ? WS_CPA + (unsigned)(((hp * 16 + rt) * 192 + (j >> 1) * 64 + wn * 32 + (j & 1) * 16 + 4 * g) * 4) : 0x7fff0000u;
        asm volatile("" : "+v"(cst));
        E = uni2(ev[0], ev[1]);
      }
      u64 fall, fal2;
      unsigned fao, fa2, vP, vS, vC, vX, dS, dC, dR;
      auto ld_pw = [&](int i0) {
#pragma unroll
        for (int i = i0; i < i0 + 4; ++i) gld16(pw[i], vP, wn1 + i * 8 * 3072);
      };
      auto setup = [&]() {
        fao = (unsigned)((256 + rt * 16 + (lane & 15)) * FLS) ^ P;
        fa2 = (unsigned)(((FIRST ? 512 : 256) + (lane & 15) * 16 + (FIRST ? hp : (hp - ((1 << (lane >> 4)) >> 1)) & 15)) * FLS) ^ P;
        vP = (unsigned)((rt * 64 + (lane >> 3)) * 6144 + lw * 2048 + ((hp + (1 << lw)) & 15) * 128 + (lane & 7) * 16);
        vS = FIRST ? (unsigned)(WS_G0 + (m0 + lw * 32 + (lane & 31)) * 16)
                   : WS_ST + (unsigned)((layer - 1) * (SEQ * 128) + (rt & 7) * 32768 + g * 1024 + lw * 256 + fr * 16) + ((rt >> 3) * 8192 ^ P);
        vC = (FIRST ? WS_CPA : WS_CSP) + (unsigned)(hp * 12288 + g * 768 + lw * 192 + (fr < 12 ? fr : 0) * 16);
        if (!FIRST) vX = HH ? (unsigned)(hp * 4096 + lw * 1024 + lane * 16) : (unsigned)((rt * 16 + hp) * 1024 + lane * 16);
        vW0 = (unsigned)((kB * DM + hp * 64 + ((lane & 7) ^ ((lane >> 4) << 1)) * 8) * 2); vW1 = vW0 + 8 * DM * 2 - 1024u;
        dS = OFF_RS + lw * 256 + (FIRST ? (lane & 31) * 8 : fr * 16 + (g & 1) * 8); dC = OFF_CS + lw * 192 + (fr < 12 ? fr : 0) * 16 + g * 4;
        ko2 = korder(bid & 3, hp);
        dR = OFF_R2 + lw * 128 + (FIRST ? (lane & 31) * 4 : fr * 8 + (g & 1) * 4);
        asm volatile("" : "+v"(dR));
        asm volatile("" : "+v"(vS), "+v"(vX), "+v"(vW0), "+v"(vW1), "+v"(dS));
        asm volatile("" : "+s"(ko2));
        if (FIRST || HH) asm volatile("" : "+v"(vC), "+v"(dC), "+v"(fa2));
        if (FIRST) asm volatile("" : "+v"(vP)); else asm volatile("" : "+v"(fao));
      };
      if (!FIRST) {
        const char* xa1 = xa + ktile(1) * KS;
        const char* xa2 = xa + ktile(2) * KS;
        const u16* b2 = W + (size_t)ktile(2) * 64 * 3072;
        unsigned sA = lds_addr(smem) + STAGE + lw * 4096, sB = lds_addr(smem) + 2 * STAGE + ABYTES + lw * 6144;
        const unsigned fo = nv[3];
        WAITV(0);
        BAR;
        u64 fl1;
        if (lw == 0) {
          __builtin_amdgcn_s_sleep(2);
          asm volatile("global_load_dwordx2 %0, %1, %2 sc1" : "=v"(fl1) : "v"(fo), "s"(ws) : "memory");
        }
        glds4v(b2, vB0, vB1, vB0, vB1, sB);
        glds2v(b2 + 2048, vB0, vB1, sB + 4096);
        asm volatile("" : "+s"(xa1), "+s"(xa2), "+s"(sA));
        setup();
        if (lw == 0) {
          WAITV(6);
          __builtin_amdgcn_sched_barrier(0);
          asm volatile("" : "+v"(fl1));
          const u64 m = __ballot(fo != 0u);
          if (__ballot(fl1 != E + (unsigned)layer) & m) poll_eq(ws, fo, E + (unsigned)layer, m, ~0ull);
        }
        BAR;
        glds4c(xa1, vA, sA); glds4c(xa2, vA, sA + STAGE);
        __builtin_amdgcn_sched_barrier(0);
        prep();
      }
      bf16x8 cf[6];
      f32x4 csum = {0, 0, 0, 0};
      if (FIRST) { fall = E + 1u; vX = (unsigned)(bid * 8192 + lw * 2048 + lane * 16); asm volatile("" : "+v"(fall), "+v"(vX)); }
#pragma unroll
      for (int kt = 0; kt < 13; ++kt) {
        const int cur = (kt % 3) * STAGE;
        if (!FIRST && kt == 0) { WAITV(4); }
        else if (FIRST && kt == 4 && lw == 1) { WAITVN(N + 2); }
        else if ((!FIRST && kt == 1 && lw == 3) || ((FIRST || HH) && kt == 10 && lw == 3) || (FIRST && (kt == 2 || kt == 4 || kt == 5 || (kt == 11 && lw == 0)))) { WAITVN(N + 1); }
        else { WAITVN(N); }
        if (!FIRST && kt == 2 && lw == 3) {
          asm volatile("" : "+v"(fall));
          if (__any(fall != E + (unsigned)layer)) poll_flags(ws, P, E, 1, rt, hp, lane, layer, 0);
        }
        if (HH && kt == 11 && lw == 3) {
          asm volatile("" : "+v"(fal2));
          if (__any(fal2 != E + (unsigned)layer)) {
            _Pragma("unroll 1") for (int j = 0; j < 4; ++j) poll_flags(ws, P, E, 1, rt, (hp - ((1 << j) >> 1)) & 15, lane, layer, 2);
          }
        }
        if (FIRST && kt == 11 && lw == 3) {
          asm volatile("" : "+v"(fal2));
          if (__any(fal2 != E + 1u)) poll_flags(ws, P, E, 2, rt, hp, lane, 1u, 2);
        }
        BAR;
        if (FIRST && kt == 1) {
          asm volatile("" ::: "memory");
#pragma unroll
          for (int i = 0; i < 6; ++i) { const char* t = smem + cso[i >> 1] + (i & 1) * 12288; cf[i] = cat8(tr_read(t), tr_read(t + 512)); }
        }
        if (FIRST && kt == 2 && lw == 2 && bid < 64 && (bid & 7) == ((bid >> 3) & 3)) {
          u64 t0, t1; unsigned d;
          asm volatile("s_memtime %0\n\ts_load_dword %2, %3, 0x0\n\ts_waitcnt lgkmcnt(0)\n\ts_memtime %1\n\ts_waitcnt lgkmcnt(0)"
                       : "=&s"(t0), "=&s"(t1), "=&s"(d) : "s"(ws + WS_PL + (bid >> 5) * 8192 + (bid & 31) * 256) : "memory");
          d = (unsigned)(t1 - t0);
          const u64 v = ((E + 1u) << 16) | (d < 65535u ? d : 65535u);
          __builtin_amdgcn_raw_buffer_store_b64(__builtin_bit_cast(u32x2, v), rsW, lane == 0 ? WS_PR + (unsigned)(bid & 56) : 0x7fff0000u, 0, 16);
        }
        if (FIRST && kt == 12 && lw == 2) asm volatile("global_load_dwordx2 %0, %1, %2 sc1" : "=v"(fal2) : "v"((lane & 7) * 8), "s"(ws + WS_PR) : "memory");
        if (FIRST && kt >= 2 && kt < 6 && lw == kt - 2) setup();
        if (FIRST && kt >= 6 && kt < 10 && (lw >> 1) == ((kt - 6) >> 1)) prep(1 + (kt & 1));
        stageA(kt + 3, cur);
        if (FIRST && kt == 1) {
          asm volatile("" : "+v"(sel[0]));
#pragma unroll
          for (int i = 0; i < 6; ++i) csum = MFMA(cf[i], sel[i >> 1], csum);
          asm volatile("" : "+v"(csum));
        }
        stageB(W, kt + 3, cur);
        if (FIRST) {
          if (kt == 1) __builtin_amdgcn_raw_buffer_store_b128(__builtin_bit_cast(u32x4, csum), rsW, cst, 0, 16);
          if (kt == 3 && tid == 320) __builtin_amdgcn_raw_buffer_store_b64(__builtin_bit_cast(u32x2, fall), rsW, (WS_FLAGS + (unsigned)(512 + rt * 16 + hp) * FLS) ^ P, 0, 16);
          if (kt == 3 || kt == 4) glds16s(wo + (kt - 3) * 512, vX, lds_addr(smem) + OFF_JK);
        }
        if constexpr (!HH) if (FIRST && kt == 10 && lw == 0) {
          CODE_TOUCH(1, ".Lmk_ep0", ".Lmk_end")
          asm volatile("s_mov_b32 m0, %2\n\ts_nop 0\n\tglobal_load_lds_dword %0, %1" :: "v"(v), "s"(pc), "s"(lds_addr(smem) + OFF_JK) : "memory", "m0");
        }
        if (!FIRST && kt == 0 && lw == 3) asm volatile("global_load_dwordx2 %0, %1, %2 sc1" : "=v"(fall) : "v"(fao), "s"(ws + WS_FLAGS) : "memory");
        if ((FIRST || HH) && kt == 9 && lw == 3) asm volatile("global_load_dwordx2 %0, %1, %2 sc1" : "=v"(fal2) : "v"(fa2), "s"(ws + WS_FLAGS) : "memory");
      }
      if (FIRST && lw < 3) { ld_pw(0); WAITVN(N + 4); } else { WAITVN(N); }
      BAR;
      u32x4 sv[4], cv[4];
      auto ld_c = [&](u32x4& d, unsigned v, int so) { asm volatile("buffer_load_dwordx4 %0, %1, %2, %3 offen sc1" : "=v"(d) : "v"(v), "s"(rsW), "s"(so) : "memory"); };
      auto wo_tile = [&](int t) { glds2v(wo + (size_t)knib(ko2, t) * 64 * DM, vW0, vW1, lds_addr(smem) + OFF_WO + t * 8192 + lw * 2048); };
#pragma unroll
      for (int i = 0; i < (FIRST ? 1 : 4); ++i) ld_c(sv[i], vS, (i & 1) * 4096 + (i >> 1) * 16384);
      if (FIRST || HH) {
#pragma unroll
        for (int i = 0; i < 4; ++i) ld_c(cv[i], vC, i * 3072);
      }
      if (HH) glds16s(wphi, vX, lds_addr(smem) + OFF_WP + lw * 1024);
      else if (!FIRST) glds16c(ws + WS_CSA, vX, lds_addr(smem) + OFF_CS);
      wo_tile(0); wo_tile(1);
      if (HH || (FIRST && lw < 3)) { WAITV(13); } else { WAITV(9); }
      BAR;
      wo_tile(2); wo_tile(3); wo_tile(4); wo_tile(5);
      if (FIRST) {
        glds16s(wn2, vX, lds_addr(smem) + OFF_JK); glds16s(wn2 + 512, vX, lds_addr(smem) + OFF_JK);
        WAITV(14);
        if (lw < 3) ld_pw(4);
      } else {
        WAITV(12);
      }
      auto fin = [&]() {
        if (FIRST) {
          const unsigned tag = (unsigned)E + 1u;
          asm volatile("" : "+v"(sv[0]));
          _Pragma("unroll 1") for (unsigned spins = 0; spins < (1u << 18); ++spins) {
            if (__all(sv[0][1] == tag && sv[0][3] == tag)) break;
            __builtin_amdgcn_s_sleep(1);
            asm volatile("buffer_load_dwordx4 %0, %1, %2, 0 offen sc1\n\ts_waitcnt vmcnt(0)" : "=v"(sv[0]) : "v"(vS), "s"(rsW) : "memory");
          }
          *(u32x2*)(smem + dS) = u32x2{sv[0][0], sv[0][2]};
          { unsigned* p = (unsigned*)(smem + dR); p[0] = sv[0][0]; p[128] = sv[0][2]; }
        } else {
          asm volatile("" : "+v"(sv[0]), "+v"(sv[1]), "+v"(sv[2]), "+v"(sv[3]));
          const f32x4 t = (__builtin_bit_cast(f32x4, sv[0]) + __builtin_bit_cast(f32x4, sv[1])) + (__builtin_bit_cast(f32x4, sv[2]) + __builtin_bit_cast(f32x4, sv[3]));
          const unsigned u = xs4(t[0], t[2], t[1], t[3]);
          auto h = __builtin_amdgcn_permlane32_swap(u, u, false, false);
          const float mean = __uint_as_float(h[0]) * (1.0f / 1024.0f);
          const float var = fmaxf(__uint_as_float(h[1]) * (1.0f / 1024.0f) - mean * mean, 0.f);
          const float rs = __builtin_amdgcn_rsqf(var + 1e-5f);
          *(float2*)(smem + dS) = make_float2(-mean, rs);
          { float* p = (float*)(smem + dR); p[0] = -mean; p[128] = rs; }
        }
      };
      auto cfin = [&]() {
        if (FIRST || HH) {
          asm volatile("" : "+v"(cv[0]), "+v"(cv[1]), "+v"(cv[2]), "+v"(cv[3]));
          const f32x4 t = (__builtin_bit_cast(f32x4, cv[0]) + __builtin_bit_cast(f32x4, cv[1])) + (__builtin_bit_cast(f32x4, cv[2]) + __builtin_bit_cast(f32x4, cv[3]));
          *(unsigned*)(smem + dC) = xs4(t[0], t[1], t[2], t[3]);
        }
      };
      cfin();
      if (FIRST && lw == 2) {
        asm volatile("" : "+v"(fal2));
        _Pragma("unroll 1") for (unsigned spins = 0; spins < (1u << 18); ++spins) {
          if (__all(((fal2 ^ ((E + 1u) << 16)) >> 16) == 0)) break;
          __builtin_amdgcn_s_sleep(1);
          asm volatile("global_load_dwordx2 %0, %1, %2 sc1\n\ts_waitcnt vmcnt(0)" : "=v"(fal2) : "v"((lane & 7) * 8), "s"(ws + WS_PR) : "memory");
        }
        unsigned d = (unsigned)fal2 & 0xffffu;
        d = min(d, (unsigned)__builtin_amdgcn_update_dpp(0u, d, 0xb1, 0xf, 0xf, false));
        d = min(d, (unsigned)__builtin_amdgcn_update_dpp(0u, d, 0x4e, 0xf, 0xf, false));
        *(unsigned*)(smem + OFF_E + 256) = __builtin_amdgcn_readlane(d, 4) < __builtin_amdgcn_readlane(d, 0) ? 8192u : 0u;
      }
      if (!HH) BAR;
      fin();
      WAITL0;
    };
    typedef std::integral_constant<bool, true> T_;
    typedef std::integral_constant<bool, false> F_;
    __builtin_amdgcn_s_setprio(3);
    if (first) run(T_{}); else run(F_{});
    __builtin_amdgcn_s_setprio(0);
    BAR;
    if constexpr (!HH) asm volatile(".Lmk_ep0:");
    {
      const char* xr = smem + XS + lane * 16;
#pragma unroll
      for (int i = 0; i < 2; ++i)
#pragma unroll
        for (int j = HH ? 2 : 0; j < (HH ? 4 : 6); ++j) acc[i][j] = *(const f32x4*)(xr + (i * (HH ? 8 : 6) + j) * 1024);
      if (HH) asm volatile("" : "=v"(acc[0][0]), "=v"(acc[0][1]), "=v"(acc[1][0]), "=v"(acc[1][1]));
    }
    asm volatile("" : "={v[64:79]}"(xq));
  } else {
    prep(1);
    if (!first) publish(rsW, tid, 256 + rt * 16 + hp, E + (unsigned)layer, P);
    prep(2);
    const int fl = (q4 >> 1) | ((g & 1) << 1);
    const unsigned bo = lds_addr(smem) + ABYTES + (g >> 1) * 6144 + (g & 1) * 1024 + q4 * 128 + p4 * 8;
    const unsigned bo0 = bo + (((wn * 2 + 0) ^ fl) << 5), bo1 = bo + (((wn * 2 + 1) ^ fl) << 5);
    const unsigned ar = lds_addr(smem) + (wm * 64 + fr) * 128;
    const unsigned aK0 = ar + ((g ^ (fr & 7)) << 4), aK1 = ar + (((4 + g) ^ (fr & 7)) << 4);
    if (first) {
      const int row = m0 + hp * 8 + w * 2 + (lane >> 5);
      static_assert(HH || (OFF_SP == 122880 && OFF_KS + 12288 <= OFF_JK), "addresses used at the top of the kernel");
      asm volatile("" :: "v"(aK0), "v"(aK1), "v"(bo0), "v"(bo1));
      unsigned go = (lane & 31) == 0 ? (unsigned)(WS_G0 + row * 16) : 0x7fff0000u;
      unsigned ho = WS_HL + (unsigned)(((rt & 7) * 8 + (hp >> 1)) * 16384 + (hp & 1) * 4096 + tid * 16 + (rt >> 3) * 8192);
      asm volatile("" : "+v"(go), "+v"(ho));
      WAITV(0);
      u32x4 xs[4];
      const u32x2 ev = *(const u32x2*)(smem + OFF_E);
#pragma unroll
      for (int i = 0; i < 4; ++i) xs[i] = *(const u32x4*)(smem + OFF_SP + w * 4096 + (lane >> 5) * 2048 + (lane & 31) * 16 + i * 512);
      typedef __attribute__((ext_vector_type(2))) __bf16 bf2;
      const bf2 one2 = __builtin_bit_cast(bf2, 0x3F803F80u);
      float sa[2] = {zero1(), zero1()}, sq[2] = {zero1(), zero1()};
#pragma unroll
      for (int i = 0; i < 16; ++i) {
        const unsigned u = xs[i >> 2][i & 3];
        const bf2 t = __builtin_bit_cast(bf2, u);
        sa[i & 1] = __builtin_amdgcn_fdot2_f32_bf16(t, one2, sa[i & 1], false);
        sq[i & 1] = __builtin_amdgcn_fdot2_f32_bf16(t, t, sq[i & 1], false);
      }
      float sm = r16_sum(sa[0] + sa[1]), m2 = r16_sum(sq[0] + sq[1]);
      {
        auto r = __builtin_amdgcn_permlane16_swap(__float_as_uint(sm), __float_as_uint(sm), false, false);
        auto q = __builtin_amdgcn_permlane16_swap(__float_as_uint(m2), __float_as_uint(m2), false, false);
        sm = __uint_as_float(r[0]) + __uint_as_float(r[1]); m2 = __uint_as_float(q[0]) + __uint_as_float(q[1]);
      }
      const float mean = sm * (1.0f / 1024.0f);
      m2 = fmaxf(m2 - sm * mean, 0.f);
      const unsigned tag = ev[0] + 1u;
      u32x4 gw = {__float_as_uint(-mean), tag, __float_as_uint(__builtin_amdgcn_rsqf(m2 * (1.0f / 1024.0f) + 1e-5f)), tag};
      __builtin_amdgcn_raw_buffer_store_b128(gw, rsW, go, 0, 16);
      __builtin_amdgcn_raw_buffer_store_b128(empty4(), rsW, ho, 0, 16);
      if (bid < 64 && tid == 0) __builtin_amdgcn_raw_buffer_store_b32(tag, rsW, WS_PL + (unsigned)((bid >> 5) * 8192 + (bid & 31) * 256), 0, 16);
      E = uni2(ev[0], ev[1]);
    }
    f32x32 ac0;
    f32x16 ac1;
    f32x8 av[2];
    asm volatile("" :: "v"(aK0), "v"(aK1), "v"(bo0), "v"(bo1));
    if (first) BAR;
#define G1_OUT
#define G1_T(a)
#define G1_TEND
#define G1_TCL
    asm volatile(G1_MACROS
        ".p2align 3\n"
        "G1RDB 0, %[b0], %[b1], 0\n G1RDA %[a0], %[a1], 0\n G1RDB 1, %[b0], %[b1], 0\n"
        "ds_read_b128 v[64:67], %[xa]\n ds_read_b128 v[68:71], %[xa] offset:2048\n"
        "ds_read_b128 v[72:75], %[xa] offset:4096\n ds_read_b128 v[76:79], %[xa] offset:6144\n"
        "s_cmp_eq_u32 %[lay], 0\n s_waitcnt lgkmcnt(12)\n"
        "G1SG 0, 1, 1, %[a0], %[a1], %[b0], %[b1], 0\n"
        "s_cbranch_scc1 .Lg1p%=\n s_barrier\n.Lg1p%=:\n"
        "s_mov_b32 vcc_lo, 16\n"
        "G1SG 1, 1, 1, %[a0], %[a1], %[b0], %[b1], 40960\n"
        "G1SG 2, 1, 2, %[a0], %[a1], %[b0], %[b1], 40960\n"
        "s_cbranch_scc1 .Lg1a%=\n"
        "s_nop 0\n"
        G1_T(88)
        "s_barrier\n"
        G1_T(92)
        "G1RDA %[a0], %[a1], 40960\n"
        "s_waitcnt lgkmcnt(4)\n"
        "s_branch .Lg1b%=\n"
        ".Lg1a%=:\n"
        G1_T(88)
        "s_barrier\n"
        G1_T(92)
        "G1RDB 0, %[b0], %[b1], 40960\n G1RDA %[a0], %[a1], 40960\n G1RDB 1, %[b0], %[b1], 40960\n"
        ".p2align 3\n s_nop 0\n"
        ".Lg1b%=:\n"
        "s_waitcnt lgkmcnt(8)\n"
        "G1SG 0, 0, 1, %[a0], %[a1], %[b0], %[b1], 40960\n"
        "s_barrier\n"
        "G1SG 1, 0, 1, %[a2], %[a3], %[b2], %[b3], 0\n"
        "G1SG 2, 0, 1, %[a2], %[a3], %[b2], %[b3], 0\n"
        "s_lshr_b32 vcc_lo, vcc_lo, 1\n s_waitcnt vmcnt(0) lgkmcnt(8)\n"
        "G1SG 0, 0, 1, %[a2], %[a3], %[b2], %[b3], 0\n"
        "s_barrier\n"
        "G1SG 1, 0, 1, %[a0], %[a1], %[b0], %[b1], 0\n"
        "G1SG 2, 0, 1, %[a0], %[a1], %[b0], %[b1], 0\n"
        "s_cbranch_scc0 .Lg1c%=\n s_waitcnt lgkmcnt(8)\n"
        "G1SG 0, 0, 1, %[a0], %[a1], %[b0], %[b1], 0\n"
        "s_barrier\n"
        "G1SG 1, 0, 1, %[a0], %[a1], %[b0], %[b1], 40960\n"
        "G1SG 2, 0, 1, %[a0], %[a1], %[b0], %[b1], 40960\n"
        "s_branch .Lg1b%=\n"
        ".Lg1c%=:\n"
        "s_waitcnt lgkmcnt(8)\n"
        "G1END 1, 0, 8, 0, %[xs], %[b0], %[b1]\n"
        "s_waitcnt lgkmcnt(8)\n s_nop 0\n"
        "G1END 1, 8, 8, 0, %[xs], %[b0], %[b1]\n"
        "s_waitcnt lgkmcnt(0)\n s_nop 0\n"
        "G1END 1, 16, 8, 0, %[xs], %[b0], %[b1]\n"
        ".if %[hh] == 0\n s_nop 0\n s_barrier\n .endif\n"
        G1_T(96)
        ".if %[hh]\n G1END 0, 0, 20, 0xffff76103298, %[xs], %[b0], %[b1]\n .else\n G1END 0, 0, 18, 0xba5498327610, %[xs], %[b0], %[b1]\n .endif\n"
        "s_waitcnt lgkmcnt(0)\n s_barrier\n"
        ".if %[hh]\n G1END 0, 20, 24-20, 0xffffffffffff, %[xs], %[b0], %[b1]\n .else\n G1END 0, 18, 24-18, 0xffffffffffff, %[xs], %[b0], %[b1]\n .endif\n"
        "s_nop 7\n"
        G1_TEND G1_PURGE
        : "=&{v[160:191]}"(ac0), "=&{v[192:207]}"(ac1), "=&{v[64:79]}"(xq), "=&{v[224:231]}"(av[0]), "=&{v[248:255]}"(av[1]),
          "+{v[48:63]}"(pa), "+{v[36:39]}"(bias[0]), "+{v[40:43]}"(bias[1]), "+{v[44:47]}"(bias[2]) G1_OUT
        : [a0] "v"(aK0), [a1] "v"(aK1), [a2] "v"(aK0 + 2 * STAGE), [a3] "v"(aK1 + 2 * STAGE), [b0] "v"(bo0), [b1] "v"(bo1),
          [b2] "v"(bo0 + 2 * STAGE), [b3] "v"(bo1 + 2 * STAGE), [xs] "v"(lds_addr(smem) + XS + lane * 16), [xa] "v"(wn ? aK1 : aK0),
          [lay] "s"(layer), [hh] "n"(MODE)
        : "memory", "scc", "vcc", G1_TCL G1_CLOBBER);
    acc[0][0] = quad<0>(ac0); acc[0][1] = quad<1>(ac0); acc[0][2] = quad<2>(ac0); acc[0][3] = quad<3>(ac0); acc[0][4] = quad<4>(ac0); acc[0][5] = quad<5>(ac0);
    acc[1][0] = quad<6>(ac0); acc[1][1] = quad<7>(ac0); acc[1][2] = quad<0>(ac1); acc[1][3] = quad<1>(ac1); acc[1][4] = quad<2>(ac1); acc[1][5] = quad<3>(ac1);
    if constexpr (HH) {
      const float cs[2] = {lds<float>(pcv), lds<float>(pcv + 64)};
      f32x4 rm[4], rr[4];
#pragma unroll
      for (int rb = 0; rb < 4; ++rb) { rm[rb] = lds<f32x4>(prv + rb * 64); rr[rb] = lds<f32x4>(prv + 512 + rb * 64); }
#pragma unroll
      for (int h = 0; h < 2; ++h) {
        f32x2 f[8];
#pragma unroll
        for (int i = 0; i < 8; ++i) {
          const int c = i >> 2, rb = (i >> 1) & 1, e = (i & 1) * 2;
          const f32x4 a = h == 0 ? acc[rb][4 + c] : c ? quad<1>(av[rb]) : quad<0>(av[rb]);
          f[i] = __builtin_elementwise_fma(f32x2{rm[2 * h + rb][e], rm[2 * h + rb][e + 1]}, f32x2{cs[c], cs[c]}, f32x2{a[e], a[e + 1]});
        }
        asm volatile("" : "+v"(f[0]), "+v"(f[1]), "+v"(f[2]), "+v"(f[3]), "+v"(f[4]), "+v"(f[5]), "+v"(f[6]), "+v"(f[7]));
#pragma unroll
        for (int i = 0; i < 8; ++i) f[i] *= f32x2{rr[2 * h + ((i >> 1) & 1)][(i & 1) * 2], rr[2 * h + ((i >> 1) & 1)][(i & 1) * 2 + 1]};
        asm volatile("" : "+v"(f[0]), "+v"(f[1]), "+v"(f[2]), "+v"(f[3]), "+v"(f[4]), "+v"(f[5]), "+v"(f[6]), "+v"(f[7]));
#pragma unroll
        for (int c = 0; c < 2; ++c) lds<u32x4>(pvf + h * 8192 + c * 1024) = u32x4{pk2(f[4 * c][0], f[4 * c][1]), pk2(f[4 * c + 1][0], f[4 * c + 1][1]), pk2(f[4 * c + 2][0], f[4 * c + 2][1]), pk2(f[4 * c + 3][0], f[4 * c + 3][1])};
      }
    }
  }
  ST(layer * 24 + 4)
  {
  const int lane = lane_id(), tid = w * 64 + lane, fr = lane & 15, g = lane >> 4;
  (void)fr; (void)g;
  auto o_put = [&](int rb, const f32x4& a, const f32x4& b) {
    const u32x4 v = {pk2(a[0], a[1]), pk2(a[2], a[3]), pk2(b[0], b[1]), pk2(b[2], b[3])};
    __builtin_amdgcn_raw_buffer_store_b128(v, rsW, ogo + rb * 1024, 0, 16);
    return v;
  };
  auto marks = [&]() {
    const u32x4 pz = empty4();
    __builtin_amdgcn_raw_buffer_store_b128(pz, rsW, dm, 0, 16);
    if (w < 2 || (!HH && w >= 4)) __builtin_amdgcn_raw_buffer_store_b128(pz, rsW, dm2, 0, 16);
  };
  if (first) { P = __builtin_amdgcn_readfirstlane(*(const unsigned*)(smem + OFF_E + 256)); ogo ^= P; hof ^= P; }
  f32x4 csq[2], csk[2], rm[2], rr[2];
  float csv[2];
  f32x2 st2[2] = {lds<f32x2>(prs), lds<f32x2>(prs + 128)};
#pragma unroll
  for (int c = 0; c < 2; ++c) {
    csk[c] = lds<f32x4>(pcs + 256 + c * 64);
    if (!HH) { csv[c] = lds<float>(pcv + c * 64); rm[1 - c] = lds<f32x4>(prv + (1 - c) * 64); rr[1 - c] = lds<f32x4>(prv + 512 + (1 - c) * 64); }
  }
#pragma unroll
  for (int c = 0; c < 2; ++c) csq[c] = lds<f32x4>(pcs + c * 64);
  if constexpr (!HH) {
    auto fold = [&](const f32x4& a, const f32x4& cs, const f32x2& st, bool scale) {
      f32x2 lo = __builtin_elementwise_fma(f32x2{cs[0], cs[1]}, f32x2{st[0], st[0]}, f32x2{a[0], a[1]});
      f32x2 hi = __builtin_elementwise_fma(f32x2{cs[2], cs[3]}, f32x2{st[0], st[0]}, f32x2{a[2], a[3]});
      if (scale) { lo *= f32x2{st[1], st[1]}; hi *= f32x2{st[1], st[1]}; }
      return u32x2{pk2(lo[0], lo[1]), pk2(hi[0], hi[1])};
    };
    auto cat2 = [](u32x2 a, u32x2 b) { return __builtin_bit_cast(bf16x8, u32x4{a[0], a[1], b[0], b[1]}); };
    auto foldv = [&](int rb, int c) {
      const f32x4& a = acc[rb][4 + c];
      const f32x2 lo = __builtin_elementwise_fma(f32x2{rm[rb][0], rm[rb][1]}, f32x2{csv[c], csv[c]}, f32x2{a[0], a[1]}) * f32x2{rr[rb][0], rr[rb][1]};
      const f32x2 hi = __builtin_elementwise_fma(f32x2{rm[rb][2], rm[rb][3]}, f32x2{csv[c], csv[c]}, f32x2{a[2], a[3]}) * f32x2{rr[rb][2], rr[rb][3]};
      return u32x2{pk2(lo[0], lo[1]), pk2(hi[0], hi[1])};
    };
    bf16x8 qf[2], kf[2];
    u32x2 vt[3][2];
#pragma unroll
    for (int rb = 1; rb >= 0; --rb) {
      kf[rb] = cat2(fold(acc[rb][2], csk[0], st2[rb], true), fold(acc[rb][3], csk[1], st2[rb], true));
      const u32x2 va0 = vt[rb + 1][0] = foldv(rb, 0), va1 = vt[rb + 1][1] = foldv(rb, 1);
      if (rb == 1) {
        if (R0 == 96) {
          u32x4 hk = __builtin_bit_cast(u32x4, kf[1]), hv = {va0[0], va0[1], va1[0], va1[1]};
#pragma unroll
          for (int e = 0; e < 4; ++e) { hk[e] = hk[e] < EMPTY - 1u ? hk[e] : EMPTY - 1u; hv[e] = hv[e] < EMPTY - 1u ? hv[e] : EMPTY - 1u; }
          __builtin_amdgcn_raw_buffer_store_b128(hk, rsW, hof, 0, 16);
          __builtin_amdgcn_raw_buffer_store_b128(hv, rsW, hof + 1024, 0, 16);
        } else {
          lds<bf16x8>(pkw + 4096) = kf[1];
          lds<u32x2>(pkw + 5120) = va0; lds<u32x2>(pkw + 5128) = va1;
        }
        asm volatile("" : "+v"(st2[0]), "+v"(st2[1]), "+v"(csv[0]), "+v"(csv[1]));
      }
    }
#pragma unroll
    for (int rb = 0; rb < 2; ++rb) qf[rb] = cat2(fold(acc[rb][0], csq[0], st2[rb], false), fold(acc[rb][1], csq[1], st2[rb], false));
    f32x4 sc[2][2];
    sc[1][0] = MFMA(kf[0], qf[1], bias[0]);
    sc[1][1] = MFMA(kf[1], qf[1], bias[1]);
    sc[0][1] = MFMA(kf[0], qf[0], bias[1]);
    constexpr float SCL = 0.17677669529663687f * 1.4426950408889634f;
    const bf16x8 ones = ones8();
    auto attn = [&](int rb) {
      const f32x4 z4 = {0, 0, 0, 0};
      const float scl = st2[rb][1] * SCL;
      asm volatile("s_nop 7" : "+v"(sc[rb][0]), "+v"(sc[rb][1]));
      unsigned m = __float_as_uint(vmax3f_n(vmax3f(sc[rb][0][0], sc[rb][0][1], sc[rb][0][2]), vmax3f(sc[rb][0][3], sc[rb][1][0], sc[rb][1][1]), vmaxf(sc[rb][1][2], sc[rb][1][3])));
      auto r = __builtin_amdgcn_permlane16_swap(m, m, false, false);
      m = __float_as_uint(vmaxf_n(__uint_as_float(r[0]), __uint_as_float(r[1])));
      auto r2 = __builtin_amdgcn_permlane32_swap(m, m, false, false);
      const float mxs = -vmaxf(__uint_as_float(r2[0]), __uint_as_float(r2[1])) * scl;
      u32x4 pv;
#pragma unroll
      for (int i = 0; i < 4; ++i) {
        const f32x2 t = __builtin_elementwise_fma(f32x2{sc[rb][i >> 1][(i & 1) * 2], sc[rb][i >> 1][(i & 1) * 2 + 1]}, f32x2{scl, scl}, f32x2{mxs, mxs});
        pv[i] = pk2(__builtin_amdgcn_exp2f(t[0]), __builtin_amdgcn_exp2f(t[1]));
      }
      const bf16x8 pf = __builtin_bit_cast(bf16x8, pv);
      const f32x4 den = MFMA(ones, pf, z4);
      const f32x4 o0 = MFMA(cat2(vt[rb][0], vt[rb + 1][0]), pf, z4), o1 = MFMA(cat2(vt[rb][1], vt[rb + 1][1]), pf, z4);
      const float inv = __builtin_amdgcn_rcpf(den[0]);
      return o_put(rb, o0 * inv, o1 * inv);
    };
    u32x4 hk, hv;
    auto ld_h = [&]() {
      asm volatile("s_nop 7\n\tbuffer_load_dwordx4 %0, %2, %3, 0 offen sc1\n\tbuffer_load_dwordx4 %1, %2, %3, 0 offen offset:1024 sc1" : "=&v"(hk), "=&v"(hv) : "v"(hof), "s"(rsW) : "memory");
    };
    asm volatile("" : "+v"(sc[0][1]));
    if (R0 == 0) ld_h();
    const u32x4 ov = attn(1);
    if (R0 == 0) {
      WAITL0; BAR;
      WAITV(1);
      _Pragma("unroll 1") for (unsigned spins = 0; spins < (1u << 18); ++spins) {
        __builtin_amdgcn_sched_barrier(0); asm volatile("" : "+v"(hk), "+v"(hv));
        unsigned mxw = 0;
#pragma unroll
        for (int e = 0; e < 4; ++e) { mxw = hk[e] > mxw ? hk[e] : mxw; mxw = hv[e] > mxw ? hv[e] : mxw; }
        if (__all(mxw != EMPTY)) break;
        ld_h();
        WAITV(0);
      }
      asm volatile("" : "+v"(hk), "+v"(hv));
      if (first) marks();
    } else {
      if (first) marks();
      if (first && tid >= 128 && tid < 176) __builtin_amdgcn_raw_buffer_store_b128(*(const u32x4*)(smem + OFF_CS + (tid - 128) * 16), rsW, WS_CSA + (unsigned)((rt * 16 + hp) * 1024 + (tid - 128) * 16), 0, 16);
      WAITL0; BAR;
      asm volatile("" ::: "memory");
      hk = lds<u32x4>(pkw); hv = lds<u32x4>(pkw + 1024);
    }
    ST(layer * 24 + 5)
    asm volatile("" ::: "memory");
    vt[0][0] = u32x2{hv[0], hv[1]}; vt[0][1] = u32x2{hv[2], hv[3]};
    sc[0][0] = MFMA(__builtin_bit_cast(bf16x8, hk), qf[0], bias[2]);
    lds<u32x4>(opo + 1024) = ov;
    lds<u32x4>(opo) = attn(0);
  } else {
    bf16x8 wf[2];
#pragma unroll
    for (int fb = 0; fb < 2; ++fb) wf[fb] = cat8(tr_read(pwf + fb * 32), tr_read(pwf + fb * 32 + 16 * 64));
    __builtin_amdgcn_sched_barrier(0);
    if (layer == 1) marks();
    const f32x4 z4 = {0, 0, 0, 0};
    constexpr float L2E = 1.4426950408889634f;
    const unsigned kvo = WS_KV + (layer >> 1) * KVLB, kfo = WS_KF + (layer >> 1) * KFLB + hp * 16384, kzo = WS_KF + (layer >> 1) * KFLB + 16 * 16384 + hp * 256;
    bf16x8 qp[2][2];
    auto phi2 = [&](auto qktag) {
      constexpr int QK = decltype(qktag)::value & 1;
      constexpr int RB0 = (decltype(qktag)::value >> 1) & 1, RB1 = (decltype(qktag)::value & 4) ? RB0 + 1 : 2;
      float pv[2][8];
      f32x4 pm[2][2];
      f32x2 f[2][4];
#pragma unroll
      for (int rb = RB0; rb < RB1; ++rb) {
        asm volatile("" : "+v"(st2[rb]));
        const f32x2 nm = {st2[rb][0], st2[rb][0]};
#pragma unroll
        for (int i = 0; i < 4; ++i) {
          const f32x4& a = acc[rb][2 * QK + (i >> 1)];
          const f32x4& cs = QK ? csk[i >> 1] : csq[i >> 1];
          f[rb][i] = __builtin_elementwise_fma(f32x2{cs[(i & 1) * 2], cs[(i & 1) * 2 + 1]}, nm, f32x2{a[(i & 1) * 2], a[(i & 1) * 2 + 1]});
        }
      }
#pragma unroll
      for (int rb = RB0; rb < RB1; ++rb) asm volatile("" : "+v"(f[rb][0]), "+v"(f[rb][1]), "+v"(f[rb][2]), "+v"(f[rb][3]));
#pragma unroll
      for (int rb = RB0; rb < RB1; ++rb) {
        const bf16x8 xf = __builtin_bit_cast(bf16x8, u32x4{pk2(f[rb][0][0], f[rb][0][1]), pk2(f[rb][1][0], f[rb][1][1]), pk2(f[rb][2][0], f[rb][2][1]), pk2(f[rb][3][0], f[rb][3][1])});
        pm[rb][0] = MFMA(wf[0], xf, z4); pm[rb][1] = MFMA(wf[1], xf, z4);
      }
      if (QK) asm volatile("s_nop 7" : "+v"(pm[0][0]), "+v"(pm[0][1]), "+v"(pm[1][0]), "+v"(pm[1][1]));
      else asm volatile("s_nop 7" : "+v"(pm[RB0][0]), "+v"(pm[RB0][1]));
#pragma unroll
      for (int rb = RB0; rb < RB1; ++rb)
#pragma unroll
        for (int i = 0; i < 4; ++i) { pv[rb][i] = pm[rb][0][i]; pv[rb][4 + i] = pm[rb][1][i]; }
      float mx[2], mn[2];
#pragma unroll
      for (int u = RB0; u < RB1; ++u) max8pn(pm[u][0], pm[u][1], mx[u], mn[u]);
      if (RB1 - RB0 == 2) xr4<true>(mx[0], mn[0], mx[1], mn[1]); else xr2<true>(mx[RB0], mn[RB0]);
      float ep[2][8], en[2][8], sp[2], sn[2];
#pragma unroll
      for (int u = RB0; u < RB1; ++u) {
        const float c = st2[u][1] * L2E, mxs = -mx[u] * c, mns = -mn[u] * c;
#pragma unroll
        for (int i = 0; i < 4; ++i) {
          const f32x2 p2 = {pv[u][2 * i], pv[u][2 * i + 1]};
          const f32x2 tp = __builtin_elementwise_fma(p2, f32x2{c, c}, f32x2{mxs, mxs});
          const f32x2 tn = __builtin_elementwise_fma(-p2, f32x2{c, c}, f32x2{mns, mns});
          ep[u][2 * i] = __builtin_amdgcn_exp2f(tp[0]); ep[u][2 * i + 1] = __builtin_amdgcn_exp2f(tp[1]);
          en[u][2 * i] = __builtin_amdgcn_exp2f(tn[0]); en[u][2 * i + 1] = __builtin_amdgcn_exp2f(tn[1]);
        }
        const f32x2 a2 = (f32x2{ep[u][0], ep[u][1]} + f32x2{ep[u][2], ep[u][3]}) + (f32x2{ep[u][4], ep[u][5]} + f32x2{ep[u][6], ep[u][7]});
        const f32x2 b2 = (f32x2{en[u][0], en[u][1]} + f32x2{en[u][2], en[u][3]}) + (f32x2{en[u][4], en[u][5]} + f32x2{en[u][6], en[u][7]});
        sp[u] = a2[0] + a2[1]; sn[u] = b2[0] + b2[1];
      }
      if (RB1 - RB0 == 2) xr4<false>(sp[0], sn[0], sp[1], sn[1]); else xr2<false>(sp[RB0], sn[RB0]);
#pragma unroll
      for (int rb = RB0; rb < RB1; ++rb) {
        float ip = __builtin_amdgcn_rcpf(sp[rb]), in = __builtin_amdgcn_rcpf(sn[rb]);
        u32x2 o0, o1, o2, o3;
        o0[0] = pk2(ep[rb][0] * ip, ep[rb][1] * ip); o0[1] = pk2(ep[rb][2] * ip, ep[rb][3] * ip);
        o1[0] = pk2(ep[rb][4] * ip, ep[rb][5] * ip); o1[1] = pk2(ep[rb][6] * ip, ep[rb][7] * ip);
        o2[0] = pk2(en[rb][0] * in, en[rb][1] * in); o2[1] = pk2(en[rb][2] * in, en[rb][3] * in);
        o3[0] = pk2(en[rb][4] * in, en[rb][5] * in); o3[1] = pk2(en[rb][6] * in, en[rb][7] * in);
        if (QK == 0) {
          qp[rb][0] = __builtin_bit_cast(bf16x8, (u32x4){o0[0], o0[1], o1[0], o1[1]});
          qp[rb][1] = __builtin_bit_cast(bf16x8, (u32x4){o2[0], o2[1], o3[0], o3[1]});
          asm volatile("" : "+v"(qp[rb][0]), "+v"(qp[rb][1]));
        } else {
          const unsigned dst = ppk + rb * 16 * PSTR;
          lds<u32x2>(dst) = o0; lds<u32x2>(dst + 32) = o1; lds<u32x2>(dst + 64) = o2; lds<u32x2>(dst + 96) = o3;
        }
      }
    };
    phi2(std::integral_constant<int, 1>{});
    bf16x8 one8 = ones8();
    asm volatile("" : "+v"(one8));
    WAITL0; BAR;
    ST(layer * 24 + 5)
    {
      bf16x8 af[4], v0[4], v1[4];
      asm volatile("" ::: "memory");
#pragma unroll
      for (int sw = 0; sw < 4; ++sw) {
        af[sw] = cat8(tr_read(ppa + sw * (64 * PSTR)), tr_read(ppa + sw * (64 * PSTR) + 16 * PSTR));
        v0[sw] = lds<bf16x8>(pvr + sw * 4096);
        v1[sw] = lds<bf16x8>(pvr + sw * 4096 + 1024);
      }
      __builtin_amdgcn_sched_barrier(0);
      f32x4 kv[3] = {z4, z4, z4};
#pragma unroll
      for (int sw = 0; sw < 4; ++sw) {
        kv[0] = MFMA(af[sw], v0[sw], kv[0]);
        kv[1] = MFMA(af[sw], v1[sw], kv[1]);
        kv[2] = MFMA(af[sw], one8, kv[2]);
      }
      auto pc = [](float x, float y) { const unsigned t = pk2(x, y); return t < EMPTY - 1u ? t : EMPTY - 1u; };
      __builtin_amdgcn_raw_buffer_store_b128(u32x4{pc(kv[0][0], kv[0][1]), pc(kv[0][2], kv[0][3]), pc(kv[1][0], kv[1][1]), pc(kv[1][2], kv[1][3])}, rsW, gkv, 0, 16);
      __builtin_amdgcn_raw_buffer_store_b64(u32x2{pc(kv[2][0], kv[2][1]), pc(kv[2][2], kv[2][3])}, rsW, gkz, 0, 16);
      __builtin_amdgcn_sched_barrier(0);
      if (kg) {
#pragma unroll
        for (int i = 0; i < 4; ++i) acc[i >> 1][i & 1] = lds<f32x4>(sb + XS + lane * 16 + ((i >> 1) * 8 + (i & 1)) * 1024);
      }
    }
    ST(layer * 24 + 6)
    u32x4 l1[8];
    const bool red = w == 7 || (w == 6 && (rt & 7) == 0);
    unsigned lo = kvo + (unsigned)((hp + (lane >> 5) * 128) * 16384) + ((((rt >> 3) ^ (lane >> 5) ^ 1) & 1) * 8192 ^ P) +
                  (w == 7 ? (unsigned)((rt & 4) * 512 + (g & 1) * 1024 + (rt & 3) * 256 + fr * 16) : (unsigned)(4096 + (lane & 7) * 16));
    unsigned fs = w == 7 ? kfo + (unsigned)((rt >> 2) * 2048 + (lane >> 5) * 1024 + (rt & 3) * 256 + fr * 16 + (g & 1) * 8)
                : (lane & 24) ? 0x7fff0000u : kzo + (unsigned)((rt >> 3) * 128 + (lane & 4) * 16 + (lane & 1) * 32 + (lane >> 5) * 16 + (lane & 2) * 4);
    auto l1_load = [&]() {
      asm volatile("" ::: "memory");
#pragma unroll
      for (int p = 0; p < 8; ++p) l1[p] = __builtin_amdgcn_raw_buffer_load_b128(rsW, lo, p * (16 * 16384), 16);
      __builtin_amdgcn_sched_barrier(0);
    };
    if (red) asm volatile("" : "+v"(lo), "+v"(fs));
    BAR;
    if (red) {
      typedef __attribute__((ext_vector_type(2))) __bf16 bf2;
      unsigned c0 = 0x00003F80u, c1 = 0x3F800000u;
      asm volatile("" : "+v"(c0), "+v"(c1));
      __builtin_amdgcn_s_setprio(3);
      __builtin_amdgcn_s_sleep(5);
      _Pragma("unroll 1") for (unsigned spins = 0; spins < (1u << 18); ++spins) {
        l1_load();
        unsigned mxw = 0;
#pragma unroll
        for (int p = 0; p < 8; ++p)
#pragma unroll
          for (int e = 0; e < 4; ++e) mxw = l1[p][e] > mxw ? l1[p][e] : mxw;
        if (__all(mxw != EMPTY)) break;
      }
      float s4[4];
      float a[8] = {zero1(), zero1(), zero1(), zero1(), zero1(), zero1(), zero1(), zero1()};
#pragma unroll
      for (int p = 0; p < 8; ++p)
#pragma unroll
        for (int e = 0; e < 4; ++e) {
          const unsigned u = l1[p][e];
          a[2 * e] = __builtin_amdgcn_fdot2_f32_bf16(__builtin_bit_cast(bf2, u), __builtin_bit_cast(bf2, c0), a[2 * e], false);
          a[2 * e + 1] = __builtin_amdgcn_fdot2_f32_bf16(__builtin_bit_cast(bf2, u), __builtin_bit_cast(bf2, c1), a[2 * e + 1], false);
        }
#pragma unroll
      for (int i = 0; i < 4; ++i) {
        auto r = __builtin_amdgcn_permlane32_swap(__float_as_uint(a[i]), __float_as_uint(a[i + 4]), false, false);
        s4[i] = __uint_as_float(r[0]) + __uint_as_float(r[1]);
      }
      unsigned o0 = pk2(s4[0], s4[1]), o1 = pk2(s4[2], s4[3]);
      o0 = o0 < EMPTY - 1u ? o0 : EMPTY - 1u; o1 = o1 < EMPTY - 1u ? o1 : EMPTY - 1u;
      __builtin_amdgcn_raw_buffer_store_b64(u32x2{o0, o1}, rsW, fs, 0, 16);
      __builtin_amdgcn_raw_buffer_store_b64(u32x2{o0, o1}, rsW, fs, 8192, 16);
    }
    phi2(std::integral_constant<int, 4>{});
    ST(layer * 24 + 7)
    phi2(std::integral_constant<int, 6>{});
    __builtin_amdgcn_s_setprio(0);
    ST(layer * 24 + 8)
    ST(layer * 24 + 9)
    {
      unsigned pfo = OFF_KV + wn * 4096 + lane * 16, pzo = OFF_KV + 8192 + wn * 128 + g * 16;
      asm volatile("" : "+v"(pfo), "+v"(pzo));
      if (w < 3) {
        u32x4 fa[3];
        unsigned fo = kfo + (unsigned)(w * 3072 + lane * 16) + ((rt >> 3) * 8192 ^ P);
        unsigned fo2 = w < 2 ? fo + 2048 : kzo + (unsigned)((lane & 15) * 16) + ((rt >> 3) * 8192 ^ P);
        unsigned lo1 = sb + OFF_KV + w * 3072 + lane * 16, lo2 = w < 2 ? lo1 + 2048 : sb + OFF_KV + 8192 + (lane & 15) * 16;
        __builtin_amdgcn_s_sleep(8);
        _Pragma("unroll 1") for (unsigned spins = 0; spins < (1u << 18); ++spins) {
          asm volatile("" ::: "memory");
          fa[0] = __builtin_amdgcn_raw_buffer_load_b128(rsW, fo, 0, 16);
          fa[1] = __builtin_amdgcn_raw_buffer_load_b128(rsW, fo + 1024, 0, 16);
          fa[2] = __builtin_amdgcn_raw_buffer_load_b128(rsW, fo2, 0, 16);
          unsigned mxw = 0;
#pragma unroll
          for (int e = 0; e < 12; ++e) mxw = fa[e >> 2][e & 3] > mxw ? fa[e >> 2][e & 3] : mxw;
          if (__all(mxw != EMPTY)) break;
          __builtin_amdgcn_s_sleep(1);
        }
        lds<u32x4>(lo1) = fa[0]; lds<u32x4>(lo1 + 1024) = fa[1]; lds<u32x4>(lo2) = fa[2];
      }
      ST(layer * 24 + 10)
      WAITL0; BAR;
      ST(layer * 24 + 11)
      bf16x8 kvf[3][2];
#pragma unroll
      for (int ks = 0; ks < 2; ++ks) {
        kvf[0][ks] = *(const bf16x8*)(smem + pfo + ks * 2048);
        kvf[1][ks] = *(const bf16x8*)(smem + pfo + ks * 2048 + 1024);
        kvf[2][ks] = *(const bf16x8*)(smem + pzo + ks * 64);
      }
#pragma unroll
      for (int rb = 0; rb < 2; ++rb) {
        f32x4 oa[3];
#pragma unroll
        for (int nb = 2; nb >= 0; --nb) {
          oa[nb] = MFMA(kvf[nb][0], qp[rb][0], z4);
          oa[nb] = MFMA(kvf[nb][1], qp[rb][1], oa[nb]);
        }
        float den = oa[2][0];
        den += 1e-6f;
        float inv = __builtin_amdgcn_rcpf(den);
        lds<u32x4>(opo + rb * 1024) = o_put(rb, oa[0] * inv, oa[1] * inv);
      }
    }
  }
  ST(layer * 24 + 12)
  if (w >= 6) {
    const unsigned v0 = (unsigned)((((w & 1) * 32 + (lane >> 3)) * DM + hp * 64 + ((lane & 7) ^ ((lane >> 4) << 1)) * 8) * 2);
    const u64 ko = korder(bid & 3, hp);
#pragma unroll
    for (int t = 6; t < 12; ++t) glds4v(wo + (size_t)knib(ko, t) * 64 * DM, v0, v0 + 15360u, v0 + 2 * 15360u, v0 + 3 * 15360u, sb + OFF_WO + t * 8192 + (w & 1) * 4096);
    WAITV(24); WAITL0; BAR;
  } else if (w >= 4) { WAITV(0); WAITL0; BAR; }
  if (first && w >= 4 && w < 7) {
    typedef __attribute__((ext_vector_type(2))) __bf16 bf2;
    unsigned c0 = 0x00003F80u, c1 = 0x3F800000u;
    asm volatile("" : "+v"(c0), "+v"(c1));
    const bf2 lo1 = __builtin_bit_cast(bf2, c0), hi1 = __builtin_bit_cast(bf2, c1);
    float a[8] = {zero1(), zero1(), zero1(), zero1(), zero1(), zero1(), zero1(), zero1()};
#pragma unroll
    for (int i = 0; i < 8; ++i) {
      asm volatile("" : "+v"(pw[i]));
#pragma unroll
      for (int e = 0; e < 4; ++e) {
        const unsigned u = pw[i][e];
        a[2 * e] = __builtin_amdgcn_fdot2_f32_bf16(__builtin_bit_cast(bf2, u), lo1, a[2 * e], false);
        a[2 * e + 1] = __builtin_amdgcn_fdot2_f32_bf16(__builtin_bit_cast(bf2, u), hi1, a[2 * e + 1], false);
      }
    }
#pragma unroll
    for (int i = 0; i < 4; ++i) {
      auto r = __builtin_amdgcn_permlane32_swap(__float_as_uint(a[i]), __float_as_uint(a[i + 4]), false, false);
      a[i] = __uint_as_float(r[0]) + __uint_as_float(r[1]);
    }
#pragma unroll
    for (int i = 0; i < 2; ++i) {
      auto r = __builtin_amdgcn_permlane16_swap(__float_as_uint(a[i]), __float_as_uint(a[i + 2]), false, false);
      a[i] = __uint_as_float(r[0]) + __uint_as_float(r[1]);
      a[i] += dpp_ror<8>(a[i]);
    }
    if (!(lane & 8))
      __builtin_amdgcn_raw_buffer_store_b64(u32x2{__float_as_uint(a[0]), __float_as_uint(a[1])}, rsW,
          (unsigned)(WS_CSP + ((((hp + (1 << (w - 4))) & 15) * 16 + rt) * 192 + (w - 4) * 64 + (lane & 7) * 8 + (lane >> 5) * 4 + ((lane >> 4) & 1) * 2) * 4), 0, 16);
  }
  }
}

#undef pcs
#undef prs
#undef opo
#undef ogo
#undef pkw
#undef hof
#undef dm
#undef dm2
#undef pcv
#undef pwf
#undef prv
#undef pvf
#undef ppk
#undef ppa
#undef pvr
#undef gkv
#undef gkz

__device__ __forceinline__ void g2_phase(char* smem, char* ws, unsigned P, u32x4& nv, u64& nk, u64 E, int w, f32x16& xq, int layer, const u16* __restrict__ x0,
                                         const u16* __restrict__ wo, const u16* __restrict__ wnx, u16* __restrict__ out) {
  IDS
  wnx = uni(wnx);
  const unsigned sb = lds_addr(smem);
  const int lw = w & 3, k2 = 16 * lw + (lane >> 3);
  const unsigned vW0 = (unsigned)((k2 * DM + hp * 64 + ((lane & 7) ^ ((lane >> 4) << 1)) * 8) * 2), vW1 = vW0 + 8 * DM * 2 - 1024u;
  const int r0 = w * 16 + (lane >> 3);
  unsigned pxr = sb + r0 * 128 + (lane & 7) * 16;
  const unsigned rmo = (unsigned)(((m0 + r0) * DM + hp * 64 + ((lane & 7) ^ (lane >> 3)) * 8) * 2);
  const unsigned imo = SX + (unsigned)(hp * 32768 + (w >> 2) * 8192 + r0 * 128 + (lane & 7) * 16);
  constexpr int S0 = 0, S1 = 16384, S2 = 32768, S3 = 147456, S4 = OFF_WO, S5 = 98304, S6 = 114688;
  constexpr int AS[16] = {S0, S1, S2, S3, S0, S1, S4, S2, S3, S0, S1, S4, S5, S3, S0, S6};
  constexpr int OFF_C = 0;
  auto bs = [](int t) { return OFF_WO + (t < 12 ? t : t - 10) * 8192; };
  if (w >= 4) {
    constexpr int WT[15] = {0, 8, 12, 14, 16, 18, 20, 18, 16, 14, 12, 12, 10, 10, 8};
    const char* o = ws + WS_O + layer * (SEQ * DM * 2) + SX;
    const unsigned vA = (unsigned)((lw >> 1) * 16384 + (lw & 1) * 4096 + lane * 16);
    const u64 kord = korder(bid & 3, hp);
    auto ldA = [&](int s) { glds4c(o + knib(kord, s) * 32768, vA, sb + AS[s] + lw * 4096); };
    auto ldW = [&](int t) { glds2v(wo + (size_t)knib(kord, t) * 64 * DM, vW0, vW1, sb + bs(t) + lw * 2048); };
    const int fB = (k2 >> 1) & 1;
    unsigned vB0 = (unsigned)((k2 * 3072 + hp * 64 + (((((lane & 7) >> 1) ^ fB) << 1) | (lane & 1)) * 8) * 2);
    unsigned vB1 = (unsigned)(((k2 + 8) * 3072 + hp * 64 + (((((lane & 7) >> 1) ^ (fB | 2)) << 1) | (lane & 1)) * 8) * 2) - 1024u;
    nk = korder(kv1(bid), hp);
    const int kn1 = knib(nk, 1);
    nv = u32x4{vB0, vB1, SX + vA, lane < 16 && ((kfirst(nk) >> lane) & 1) ? (WS_FLAGS + (unsigned)(256 + rt * 16 + lane) * FLS) ^ P : 0u};
    asm volatile("" : "+v"(nv));
    asm volatile("" : "+s"(nk));
    vB0 = nv[0]; vB1 = nv[1];
    auto ldB = [&](int s, int part) {
      const u16* b = wnx + (size_t)(s ? kn1 : hp) * 64 * 3072;
      if (part) glds4v(b, vB0, vB1, vB0, vB1, sb + s * 40960 + 16384 + lw * 6144);
      else glds2v(b + 2048, vB0, vB1, sb + s * 40960 + 16384 + lw * 6144 + 4096);
    };
    BAR;
    ldA(1); ldA(2); ldA(3); ldA(4);
    WAITV(12);
    BAR;
    auto step = [&](auto ktc) {
      constexpr int kt = decltype(ktc)::value;
      WAITVN(WT[kt]);
      BAR;
      if constexpr (kt == 1) { ldA(5); ldA(6); }
      if constexpr (kt >= 2 && kt <= 10) ldA(kt + 5);
      if constexpr (kt >= 2 && kt <= 5) ldW(kt + 10);
      if constexpr (kt >= 11) ldB((kt - 11) >> 1, (kt - 11) & 1);
      if constexpr (kt >= 8) { __builtin_amdgcn_s_sleep(kt >= 11 ? 2 : 1); if (layer == 1) __builtin_amdgcn_s_sleep(1); }
    };
    step(IC<1>{}); step(IC<2>{}); step(IC<3>{}); step(IC<4>{}); step(IC<5>{}); step(IC<6>{}); step(IC<7>{});
    step(IC<8>{}); step(IC<9>{}); step(IC<10>{}); step(IC<11>{}); step(IC<12>{}); step(IC<13>{}); step(IC<14>{});
  } else {
    const int wm = w >> 1, wn = w & 1;
    f32x4 acc[4][2];
    unsigned co[2];
#pragma unroll
    for (int cb = 0; cb < 2; ++cb) co[cb] = sb + OFF_C + (unsigned)((wm * 64 + fr) * 128 + (((wn * 4 + cb * 2 + (g >> 1)) ^ (fr & 7)) << 4) + (g & 1) * 8);
    const int fl = (q4 >> 1) | ((g & 1) << 1);
    const int bb0 = (4 * g + q4) * 128 + p4 * 8 + (((wn * 2) ^ fl) << 5), bb1 = (4 * g + q4) * 128 + p4 * 8 + (((wn * 2 + 1) ^ fl) << 5);
    const int abase = wm * 4096 + lane * 16;
    bf16x8 fa[2][2][4], fb[2][2][2];
    unsigned ra[3], rb0[2], rb1[2];
#pragma unroll
    for (int i = 0; i < 3; ++i) { ra[i] = abase + i * 65536; asm volatile("" : "+v"(ra[i])); }
#pragma unroll
    for (int i = 0; i < 2; ++i) { rb0[i] = bb0 + OFF_WO + i * 49152; rb1[i] = bb1 + OFF_WO + i * 49152; asm volatile("" : "+v"(rb0[i]), "+v"(rb1[i])); }
    auto loadf = [&](int kt, bf16x8 (&a)[2][4], bf16x8 (&b)[2][2]) {
#pragma unroll
      for (int s = 0; s < 2; ++s) {
        const char* sA = smem + ra[AS[kt] >> 16] + (AS[kt] & 65535) + s * 8192;
        const int hi = bs(kt) >= OFF_WO + 49152, ob = bs(kt) - OFF_WO - hi * 49152 + s * 4096;
        b[s][0] = cat8(tr_read(smem + rb0[hi] + ob), tr_read(smem + rb0[hi] + ob + 2048));
        b[s][1] = cat8(tr_read(smem + rb1[hi] + ob), tr_read(smem + rb1[hi] + ob + 2048));
#pragma unroll
        for (int rb = 0; rb < 4; ++rb) a[s][rb] = *(const bf16x8*)(sA + rb * 1024);
      }
    };
    auto mma = [&](bf16x8 (&a)[2][4], bf16x8 (&b)[2][2]) {
#pragma unroll
      for (int i = 0; i < 16; ++i) {
        const int rb = i >> 2, cb = ((i >> 1) ^ rb) & 1, s = ((i + 1) >> 1) & 1;
        acc[rb][cb] = MFMA(b[s][cb], a[s][rb], acc[rb][cb]);
      }
    };
    unsigned fo = lane < 16 && lane != hp ? (WS_FLAGS + (unsigned)(rt * 16 + lane) * FLS) ^ P : 0u;
    const u64 ko = korder(bid & 3, hp);
    unsigned kf = kfirst(ko), kf2 = kfirst(ko >> 16) | (1u << knib(ko, 9));
    unsigned jo = (unsigned)((((rt & 7) * 128 + (w - 1) * 64 + lane) * 3072 + (hp & 3) * 1024 + (hp | 3) * 64) * 2);
    unsigned fo1 = ((kf >> (lane & 31)) & 1) ? fo : 0u, fo2 = ((kf2 >> (lane & 31)) & 1) ? fo : 0u;
    fo = (((kf | kf2) >> (lane & 31)) & 1) ? 0u : fo;
    asm volatile("" : "+v"(fo), "+v"(fo1), "+v"(fo2));
    asm volatile("" : "+s"(kf), "+s"(kf2));
    publish(rsW, tid, rt * 16 + hp, E + (unsigned)(layer + 1), P);
    ST(layer * 24 + 13)
    u64 fl1 = 0;
    if (w == 0) asm volatile("global_load_dwordx2 %0, %1, %2 sc1" : "+v"(fl1) : "v"(fo1), "s"(ws) : "memory");
    if (layer == 0) {
      const unsigned xo = (unsigned)(((m0 + wm * 64 + fr) * DM + hp * 64 + wn * 32 + g * 8) * 2);
      asm volatile("global_load_dwordx4 v[64:67], %1, %3\n\tglobal_load_dwordx4 v[68:71], %1, %4\n\tglobal_load_dwordx4 v[72:75], %2, %3\n\tglobal_load_dwordx4 v[76:79], %2, %4"
                   : "+{v[64:79]}"(xq) : "v"(xo), "v"(xo + 32 * DM * 2), "s"(x0), "s"(x0 + 16 * DM) : "memory");
    }
    loadf(0, fa[0], fb[0]);
    unsigned junk = lane;
    if (layer < 3 && (hp & 7) < 3 && (w == 1 || w == 2))
      asm volatile("global_load_dword %0, %1, %2" : "+v"(junk) : "v"(jo), "s"(wnx) : "memory");
    if (layer == 0 && w == 3) {
      CODE_TOUCH(2, ".Lmk_g1b", ".Lmk_g1a")
      asm volatile("global_load_dword %0, %1, %2" : "+v"(junk) : "v"(v), "s"(pc) : "memory");
    }
    if (layer == 1 && w == 3) {
      CODE_TOUCH(3, ".Lmk_f0", ".Lmk_f1")
      asm volatile("global_load_dword %0, %1, %2" : "+v"(junk) : "v"(v), "s"(pc) : "memory");
    }
    __builtin_amdgcn_sched_barrier(0);
    bf16x8 id[2];
#pragma unroll
    for (int cb = 0; cb < 2; ++cb) {
      const unsigned one = (g >> 1) == cb && (g & 1) == (fr >> 3) ? 0x3F80u << ((fr & 1) * 16) : 0u;
      u32x4 t;
#pragma unroll
      for (int j = 0; j < 4; ++j) t[j] = ((fr >> 1) & 3) == j ? one : 0u;
      id[cb] = __builtin_bit_cast(bf16x8, t);
      asm volatile("" : "+v"(id[cb]));
    }
    asm volatile("" : "+v"(co[0]), "+v"(co[1]));
#pragma unroll
    for (int rb = 0; rb < 4; ++rb) { acc[rb][0] = f32x4{0, 0, 0, 0}; acc[rb][1] = f32x4{0, 0, 0, 0}; }
    mma(fa[0], fb[0]);
    __builtin_amdgcn_sched_barrier(0);
    WAITL0;
    u64 miss = 0;
    if (w == 0) { ARRIVED("+v"(fl1)); if (__ballot(fl1 != E + (unsigned)(layer + 1)) & kf) poll_eq(ws, fo1, E + (unsigned)(layer + 1), kf, kf); miss = 0xffffu & ~(kf | kf2 | (1u << hp)); }
    ST(layer * 24 + 14)
    __builtin_amdgcn_sched_barrier(0);
    BAR;
    __builtin_amdgcn_sched_barrier(0);
    asm volatile("" ::: "memory");
    if (layer == 0) WAITV(0);
    __builtin_amdgcn_sched_barrier(0);
    asm volatile("" : "+{v[64:79]}"(xq));
    const bf16x8 xf[4] = {__builtin_bit_cast(bf16x8, quad<0>(xq)), __builtin_bit_cast(bf16x8, quad<1>(xq)), __builtin_bit_cast(bf16x8, quad<2>(xq)), __builtin_bit_cast(bf16x8, quad<3>(xq))};
#pragma unroll
    for (int rb = 0; rb < 4; ++rb) { acc[rb][0] = MFMA(id[0], xf[rb], acc[rb][0]); acc[rb][1] = MFMA(id[1], xf[rb], acc[rb][1]); }
    if (w == 0) poll_eq(ws, fo2, E + (unsigned)(layer + 1), kf2, kf2);
    u64 fl2 = 0;
    auto step = [&](auto ktc) {
      constexpr int kt = decltype(ktc)::value;
      __builtin_amdgcn_sched_barrier(0);
      if (kt == 5 && miss) {
        ARRIVED("+v"(fl2));
        if (__ballot(fl2 != E + (unsigned)(layer + 1)) & miss) poll_eq(ws, fo, E + (unsigned)(layer + 1), miss, ~0ull);
      }
      WAITL0;
      BAR;
      if (kt == 2 && miss) asm volatile("global_load_dwordx2 %0, %1, %2 sc1" : "+v"(fl2) : "v"(fo), "s"(ws) : "memory");
      if (kt == 0) { ST(layer * 24 + 15) }
      __builtin_amdgcn_sched_barrier(0);
      loadf(kt + 1, fa[(kt + 1) & 1], fb[(kt + 1) & 1]);
      if (kt == 0) return;
      mma(fa[kt & 1], fb[kt & 1]);
#pragma unroll
      for (int i = 0; i < 4; ++i) {
        __builtin_amdgcn_sched_group_barrier(0x008, 1, 0);
        __builtin_amdgcn_sched_group_barrier(0x100, 1, 0);
        __builtin_amdgcn_sched_group_barrier(0x008, 1, 0);
        __builtin_amdgcn_sched_group_barrier(0x100, 3, 0);
      }
#pragma unroll
      for (int i = 0; i < 8; ++i) __builtin_amdgcn_sched_group_barrier(0x008, 1, 0);
    };
    step(IC<0>{}); step(IC<1>{}); step(IC<2>{}); step(IC<3>{}); step(IC<4>{}); step(IC<5>{}); step(IC<6>{}); step(IC<7>{});
    step(IC<8>{}); step(IC<9>{}); step(IC<10>{}); step(IC<11>{}); step(IC<12>{}); step(IC<13>{}); step(IC<14>{});
    WAITV(0);
    asm volatile("" :: "v"(junk));
    __builtin_amdgcn_sched_barrier(0);
    auto put = [&](int rb) {
#pragma unroll
      for (int cb = 0; cb < 2; ++cb) lds<u32x2>(co[cb] + rb * 2048) = u32x2{pk2(acc[rb][cb][0], acc[rb][cb][1]), pk2(acc[rb][cb][2], acc[rb][cb][3])};
    };
#pragma unroll
    for (int rb = 0; rb < 4; ++rb)
#pragma unroll
      for (int cb = 0; cb < 2; ++cb) acc[rb][cb] = MFMA(fb[1][0][cb], fa[1][0][rb], acc[rb][cb]);
#pragma unroll
    for (int rb = 0; rb < 4; ++rb) {
      __builtin_amdgcn_sched_barrier(0);
#pragma unroll
      for (int cb = 0; cb < 2; ++cb) acc[rb][cb] = MFMA(fb[1][1][cb], fa[1][1][rb], acc[rb][cb]);
      __builtin_amdgcn_sched_barrier(0);
      if (rb >= 2) put(rb - 2);
    }
    __builtin_amdgcn_sched_barrier(0);
    ST(layer * 24 + 16)
    put(2); put(3);
  }
  WAITL0; BAR;
  asm volatile("" ::: "memory");
  u32x4 xn[2];
#pragma unroll
  for (int i = 0; i < 2; ++i) xn[i] = lds<u32x4>(pxr + OFF_C + i * 1024);
  if (layer == 3) {
    const __amdgpu_buffer_rsrc_t rsO = __builtin_amdgcn_make_buffer_rsrc((void*)uni(out), (short)0, SEQ * DM * 2, 0x00020000);
    __builtin_amdgcn_raw_buffer_store_b128(xn[0], rsO, rmo, 0, 16);
    __builtin_amdgcn_raw_buffer_store_b128(xn[1], rsO, rmo + 8 * DM * 2, 0, 16);
  } else {
    __builtin_amdgcn_raw_buffer_store_b128(xn[0], rsW, WS_X + layer * (SEQ * DM * 2) + imo, 0, 16);
    __builtin_amdgcn_raw_buffer_store_b128(xn[1], rsW, WS_X + layer * (SEQ * DM * 2) + imo + 1024, 0, 16);
    float mean[2], m2[2];
#pragma unroll
    for (int i = 0; i < 2; ++i) {
      typedef __attribute__((ext_vector_type(2))) __bf16 bf2;
      const bf2 one2 = __builtin_bit_cast(bf2, 0x3F803F80u);
      float sa = zero1(), sq = zero1();
#pragma unroll
      for (int e = 0; e < 4; ++e) {
        const unsigned u = xn[i][e];
        const bf2 t = __builtin_bit_cast(bf2, u);
        sa = __builtin_amdgcn_fdot2_f32_bf16(t, one2, sa, false);
        sq = __builtin_amdgcn_fdot2_f32_bf16(t, t, sq, false);
      }
      mean[i] = sa;
      m2[i] = sq;
    }
#define DPA(c) "v_add_f32_dpp %0, %0, %0 " c "\n\tv_add_f32_dpp %1, %1, %1 " c "\n\tv_add_f32_dpp %2, %2, %2 " c "\n\tv_add_f32_dpp %3, %3, %3 " c "\n\t"
    asm volatile("s_nop 3\n\t" DPA("quad_perm:[1,0,3,2] row_mask:0xf bank_mask:0xf") DPA("quad_perm:[2,3,0,1] row_mask:0xf bank_mask:0xf")
                 DPA("row_half_mirror row_mask:0xf bank_mask:0xf") "s_nop 0" : "+v"(mean[0]), "+v"(m2[0]), "+v"(mean[1]), "+v"(m2[1]));
    if ((lane & 7) < 2) {
      const int i = lane & 1;
      const u32x2 sv = {__float_as_uint(i ? mean[1] : mean[0]), __float_as_uint(i ? m2[1] : m2[0])};
      __builtin_amdgcn_raw_buffer_store_b64(sv, rsW, WS_ST + layer * (SEQ * 128) + (unsigned)((rt & 7) * 32768 + (hp >> 3) * 16384 + (hp & 7) * 1024 + (r0 + i * 8) * 8) + ((rt >> 3) * 8192 ^ P), 0, 16);
    }
  }
  ST(layer * 24 + 17)
  ST(layer * 24 + 18)
}

__global__ __launch_bounds__(512, 2) void mega_kernel(const u16* __restrict__ x0, const u16* __restrict__ wqkvA,
                                                      const u16* __restrict__ woA, const u16* __restrict__ wqkvB,
                                                      const u16* __restrict__ wphi, const u16* __restrict__ woB,
                                                      u16* __restrict__ out, char* __restrict__ ws) {
  unsigned P = 0;
  extern __shared__ __attribute__((aligned(16))) char smem[];
  const int w = __builtin_amdgcn_readfirstlane(threadIdx.x >> 6);
  u64 E = 0;
  {
    const int bid = blockIdx.x, lane = threadIdx.x & 63;
    const int rt = 2 * (bid & 4) + ((bid >> 3) & 7), hp = 4 * (bid & 3) + (bid >> 6), m0 = rt * 128;
    if (w < 4) {
      if (w == 3) {
        unsigned long long pc;
        unsigned len;
        asm volatile("s_getpc_b64 %0\n.Lmk_pc:\n\ts_mov_b32 %1, .Lmk_end-.Lmk_pc" : "=s"(pc), "=s"(len));
        const unsigned per = ((len >> 10) + 1) * 128, off = ((bid >> 3) & 7) * per + (lane * 128 < per ? lane * 128 : 0);
        asm volatile("s_mov_b32 m0, %2\n\ts_nop 0\n\tglobal_load_lds_dword %0, %1" :: "v"(off < len - 4 ? off : 0u), "s"(pc), "s"(lds_addr(smem) + LDS_TOTAL - 1024) : "memory", "m0");
      }
      const unsigned xo = (unsigned)((m0 + hp * 8 + w * 2) * (DM * 2) + lane * 16);
      glds4v(x0, xo, xo, xo, xo, lds_addr(smem) + 122880 + w * 4096);
      asm volatile("s_mov_b32 m0, %2\n\ts_nop 0\n\tglobal_load_lds_dword %0, %1" :: "v"(lane * 4), "s"(ws), "s"(lds_addr(smem) + OFF_E) : "memory", "m0");
    } else {
      const int lw = w & 3;
      const int kB = 16 * lw + (lane >> 3), fB = (kB >> 1) & 1;
      const unsigned vB0 = (unsigned)((kB * 3072 + hp * 64 + (((((lane & 7) >> 1) ^ fB) << 1) | (lane & 1)) * 8) * 2);
      const unsigned vB1 = (unsigned)(((kB + 8) * 3072 + hp * 64 + (((((lane & 7) >> 1) ^ (fB | 2)) << 1) | (lane & 1)) * 8) * 2) - 1024u;
      const unsigned vA = (unsigned)(((m0 + lw * 32 + (lane >> 3)) * DM + ((lane & 7) ^ (lane >> 3)) * 8) * 2);
#pragma unroll
      for (int s = 0; s < 1; ++s) {
        const int kt = (rt & 8) + s;
        const unsigned sA = lds_addr(smem) + s * 40960, sB = sA + 16384;
        glds4v(x0 + kt * 64, vA, vA + 15360u, vA + 2 * 15360u, vA + 3 * 15360u, sA + lw * 4096);
        const u16* b = wqkvA + (size_t)kt * 64 * 3072;
        glds4v(b, vB0, vB1, vB0, vB1, sB + lw * 6144);
        glds2v(b + 2048, vB0, vB1, sB + lw * 6144 + 4096);
      }
    }
  }
  f32x16 xq;
  u32x4 nv;
  u64 nk;
  asm volatile("" : "=v"(nv));
  asm volatile("" : "=s"(nk));
#pragma unroll 1
  for (int layer = 0; layer < 4; ++layer) {
    if (layer & 1) g1_phase<1>(smem, ws, P, nv, nk, E, w, xq, layer, x0, wqkvB, woB, wphi, nullptr, nullptr);
    else g1_phase<0>(smem, ws, P, nv, nk, E, w, xq, layer, x0, wqkvA, woA, nullptr, wqkvB, woB);
    g2_phase(smem, ws, P, nv, nk, E, w, xq, layer, x0, (layer & 1) ? woB : woA, (layer & 1) ? wqkvA : wqkvB, out);
  }
  asm volatile(".Lmk_end:");
  WAITV(0);
  if (blockIdx.x == 0 && w == 0 && lane_id() == 0) __hip_atomic_store((gu64*)ws, E + 16u, __ATOMIC_RELAXED, __HIP_MEMORY_SCOPE_AGENT);
}

extern "C" void kernel_launch(void* const* d_in, const int* in_sizes, int n_in,
                              void* d_out, int out_size, void* d_ws, size_t ws_size,
                              hipStream_t stream) {
  hipFuncSetAttribute((const void*)mega_kernel, hipFuncAttributeMaxDynamicSharedMemorySize, LDS_TOTAL);
  mega_kernel<<<256, 512, LDS_TOTAL, stream>>>((const u16*)d_in[0], (const u16*)d_in[1], (const u16*)d_in[2], (const u16*)d_in[4],
                                               (const u16*)d_in[5], (const u16*)d_in[6], (u16*)d_out, (char*)d_ws);
}
```

```cpp
#include <hip/hip_runtime.h>
#include <hip/hip_bf16.h>
#include <stdint.h>
#include <type_traits>
#pragma clang diagnostic ignored "-Winline-asm"
#pragma clang diagnostic ignored "-Wunused-value"

typedef unsigned short u16;
typedef __attribute__((ext_vector_type(8))) short bf16x8;
typedef __attribute__((ext_vector_type(4))) short bf16x4;
typedef __attribute__((ext_vector_type(4))) float f32x4;
typedef __attribute__((ext_vector_type(4))) unsigned u32x4;
typedef __attribute__((ext_vector_type(2))) unsigned u32x2;
typedef __attribute__((ext_vector_type(8))) float f32x8;
typedef __attribute__((ext_vector_type(16))) unsigned u32x16;
typedef __attribute__((ext_vector_type(16))) float f32x16;
typedef __attribute__((ext_vector_type(32))) float f32x32;
template <int I, typename T> __device__ __forceinline__ f32x4 quad(const T& v) { return __builtin_shufflevector(v, v, 4 * I, 4 * I + 1, 4 * I + 2, 4 * I + 3); }

constexpr int SEQ = 2048, DM = 1024, NH = 32;
#define ST(i)
constexpr int KVP = 64 * 32 + 64;

typedef __attribute__((ext_vector_type(2))) float f32x2;
typedef __attribute__((ext_vector_type(2))) __bf16 bf16x2_t;
__device__ __forceinline__ unsigned pk2(float a, float b) {
  f32x2 v = {a, b};
  bf16x2_t r = __builtin_convertvector(v, bf16x2_t);
  return __builtin_bit_cast(unsigned, r);
}

__device__ __forceinline__ bf16x4 tr_read(const char* p) {
  return __builtin_amdgcn_ds_read_tr16_b64_v4i16((__attribute__((address_space(3))) bf16x4*)p);
}
template <typename T> __device__ __forceinline__ __attribute__((address_space(3))) T& lds(unsigned a) { return *(__attribute__((address_space(3))) T*)(size_t)a; }
__device__ __forceinline__ bf16x4 tr_read(unsigned a) { return __builtin_amdgcn_ds_read_tr16_b64_v4i16(&lds<bf16x4>(a)); }
__device__ __forceinline__ bf16x8 cat8(bf16x4 a, bf16x4 b) {
  bf16x8 f = {a[0], a[1], a[2], a[3], b[0], b[1], b[2], b[3]};
  return f;
}
__device__ __forceinline__ void glds16s(const void* sbase, unsigned voff, unsigned lds_base) {
  asm volatile("s_mov_b32 m0, %2\n\ts_nop 0\n\tglobal_load_lds_dwordx4 %0, %1" :: "v"(voff), "s"(sbase), "s"(lds_base) : "memory", "m0");
}
__device__ __forceinline__ void glds16c(const void* sbase, unsigned voff, unsigned lds_base) {
  asm volatile("s_mov_b32 m0, %2\n\ts_nop 0\n\tglobal_load_lds_dwordx4 %0, %1 sc1" :: "v"(voff), "s"(sbase), "s"(lds_base) : "memory", "m0");
}
#define GL_ "\n\tglobal_load_lds_dwordx4 "
__device__ __forceinline__ void glds4c(const void* sbase, unsigned v, unsigned lds_base) {
  asm volatile("s_mov_b32 m0, %2\n\ts_nop 0" GL_ "%0, %1 sc1" GL_ "%0, %1 offset:1024 sc1" GL_ "%0, %1 offset:2048 sc1" GL_ "%0, %1 offset:3072 sc1"
               :: "v"(v), "s"(sbase), "s"(lds_base) : "memory", "m0");
}
__device__ __forceinline__ void glds4v(const void* sbase, unsigned v0, unsigned v1, unsigned v2, unsigned v3, unsigned lds_base) {
  asm volatile("s_mov_b32 m0, %5\n\ts_nop 0" GL_ "%0, %4" GL_ "%1, %4 offset:1024" GL_ "%2, %4 offset:2048" GL_ "%3, %4 offset:3072"
               :: "v"(v0), "v"(v1), "v"(v2), "v"(v3), "s"(sbase), "s"(lds_base) : "memory", "m0");
}
__device__ __forceinline__ void glds2v(const void* sbase, unsigned v0, unsigned v1, unsigned lds_base) {
  asm volatile("s_mov_b32 m0, %3\n\ts_nop 0" GL_ "%0, %2" GL_ "%1, %2 offset:1024" :: "v"(v0), "v"(v1), "s"(sbase), "s"(lds_base) : "memory", "m0");
}
__device__ __forceinline__ void gld16(u32x4& d, unsigned voff, const void* sbase) {
  asm volatile("global_load_dwordx4 %0, %1, %2" : "=v"(d) : "v"(voff), "s"(sbase) : "memory");
}
__device__ __forceinline__ unsigned lds_addr(const void* p) { return (unsigned)(size_t)p; }
#define WAITV(n) asm volatile("s_waitcnt vmcnt(" #n ")" ::: "memory")
#define WAITVN(n) asm volatile("s_waitcnt vmcnt(%0)" :: "n"(n) : "memory")
#define WAITL0 do { asm volatile("" ::: "memory"); __builtin_amdgcn_s_waitcnt(0xC07F); asm volatile("" ::: "memory"); } while (0)
#define BAR __builtin_amdgcn_s_barrier()
#define ARRIVED(...) do { WAITV(0); __builtin_amdgcn_sched_barrier(0); asm volatile("" : __VA_ARGS__); } while (0)

__device__ __forceinline__ float vmaxf(float a, float b) { float r; asm("v_max_f32 %0, %1, %2" : "=v"(r) : "v"(a), "v"(b)); return r; }
template <int N> __device__ __forceinline__ float dpp_ror(float x) {
  return __uint_as_float(__builtin_amdgcn_update_dpp(0u, __float_as_uint(x), 0x120 + N, 0xf, 0xf, false));
}
__device__ __forceinline__ float r16_sum(float x) { x += dpp_ror<8>(x); x += dpp_ror<4>(x); x += dpp_ror<2>(x); return x + dpp_ror<1>(x); }
__device__ __forceinline__ float vmax3f(float a, float b, float c) { float r; asm("v_max3_f32 %0, %1, %2, %3" : "=v"(r) : "v"(a), "v"(b), "v"(c)); return r; }
__device__ __forceinline__ void max8pn(const f32x4& a, const f32x4& b, float& mx, float& mn) {
  asm("v_max3_f32 %0, %2, %3, %4\n\tv_max3_f32 %1, -%2, -%3, -%4\n\tv_max3_f32 %0, %0, %5, %6\n\tv_max3_f32 %1, %1, -%5, -%6\n\t"
      "v_max3_f32 %0, %0, %7, %8\n\tv_max3_f32 %1, %1, -%7, -%8\n\tv_max_f32 %0, %0, %9\n\tv_max_f32 %1, %1, -%9\n\ts_nop 0"
      : "=&v"(mx), "=&v"(mn) : "v"(a[0]), "v"(a[1]), "v"(a[2]), "v"(a[3]), "v"(b[0]), "v"(b[1]), "v"(b[2]), "v"(b[3]));
}
__device__ __forceinline__ float vmaxf_n(float a, float b) { float r; asm("v_max_f32 %0, %1, %2\n\ts_nop 0" : "=v"(r) : "v"(a), "v"(b)); return r; }
__device__ __forceinline__ float vmax3f_n(float a, float b, float c) { float r; asm("v_max3_f32 %0, %1, %2, %3\n\ts_nop 0" : "=v"(r) : "v"(a), "v"(b), "v"(c)); return r; }
template <bool MAXOP> __device__ __forceinline__ float rop(float a, float b) { return MAXOP ? vmaxf_n(a, b) : a + b; }
template <bool MAXOP> __device__ __forceinline__ void xr2(float& a, float& b) {
  auto r = __builtin_amdgcn_permlane16_swap(__float_as_uint(a), __float_as_uint(b), false, false);
  unsigned u = __float_as_uint(rop<MAXOP>(__uint_as_float(r[0]), __uint_as_float(r[1])));
  auto r2 = __builtin_amdgcn_permlane32_swap(u, u, false, false);
  u = __float_as_uint(rop<MAXOP>(__uint_as_float(r2[0]), __uint_as_float(r2[1])));
  auto r3 = __builtin_amdgcn_permlane16_swap(u, u, false, false);
  a = __uint_as_float(r3[0]); b = __uint_as_float(r3[1]);
}
template <bool MAXOP> __device__ __forceinline__ void xr4(float& a, float& b, float& c, float& d) {
  auto r = __builtin_amdgcn_permlane16_swap(__float_as_uint(a), __float_as_uint(b), false, false);
  auto q = __builtin_amdgcn_permlane16_swap(__float_as_uint(c), __float_as_uint(d), false, false);
  const unsigned ab = __float_as_uint(rop<MAXOP>(__uint_as_float(r[0]), __uint_as_float(r[1])));
  const unsigned cd = __float_as_uint(rop<MAXOP>(__uint_as_float(q[0]), __uint_as_float(q[1])));
  auto t = __builtin_amdgcn_permlane32_swap(ab, cd, false, false);
  const unsigned u = __float_as_uint(rop<MAXOP>(__uint_as_float(t[0]), __uint_as_float(t[1])));
  auto h = __builtin_amdgcn_permlane32_swap(u, u, false, false);
  auto p0 = __builtin_amdgcn_permlane16_swap(h[0], h[0], false, false);
  auto p1 = __builtin_amdgcn_permlane16_swap(h[1], h[1], false, false);
  a = __uint_as_float(p0[0]); b = __uint_as_float(p0[1]); c = __uint_as_float(p1[0]); d = __uint_as_float(p1[1]);
}

__device__ __forceinline__ unsigned xs4(float a, float b, float c, float d) {
  auto r = __builtin_amdgcn_permlane16_swap(__float_as_uint(a), __float_as_uint(b), false, false);
  auto q = __builtin_amdgcn_permlane16_swap(__float_as_uint(c), __float_as_uint(d), false, false);
  auto t = __builtin_amdgcn_permlane32_swap(__float_as_uint(__uint_as_float(r[0]) + __uint_as_float(r[1])), __float_as_uint(__uint_as_float(q[0]) + __uint_as_float(q[1])), false, false);
  return __float_as_uint(__uint_as_float(t[0]) + __uint_as_float(t[1]));
}

#define MFMA(a, b, c) __builtin_amdgcn_mfma_f32_16x16x32_bf16(a, b, c, 0, 0, 0)

typedef unsigned long long u64;
typedef __attribute__((address_space(1))) unsigned long long gu64;
constexpr unsigned WS_PL = 0x1a0000, WS_PR = 1024;
constexpr unsigned WS_FLAGS = 16384;
constexpr int FLS = 64;
constexpr unsigned WS_G0 = 65536;
constexpr unsigned WS_CSP = WS_G0 + SEQ * 16;
constexpr unsigned WS_ST = WS_CSP + 16 * 16 * 768;
constexpr unsigned WS_X = 1u << 21;
constexpr unsigned WS_O = WS_X + 3u * SEQ * DM * 2;
constexpr unsigned KVPB = KVP * 2, KVFB = KVP * 4;
constexpr unsigned EMPTY = 0xffffffffu;
constexpr unsigned WS_KV = WS_O + 4u * SEQ * DM * 2;
constexpr unsigned KVLB = 256u * 16384;
constexpr unsigned WS_KF = 0x110000, KFLB = 17u * 16384;
constexpr unsigned WS_HL = WS_KV + 2u * KVLB;
static_assert(WS_ST % 16384 == 0 && WS_ST + 3u * SEQ * 128 <= WS_KF && WS_KF + 2 * KFLB <= WS_PL && WS_PL + 16384 <= WS_X && (WS_X >> 21 & 1) && (WS_O >> 21 & 1) && (WS_KV >> 21 & 1) && (WS_HL >> 21 & 1) && WS_HL % (1u << 21) == 0, "");
constexpr unsigned WS_CSA = WS_HL + 512u * 4096;
constexpr unsigned WS_CPA = WS_CSA + 256u * 1024;
constexpr unsigned WS_END = WS_CPA + 16 * 16 * 768;
constexpr int LDS_TOTAL = 163840;
constexpr int OFF_E = 139264;
constexpr int OFF_WO = 49152;
template <int V> using IC = std::integral_constant<int, V>;
__device__ __forceinline__ bf16x8 ones8() { unsigned e = 0x3F803F80u; asm volatile("" : "+v"(e)); return __builtin_bit_cast(bf16x8, u32x4{e, e, e, e}); }
__device__ __forceinline__ float zero1() { float z; asm volatile("v_mov_b32 %0, 0" : "=v"(z)); return z; }
__device__ __forceinline__ u32x4 empty4() { unsigned e = EMPTY; asm volatile("" : "+v"(e)); return u32x4{e, e, e, e}; }
__device__ __forceinline__ u64 korder(int v, int own) {
  const u64 t = v == 0 ? 0x3bf7210edca98654ull : v == 1 ? 0xb37f654210edca98ull : v == 2 ? 0x3bf7a98654210edcull : 0xb37fedca98654210ull;
  const u64 inv = v == 0 ? 0xd876e543c210fba9ull : v == 1 ? 0xc543f210dba9e876ull : v == 2 ? 0xd210eba9c876f543ull : 0xcba9f876d543e210ull;
  const u64 low = (1ull << (4 * ((inv >> (4 * own)) & 15))) - 1ull;
  return (u64)own | ((t & low) << 4) | (t & ~((low << 4) | 15ull));
}
__device__ __forceinline__ u64 korder0(int rt) {
  const int r = rt & 7;
  const u64 own = (0x765432765432ull >> (8 * ((0x4924 >> (2 * r)) & 3))) & 0xffffffull, oth = (0x7654321076543210ull >> (4 * r)) & 0xffffffffull;
  const u64 t = 0x10ull | (oth << 8) | (own << 40);
  return t | (rt < 8 ? 0x0000008888888800ull : 0x8888880000000088ull);
}
__device__ __forceinline__ int knib(u64 t, int s) { return (int)(t >> (4 * s)) & 15; }
__device__ __forceinline__ unsigned kfirst(u64 t) { return (1u << knib(t, 1)) | (1u << knib(t, 2)) | (1u << knib(t, 3)) | (1u << knib(t, 4)); }
__device__ __forceinline__ int kv1(int bid) { return (bid + (0x4924 >> ((bid >> 2) & 14))) & 3; }

template <typename T> __device__ __forceinline__ T* uni(T* p) {
  const unsigned long long v = (unsigned long long)p;
  const unsigned lo = __builtin_amdgcn_readfirstlane((unsigned)v), hi = __builtin_amdgcn_readfirstlane((unsigned)(v >> 32));
  return (T*)(((unsigned long long)hi << 32) | lo);
}
__device__ __forceinline__ u64 uni2(unsigned lo, unsigned hi) {
  lo = __builtin_amdgcn_readfirstlane(lo); hi = __builtin_amdgcn_readfirstlane(hi);
  return ((u64)hi << 32) | lo;
}
__device__ __forceinline__ int lane_id() {
  int l;
  asm volatile("v_mbcnt_lo_u32_b32 %0, -1, 0\n\tv_mbcnt_hi_u32_b32 %0, -1, %0" : "=v"(l));
  __builtin_assume(l >= 0 && l < 64);
  return l;
}
#define CODE_TOUCH(n, from, to) \
  unsigned long long pc; unsigned v; \
  { unsigned o, len; \
    asm volatile("s_getpc_b64 %0\n.Lmk_t" #n ":\n\ts_mov_b32 %1, " from "-.Lmk_t" #n "+0x40000\n\ts_mov_b32 %2, " to "-" from : "=s"(pc), "=s"(o), "=s"(len)); \
    pc -= 0x40000; v = (unsigned)(((bid >> 3) & 7) + lane * 8) * 128; v = o + ((int)v < (int)len ? v : 0u); }
#define IDS \
  const int lane = lane_id(); \
  asm volatile("" : "+s"(w)); \
  const int tid = w * 64 + lane; \
  int bid = blockIdx.x; asm volatile("" : "+s"(bid)); \
  ws = uni(ws); asm volatile("" : "+s"(ws)); \
  layer = __builtin_amdgcn_readfirstlane(layer); x0 = uni(x0); wo = uni(wo); \
    \
  const int rt = 2 * (bid & 4) + ((bid >> 3) & 7); \
  const int hp = 4 * (bid & 3) + (bid >> 6); \
  const int m0 = rt * 128; \
  P = __builtin_amdgcn_readfirstlane(P); \
  const unsigned SX = (unsigned)((rt & 7) * 524288) + ((((rt >> 3) ^ (rt >> 2) ^ 1) & 1) * 8192 ^ P); (void)SX; \
  const int fr = lane & 15, g = lane >> 4; \
  const int q4 = (lane & 15) >> 2, p4 = lane & 3; \
  const __amdgpu_buffer_rsrc_t rsW = __builtin_amdgcn_make_buffer_rsrc((void*)ws, (short)0, (int)WS_END, 0x00020000); \
  (void)m0; (void)fr; (void)q4; (void)p4; (void)rsW; (void)g;

__device__ __forceinline__ void publish(__amdgpu_buffer_rsrc_t rsW, int tid, unsigned idx, u64 v, unsigned P) {
  unsigned off = (WS_FLAGS + idx * FLS) ^ P;
  asm volatile("" : "+v"(off), "+v"(v));
  WAITV(0);
  WAITL0; BAR;
  if (tid == 192) __builtin_amdgcn_raw_buffer_store_b64(__builtin_bit_cast(u32x2, v), rsW, off, 0, 16);
  asm volatile("" ::: "memory");
}
__device__ __forceinline__ void poll_flags(char* ws, unsigned P, u64 E, int set, int rt, int hp, int lane, unsigned n, int grp) {
  const int l = lane & 15, g = lane >> 4;
  const int fi = set * 256 + (g == 2 ? l * 16 + hp : rt * 16 + l);
  const unsigned need = g == grp ? n : 0u;
  const unsigned off = need ? (WS_FLAGS + fi * FLS) ^ P : 0u;
  _Pragma("unroll 1") for (unsigned spins = 0; spins < (1u << 18); ++spins) {
    u64 d;
    asm volatile("global_load_dwordx2 %0, %1, %2 sc1\n\ts_waitcnt vmcnt(0)" : "=v"(d) : "v"(off), "s"(ws) : "memory");
    d -= E;
    const bool ok = (need == 0u) || ((unsigned)(d >> 32) == 0u && (unsigned)d >= need && (unsigned)d <= 16u);
    if (__all(ok)) break;
    __builtin_amdgcn_s_sleep(1);
  }
}

__device__ __forceinline__ u64 poll_eq(const char* ws, unsigned off, u64 want, u64 mask, u64 now) {
  u64 miss;
  _Pragma("unroll 1") for (unsigned spins = 0; spins < (1u << 18); ++spins) {
    u64 d;
    asm volatile("global_load_dwordx2 %0, %1, %2 sc1\n\ts_waitcnt vmcnt(0)" : "=v"(d) : "v"(off), "s"(ws) : "memory");
    miss = __ballot(d != want) & mask;
    if (!(miss & now)) break;
    __builtin_amdgcn_s_sleep(1);
  }
  return miss;
}

#define G1_MACROS \
  ".macro G1M rb, j, s, z\n" \
  " .set g1d, 160+24*(\\rb)+4*(\\j)\n .set g1a, 80+40*(\\s)+4*(\\rb)\n .set g1b, 96+40*(\\s)+4*(\\j)\n" \
  " .if (\\j) >= 4\n  .set g1t, g1a\n  .set g1a, g1b\n  .set g1b, g1t\n .endif\n" \
  " .if \\z\n  v_mfma_f32_16x16x32_bf16 v[g1d:g1d+3], v[g1b:g1b+3], v[g1a:g1a+3], 0\n" \
  " .else\n  v_mfma_f32_16x16x32_bf16 v[g1d:g1d+3], v[g1b:g1b+3], v[g1a:g1a+3], v[g1d:g1d+3]\n .endif\n" \
  ".endm\n" \
  ".macro G1RA rb, s, ra, oa\n" \
  " ds_read_b128 v[80+40*(\\s)+4*(\\rb):83+40*(\\s)+4*(\\rb)], \\ra offset:(\\oa)+(((\\rb)*2048)^(4096*(1-%[hh])))\n" \
  ".endm\n" \
  ".macro G1RB k, s, r0, r1, ob\n" \
  " .if (\\k) & 2\n  ds_read_b64_tr_b16 v[96+40*(\\s)+2*(\\k):97+40*(\\s)+2*(\\k)], \\r1 offset:(\\ob)+((\\k)>>2)*2048+((\\k)&1)*512\n" \
  " .else\n  ds_read_b64_tr_b16 v[96+40*(\\s)+2*(\\k):97+40*(\\s)+2*(\\k)], \\r0 offset:(\\ob)+((\\k)>>2)*2048+((\\k)&1)*512\n .endif\n" \
  ".endm\n" \
    \
  ".macro G1BK i, g, r0, r1, ob\n G1RB 4*(\\g)+((\\i)&3), (\\i)>>2, \\r0, \\r1, (\\ob)+((\\i)>>2)*12288\n.endm\n" \
  ".macro G1AK rb, s, a0, a1, oa\n .if \\s\n  G1RA \\rb, 1, \\a1, \\oa\n .else\n  G1RA \\rb, 0, \\a0, \\oa\n .endif\n.endm\n" \
  ".macro G1RDB g, r0, r1, ob\n .set g1i, 0\n .rept 8\n  G1BK g1i, \\g, \\r0, \\r1, \\ob\n  .set g1i, g1i+1\n .endr\n.endm\n" \
  ".macro G1RDA a0, a1, oa\n .set g1i, 0\n .rept 8\n  G1AK (g1i>>1)^2, g1i&1, \\a0, \\a1, \\oa\n  .set g1i, g1i+1\n .endr\n.endm\n" \
    \
  ".macro G1Q g, rb, q, i, z\n G1M \\rb, 2*(\\g)+((((\\i)>>1)^(\\q))&1), (((\\i)+1)>>1)&1, (\\z)&&(((\\i)&1)==0)\n.endm\n" \
    \
  ".macro G1SG g, z, rd, a0, a1, r0, r1, o\n" \
  " .set g1i, 0\n .rept 16\n" \
  "  .if ((\\g) == 0) && (g1i == 8) && (\\rd)\n   s_waitcnt lgkmcnt(7)\n   s_nop 0\n  .endif\n" \
  "  .if ((\\g) == 0) && (g1i == 15)\n   s_waitcnt lgkmcnt(0)\n  .endif\n" \
  "  G1Q \\g, (g1i>>2)^(2-((\\g)&1)), g1i>>2, g1i&3, \\z\n" \
  "  .if \\rd\n" \
  "   .if (\\g) == 0\n    .if g1i < 7\n     G1BK g1i, 2, \\r0, \\r1, \\o\n    .elseif g1i == 8\n     G1BK 7, 2, \\r0, \\r1, \\o\n    .endif\n" \
  "   .elseif (\\g) == 1\n    .if g1i < 8\n     G1BK g1i, 0, \\r0, \\r1, \\o\n    .endif\n" \
  "   .elseif (g1i & 2) == 0\n    G1BK (g1i&1)+2*(g1i>>2), 1, \\r0, \\r1, \\o\n" \
  "   .elseif (\\rd) == 1\n    G1AK (g1i>>2)^2, 1-(g1i&1), \\a0, \\a1, \\o\n   .endif\n" \
  "  .endif\n" \
  "  .set g1i, g1i+1\n .endr\n" \
  ".endm\n" \
    \
    \
  ".macro G1END h, i0, n, l, xs, r0, r1\n" \
  " .set g1i, \\i0\n .rept \\n\n" \
  "  .if (\\l) && (g1i-(\\i0) < 12)\n   .set g1x, ((\\l)>>(4*(g1i-(\\i0))))&15\n" \
  "   .if g1x < 15\n    ds_write_b128 \\xs, v[208+4*g1x:211+4*g1x] offset:(g1x+2*%[hh]*(g1x/6))*1024\n   .endif\n  .endif\n" \
  "  G1Q g1i>>3, 2*(\\h)+(((g1i>>2)^(g1i>>3))&1), g1i>>2, g1i&3, 0\n" \
  "  .if ((\\l) == 0) && (g1i < 8)\n   G1BK g1i, 2, \\r0, \\r1, 0\n  .endif\n" \
  "  .set g1i, g1i+1\n .endr\n" \
  ".endm\n"
#define G1_PURGE \
  ".purgem G1M\n .purgem G1RA\n .purgem G1RB\n .purgem G1BK\n .purgem G1AK\n .purgem G1RDB\n .purgem G1RDA\n .purgem G1Q\n .purgem G1SG\n .purgem G1END\n"
#define G1_CLOBBER \
  "v80", "v81", "v82", "v83", "v84", "v85", "v86", "v87", "v88", "v89", "v90", "v91", "v92", "v93", "v94", "v95", "v96", "v97", "v98", "v99", "v100", \
  "v101", "v102", "v103", "v104", "v105", "v106", "v107", "v108", "v109", "v110", "v111", "v112", "v113", "v114", "v115", "v116", "v117", "v118", \
  "v119", "v120", "v121", "v122", "v123", "v124", "v125", "v126", "v127", "v128", "v129", "v130", "v131", "v132", "v133", "v134", "v135", "v136", \
  "v137", "v138", "v139", "v140", "v141", "v142", "v143", "v144", "v145", "v146", "v147", "v148", "v149", "v150", "v151", "v152", "v153", "v154", \
  "v155", "v156", "v157", "v158", "v159", "v208", "v209", "v210", "v211", "v212", "v213", "v214", "v215", "v216", "v217", "v218", "v219", "v220", \
  "v221", "v222", "v223", "v232", "v233", "v234", "v235", "v236", "v237", "v238", "v239", "v240", "v241", "v242", "v243", "v244", "v245", "v246", "v247"

template <int MODE>
__device__ __forceinline__ void g1_phase(char* smem, char* ws, unsigned& P, const u32x4& nv, u64 nk, u64& E, int w, f32x16& xq, int layer, const u16* __restrict__ x0, const u16* __restrict__ W,
                                         const u16* __restrict__ wo, const u16* __restrict__ wphi,
                                         const u16* __restrict__ wn1, const u16* __restrict__ wn2) {
  constexpr bool HH = (MODE == 1);
  constexpr int ABYTES = 128 * 128;
  constexpr int STAGE = ABYTES + 3 * 8192;
  constexpr int OFF_KS = 147456;
  constexpr int OFF_SP = 3 * STAGE;
  constexpr int OFF_RS = !HH ? OFF_SP : OFF_SP + 16384;
  constexpr int OFF_CS = OFF_RS + 1152;
  constexpr int OFF_WP = OFF_CS + 1536;
  constexpr int OFF_JK = LDS_TOTAL - 1024;
  constexpr int OFF_PK = 0;
  constexpr int OFF_VF = OFF_WP + 4096;
  constexpr int OFF_KV = 16384;
  constexpr int OFF_R2 = 135168;
  const int XS = HH ? 98304 + ((w >> 1) & 1) * 16384 + (w & 1) * 4096 : (w & 3) * 12288;
  constexpr int PSTR = 144;
  if constexpr (HH) asm volatile(".Lmk_g1b:"); else asm volatile(".Lmk_g1a:");
  IDS
  W = uni(W); wphi = uni(wphi); wn1 = uni(wn1); wn2 = uni(wn2);
  const int kg = w >> 2, wm = (w >> 1) & 1, wn = w & 1;
  const bool first = !HH && layer == 0;
  f32x4 acc[2][6];
  u32x4 pw[8];
  ST(layer * 24 + 0)
  const int R0 = wm * 64 + (HH ? kg : kg ^ 1) * 32;
  u32x16 pa;
#define pcs pa[0]
#define prs pa[1]
#define opo pa[2]
#define ogo pa[3]
#define pkw pa[5]
#define hof pa[7]
#define dm pa[8]
#define dm2 pa[9]
#define pcv pa[4]
#define pwf pa[5]
#define prv pa[6]
#define pvf pa[7]
#define ppk pa[10]
#define ppa pa[11]
#define pvr pa[12]
#define gkv pa[13]
#define gkz pa[14]
  f32x4 bias[3];
  const unsigned sb = lds_addr(smem);
  auto prep = [&](int part = 3) {
    if (part & 1) {
    pcs = sb + OFF_CS + wn * 128 + g * 16;
    prs = sb + OFF_RS + (R0 + fr) * 8;
    opo = (unsigned)(wn * 8192 + R0 * 64 + lane * 16);
    ogo = opo + WS_O + layer * (SEQ * DM * 2) + SX + (unsigned)(wn * 8192 + hp * 32768);
    opo += sb;
    pcv = sb + OFF_CS + (wn * 32 + 128 + fr) * 4;
    prv = sb + OFF_R2 + (R0 + 4 * g) * 4;
    if (layer < 2) {
      constexpr unsigned kvn = WS_KV + MODE * KVLB;
      dm = kvn + (unsigned)((rt * 16 + hp) * 16384 + (w >> 2) * 8192 + (tid & 255) * 16);
      dm2 = w >= 4 ? WS_HL + (unsigned)((1 << 20) + ((rt & 7) * 8 + (hp >> 1)) * 16384 + (rt >> 3) * 8192 + (hp & 1) * 4096 + (tid - 256) * 16)
          : tid < 16 ? kvn + (unsigned)((rt * 16 + hp) * 16384 + (tid >> 3) * 8192 + 4096 + (tid & 7) * 16)
          : tid < 18 ? WS_KF + MODE * KFLB + (unsigned)(16 * 16384 + (tid & 1) * 8192 + hp * 256 + rt * 16)
          : tid >= 64 && tid < 128 ? WS_KF + MODE * KFLB + (unsigned)(hp * 16384 + (tid & 32) * 256 + rt * 512 + (tid & 31) * 16) : 0x7fff0000u;
    }
    }
    if (!(part & 2)) {
    } else if constexpr (!HH) {
      pkw = sb + OFF_KS - 4096 + R0 * 128 + wn * 2048 + lane * 16;
      const int rr = R0 ? rt : rt - 1;
      hof = m0 + R0 == 0 ? 0x7fff0000u : WS_HL + (unsigned)(layer * 524288 + ((rr & 7) * 8 + (hp >> 1)) * 16384 + (hp & 1) * 4096 + wn * 2048 + lane * 16) + ((((rr + 1) >> 3 ^ 1) & 1) * 8192 ^ P);
      const unsigned out = ~((0x5555u << 2 << fr) >> (4 * g));
#pragma unroll
      for (int i = 0; i < 8; ++i) {
        unsigned t;
        asm("v_bfe_i32 %0, %1, %2, 1" : "=v"(t) : "v"(out), "n"((i >> 2) * 16 + (i & 3)));
        bias[i >> 2][i & 3] = __uint_as_float(t & 0xff61b1e6u);
      }
      bias[2] = m0 + R0 == 0 ? f32x4{-3.0e38f, -3.0e38f, -3.0e38f, -3.0e38f} : bias[0];
    } else {
      const int u = wm * 2 + kg;
      pwf = sb + OFF_WP + wn * 2048 + (4 * g + q4) * 64 + p4 * 8;
      pvf = sb + OFF_VF + w * 2048 + lane * 16;
      ppk = sb + OFF_PK + w * (32 * PSTR) + fr * PSTR + g * 8;
      ppa = sb + OFF_PK + wn * (32 * PSTR) + (4 * g + q4) * PSTR + u * 32 + p4 * 8;
      pvr = sb + OFF_VF + wn * 2048 + lane * 16;
      const unsigned doff = WS_KV + (layer >> 1) * KVLB + (unsigned)((rt * 16 + hp) * 16384) + (((wn ^ (rt >> 3) ^ 1) & 1) * 8192 ^ P);
      gkv = doff + u * 1024 + lane * 16;
      gkz = fr == 0 ? doff + 4096 + u * 32 + g * 8 : 0x7fff0000u;
    }
    if (part & 2) asm volatile("" : "+{v[48:63]}"(pa), "+{v[36:39]}"(bias[0]), "+{v[40:43]}"(bias[1]), "+{v[44:47]}"(bias[2]));
    else asm volatile("" : "+{v[48:63]}"(pa));
  };

  if (kg == 1) {
    const int lw = w & 3;
    const int kB = 16 * lw + (lane >> 3);
    const int fB = (kB >> 1) & 1;
    const int cB0 = ((((lane & 7) >> 1) ^ fB) << 1) | (lane & 1), cB1 = ((((lane & 7) >> 1) ^ (fB | 2)) << 1) | (lane & 1);

    unsigned vW0, vW1;
    u64 ko2;
    auto run = [&](auto fst) {
      constexpr bool FIRST = decltype(fst)::value;
      if constexpr (!HH) { if constexpr (FIRST) asm volatile(".Lmk_f1:"); else asm volatile(".Lmk_f0:"); }
      constexpr int N = 10;
      const unsigned vB0 = FIRST ? (unsigned)((kB * 3072 + hp * 64 + cB0 * 8) * 2) : nv[0];
      const unsigned vB1 = FIRST ? (unsigned)(((kB + 8) * 3072 + hp * 64 + cB1 * 8) * 2) - 1024u : nv[1];
      const unsigned vA = FIRST ? (unsigned)(((m0 + lw * 32 + (lane >> 3)) * DM + ((lane & 7) ^ (lane >> 3)) * 8) * 2)
                                : nv[2];
      const char* xa = FIRST ? (const char*)x0 : ws + WS_X + (layer - 1) * (SEQ * DM * 2);
      constexpr int KS = FIRST ? 128 : 32768;
      const u64 kord = FIRST ? korder0(rt) : uni2((unsigned)nk, (unsigned)(nk >> 32));
      auto ktile = [&](int ks) { return knib(kord, ks); };
      auto stageA = [&](int ks, int boff) {
        const int kt = ktile(ks);
        const unsigned sA = lds_addr(smem) + boff;
        if (FIRST) glds4v(xa + kt * KS, vA, vA + 15360u, vA + 2 * 15360u, vA + 3 * 15360u, sA + lw * 4096);
        else glds4c(xa + kt * KS, vA, sA + lw * 4096);
      };
      auto stageB = [&](const u16* Wp, int ks, int boff) {
        const int kt = ktile(ks);
        const unsigned sB = lds_addr(smem) + boff + ABYTES;
        const u16* b = Wp + (size_t)kt * 64 * 3072;
        glds4v(b, vB0, vB1, vB0, vB1, sB + lw * 6144);
        glds2v(b + 2048, vB0, vB1, sB + lw * 6144 + 4096);
      };
      bf16x8 sel[3];
      unsigned cso[3], cst;
      if (FIRST) {
        stageA(1, STAGE); stageB(W, 1, STAGE);
        __builtin_amdgcn_sched_barrier(0);
        WAITVN(N);
        BAR;
        asm volatile("" ::: "memory");
        const u32x2 ev = *(const u32x2*)(smem + OFF_E);
        stageA(2, 2 * STAGE); stageB(W, 2, 2 * STAGE);
        __builtin_amdgcn_sched_barrier(0);
        const int fl = (q4 >> 1) | ((g & 1) << 1), j = 3 * wm + (fr < 3 ? fr : 0);
#pragma unroll
        for (int i = 0; i < 3; ++i) {
          const unsigned o = fr == i ? 0x3F803F80u : 0u;
          sel[i] = __builtin_bit_cast(bf16x8, u32x4{o, o, o, o});
          cso[i] = (unsigned)(2 * STAGE + ABYTES + (g >> 1) * 6144 + (g & 1) * 1024 + q4 * 128 + p4 * 8 + (((wn * 2 + ((wm + i) & 1)) ^ fl) << 5) + ((3 * wm + i) >> 1) * 2048);
          asm volatile("" : "+v"(sel[i]), "+v"(cso[i]));
        }
        cst = fr < 3 ? WS_CPA + (unsigned)(((hp * 16 + rt) * 192 + (j >> 1) * 64 + wn * 32 + (j & 1) * 16 + 4 * g) * 4) : 0x7fff0000u;
        asm volatile("" : "+v"(cst));
        E = uni2(ev[0], ev[1]);
      }
      u64 fall, fal2;
      unsigned fao, fa2, vP, vS, vC, vX, dS, dC, dR;
      auto ld_pw = [&](int i0) {
#pragma unroll
        for (int i = i0; i < i0 + 4; ++i) gld16(pw[i], vP, wn1 + i * 8 * 3072);
      };
      auto setup = [&]() {
        fao = (unsigned)((256 + rt * 16 + (lane & 15)) * FLS) ^ P;
        fa2 = (unsigned)(((FIRST ? 512 : 256) + (lane & 15) * 16 + (FIRST ? hp : (hp - ((1 << (lane >> 4)) >> 1)) & 15)) * FLS) ^ P;
        vP = (unsigned)((rt * 64 + (lane >> 3)) * 6144 + lw * 2048 + ((hp + (1 << lw)) & 15) * 128 + (lane & 7) * 16);
        vS = FIRST ? (unsigned)(WS_G0 + (m0 + lw * 32 + (lane & 31)) * 16)
                   : WS_ST + (unsigned)((layer - 1) * (SEQ * 128) + (rt & 7) * 32768 + g * 1024 + lw * 256 + fr * 16) + ((rt >> 3) * 8192 ^ P);
        vC = (FIRST ? WS_CPA : WS_CSP) + (unsigned)(hp * 12288 + g * 768 + lw * 192 + (fr < 12 ? fr : 0) * 16);
        if (!FIRST) vX = HH ? (unsigned)(hp * 4096 + lw * 1024 + lane * 16) : (unsigned)((rt * 16 + hp) * 1024 + lane * 16);
        vW0 = (unsigned)((kB * DM + hp * 64 + ((lane & 7) ^ ((lane >> 4) << 1)) * 8) * 2); vW1 = vW0 + 8 * DM * 2 - 1024u;
        dS = OFF_RS + lw * 256 + (FIRST ? (lane & 31) * 8 : fr * 16 + (g & 1) * 8); dC = OFF_CS + lw * 192 + (fr < 12 ? fr : 0) * 16 + g * 4;
        ko2 = korder(bid & 3, hp);
        dR = OFF_R2 + lw * 128 + (FIRST ? (lane & 31) * 4 : fr * 8 + (g & 1) * 4);
        asm volatile("" : "+v"(dR));
        asm volatile("" : "+v"(vS), "+v"(vX), "+v"(vW0), "+v"(vW1), "+v"(dS));
        asm volatile("" : "+s"(ko2));
        if (FIRST || HH) asm volatile("" : "+v"(vC), "+v"(dC), "+v"(fa2));
        if (FIRST) asm volatile("" : "+v"(vP)); else asm volatile("" : "+v"(fao));
      };
      if (!FIRST) {
        const char* xa1 = xa + ktile(1) * KS;
        const char* xa2 = xa + ktile(2) * KS;
        const u16* b2 = W + (size_t)ktile(2) * 64 * 3072;
        unsigned sA = lds_addr(smem) + STAGE + lw * 4096, sB = lds_addr(smem) + 2 * STAGE + ABYTES + lw * 6144;
        const unsigned fo = nv[3];
        WAITV(0);
        BAR;
        u64 fl1;
        __builtin_amdgcn_s_sleep(6);
        asm volatile("global_load_dwordx2 %0, %1, %2 sc1" : "=v"(fl1) : "v"(fo), "s"(ws) : "memory");
        glds4v(b2, vB0, vB1, vB0, vB1, sB);
        glds2v(b2 + 2048, vB0, vB1, sB + 4096);
        asm volatile("" : "+s"(xa1), "+s"(xa2), "+s"(sA));
        setup();
        {
          WAITV(6);
          __builtin_amdgcn_sched_barrier(0);
          asm volatile("" : "+v"(fl1));
          const u64 m = __ballot(fo != 0u);
          if (__ballot(fl1 != E + (unsigned)layer) & m) poll_eq(ws, fo, E + (unsigned)layer, m, ~0ull);
        }
        glds4c(xa1, vA, sA); glds4c(xa2, vA, sA + STAGE);
        __builtin_amdgcn_sched_barrier(0);
        prep();
      }
      bf16x8 cf[6];
      f32x4 csum = {0, 0, 0, 0};
      if (FIRST) { fall = E + 1u; vX = (unsigned)(bid * 8192 + lw * 2048 + lane * 16); asm volatile("" : "+v"(fall), "+v"(vX)); }
#pragma unroll
      for (int kt = 0; kt < 13; ++kt) {
        const int cur = (kt % 3) * STAGE;
        if (!FIRST && kt == 0) { WAITV(4); }
        else if (FIRST && kt == 4 && lw == 1) { WAITVN(N + 2); }
        else if ((!FIRST && kt == 1 && lw == 3) || ((FIRST || HH) && kt == 10 && lw == 3) || (FIRST && (kt == 2 || kt == 4 || kt == 5 || (kt == 11 && lw == 0)))) { WAITVN(N + 1); }
        else { WAITVN(N); }
        if (!FIRST && kt == 2 && lw == 3) {
          asm volatile("" : "+v"(fall));
          if (__any(fall != E + (unsigned)layer)) poll_flags(ws, P, E, 1, rt, hp, lane, layer, 0);
        }
        if (HH && kt == 11 && lw == 3) {
          asm volatile("" : "+v"(fal2));
          if (__any(fal2 != E + (unsigned)layer)) {
            _Pragma("unroll 1") for (int j = 0; j < 4; ++j) poll_flags(ws, P, E, 1, rt, (hp - ((1 << j) >> 1)) & 15, lane, layer, 2);
          }
        }
        if (FIRST && kt == 11 && lw == 3) {
          asm volatile("" : "+v"(fal2));
          if (__any(fal2 != E + 1u)) poll_flags(ws, P, E, 2, rt, hp, lane, 1u, 2);
        }
        BAR;
        if (FIRST && kt == 1) {
          asm volatile("" ::: "memory");
#pragma unroll
          for (int i = 0; i < 6; ++i) { const char* t = smem + cso[i >> 1] + (i & 1) * 12288; cf[i] = cat8(tr_read(t), tr_read(t + 512)); }
        }
        if (FIRST && kt == 2 && lw == 2 && bid < 64 && (bid & 7) == ((bid >> 3) & 3)) {
          u64 t0, t1; unsigned d;
          asm volatile("s_memtime %0\n\ts_load_dword %2, %3, 0x0\n\ts_waitcnt lgkmcnt(0)\n\ts_memtime %1\n\ts_waitcnt lgkmcnt(0)"
                       : "=&s"(t0), "=&s"(t1), "=&s"(d) : "s"(ws + WS_PL + (bid >> 5) * 8192 + (bid & 31) * 256) : "memory");
          d = (unsigned)(t1 - t0);
          const u64 v = ((E + 1u) << 16) | (d < 65535u ? d : 65535u);
          __builtin_amdgcn_raw_buffer_store_b64(__builtin_bit_cast(u32x2, v), rsW, lane == 0 ? WS_PR + (unsigned)(bid & 56) : 0x7fff0000u, 0, 16);
        }
        if (FIRST && kt == 12 && lw == 2) asm volatile("global_load_dwordx2 %0, %1, %2 sc1" : "=v"(fal2) : "v"((lane & 7) * 8), "s"(ws + WS_PR) : "memory");
        if (FIRST && kt >= 2 && kt < 6 && lw == kt - 2) setup();
        if (FIRST && kt >= 6 && kt < 10 && (lw >> 1) == ((kt - 6) >> 1)) prep(1 + (kt & 1));
        stageA(kt + 3, cur);
        if (FIRST && kt == 1) {
          asm volatile("" : "+v"(sel[0]));
#pragma unroll
          for (int i = 0; i < 6; ++i) csum = MFMA(cf[i], sel[i >> 1], csum);
          asm volatile("" : "+v"(csum));
        }
        stageB(W, kt + 3, cur);
        if (FIRST) {
          if (kt == 1) __builtin_amdgcn_raw_buffer_store_b128(__builtin_bit_cast(u32x4, csum), rsW, cst, 0, 16);
          if (kt == 3 && tid == 320) __builtin_amdgcn_raw_buffer_store_b64(__builtin_bit_cast(u32x2, fall), rsW, (WS_FLAGS + (unsigned)(512 + rt * 16 + hp) * FLS) ^ P, 0, 16);
          if (kt == 3 || kt == 4) glds16s(wo + (kt - 3) * 512, vX, lds_addr(smem) + OFF_JK);
        }
        if constexpr (!HH) if (FIRST && kt == 10 && lw == 0) {
          CODE_TOUCH(1, ".Lmk_ep0", ".Lmk_end")
          asm volatile("s_mov_b32 m0, %2\n\ts_nop 0\n\tglobal_load_lds_dword %0, %1" :: "v"(v), "s"(pc), "s"(lds_addr(smem) + OFF_JK) : "memory", "m0");
        }
        if (!FIRST && kt == 0 && lw == 3) asm volatile("global_load_dwordx2 %0, %1, %2 sc1" : "=v"(fall) : "v"(fao), "s"(ws + WS_FLAGS) : "memory");
        if ((FIRST || HH) && kt == 9 && lw == 3) asm volatile("global_load_dwordx2 %0, %1, %2 sc1" : "=v"(fal2) : "v"(fa2), "s"(ws + WS_FLAGS) : "memory");
      }
      if (FIRST && lw < 3) { ld_pw(0); WAITVN(N + 4); } else { WAITVN(N); }
      BAR;
      u32x4 sv[4], cv[4];
      auto ld_c = [&](u32x4& d, unsigned v, int so) { asm volatile("buffer_load_dwordx4 %0, %1, %2, %3 offen sc1" : "=v"(d) : "v"(v), "s"(rsW), "s"(so) : "memory"); };
      auto wo_tile = [&](int t) { glds2v(wo + (size_t)knib(ko2, t) * 64 * DM, vW0, vW1, lds_addr(smem) + OFF_WO + t * 8192 + lw * 2048); };
#pragma unroll
      for (int i = 0; i < (FIRST ? 1 : 4); ++i) ld_c(sv[i], vS, (i & 1) * 4096 + (i >> 1) * 16384);
      if (FIRST || HH) {
#pragma unroll
        for (int i = 0; i < 4; ++i) ld_c(cv[i], vC, i * 3072);
      }
      if (HH) glds16s(wphi, vX, lds_addr(smem) + OFF_WP + lw * 1024);
      else if (!FIRST) glds16c(ws + WS_CSA, vX, lds_addr(smem) + OFF_CS);
      wo_tile(0); wo_tile(1);
      if (HH || (FIRST && lw < 3)) { WAITV(13); } else { WAITV(9); }
      BAR;
      wo_tile(2); wo_tile(3); wo_tile(4); wo_tile(5);
      if (FIRST) {
        glds16s(wn2, vX, lds_addr(smem) + OFF_JK); glds16s(wn2 + 512, vX, lds_addr(smem) + OFF_JK);
        WAITV(14);
        if (lw < 3) ld_pw(4);
      } else {
        WAITV(12);
      }
      auto fin = [&]() {
        if (FIRST) {
          const unsigned tag = (unsigned)E + 1u;
          asm volatile("" : "+v"(sv[0]));
          _Pragma("unroll 1") for (unsigned spins = 0; spins < (1u << 18); ++spins) {
            if (__all(sv[0][1] == tag && sv[0][3] == tag)) break;
            __builtin_amdgcn_s_sleep(1);
            asm volatile("buffer_load_dwordx4 %0, %1, %2, 0 offen sc1\n\ts_waitcnt vmcnt(0)" : "=v"(sv[0]) : "v"(vS), "s"(rsW) : "memory");
          }
          *(u32x2*)(smem + dS) = u32x2{sv[0][0], sv[0][2]};
          { unsigned* p = (unsigned*)(smem + dR); p[0] = sv[0][0]; p[128] = sv[0][2]; }
        } else {
          asm volatile("" : "+v"(sv[0]), "+v"(sv[1]), "+v"(sv[2]), "+v"(sv[3]));
          const f32x4 t = (__builtin_bit_cast(f32x4, sv[0]) + __builtin_bit_cast(f32x4, sv[1])) + (__builtin_bit_cast(f32x4, sv[2]) + __builtin_bit_cast(f32x4, sv[3]));
          const unsigned u = xs4(t[0], t[2], t[1], t[3]);
          auto h = __builtin_amdgcn_permlane32_swap(u, u, false, false);
          const float mean = __uint_as_float(h[0]) * (1.0f / 1024.0f);
          const float var = fmaxf(__uint_as_float(h[1]) * (1.0f / 1024.0f) - mean * mean, 0.f);
          const float rs = __builtin_amdgcn_rsqf(var + 1e-5f);
          *(float2*)(smem + dS) = make_float2(-mean, rs);
          { float* p = (float*)(smem + dR); p[0] = -mean; p[128] = rs; }
        }
      };
      auto cfin = [&]() {
        if (FIRST || HH) {
          asm volatile("" : "+v"(cv[0]), "+v"(cv[1]), "+v"(cv[2]), "+v"(cv[3]));
          const f32x4 t = (__builtin_bit_cast(f32x4, cv[0]) + __builtin_bit_cast(f32x4, cv[1])) + (__builtin_bit_cast(f32x4, cv[2]) + __builtin_bit_cast(f32x4, cv[3]));
          *(unsigned*)(smem + dC) = xs4(t[0], t[1], t[2], t[3]);
        }
      };
      cfin();
      if (FIRST && lw == 2) {
        asm volatile("" : "+v"(fal2));
        _Pragma("unroll 1") for (unsigned spins = 0; spins < (1u << 18); ++spins) {
          if (__all(((fal2 ^ ((E + 1u) << 16)) >> 16) == 0)) break;
          __builtin_amdgcn_s_sleep(1);
          asm volatile("global_load_dwordx2 %0, %1, %2 sc1\n\ts_waitcnt vmcnt(0)" : "=v"(fal2) : "v"((lane & 7) * 8), "s"(ws + WS_PR) : "memory");
        }
        unsigned d = (unsigned)fal2 & 0xffffu;
        d = min(d, (unsigned)__builtin_amdgcn_update_dpp(0u, d, 0xb1, 0xf, 0xf, false));
        d = min(d, (unsigned)__builtin_amdgcn_update_dpp(0u, d, 0x4e, 0xf, 0xf, false));
        *(unsigned*)(smem + OFF_E + 256) = __builtin_amdgcn_readlane(d, 4) < __builtin_amdgcn_readlane(d, 0) ? 8192u : 0u;
      }
      if (!HH) BAR;
      fin();
      WAITL0;
    };
    typedef std::integral_constant<bool, true> T_;
    typedef std::integral_constant<bool, false> F_;
    __builtin_amdgcn_s_setprio(3);
    if (first) run(T_{}); else run(F_{});
    __builtin_amdgcn_s_setprio(0);
    BAR;
    if constexpr (!HH) asm volatile(".Lmk_ep0:");
    {
      const char* xr = smem + XS + lane * 16;
#pragma unroll
      for (int i = 0; i < 2; ++i)
#pragma unroll
        for (int j = HH ? 2 : 0; j < (HH ? 4 : 6); ++j) acc[i][j] = *(const f32x4*)(xr + (i * (HH ? 8 : 6) + j) * 1024);
      if (HH) asm volatile("" : "=v"(acc[0][0]), "=v"(acc[0][1]), "=v"(acc[1][0]), "=v"(acc[1][1]));
    }
    asm volatile("" : "={v[64:79]}"(xq));
  } else {
    prep(1);
    if (!first) publish(rsW, tid, 256 + rt * 16 + hp, E + (unsigned)layer, P);
    prep(2);
    const int fl = (q4 >> 1) | ((g & 1) << 1);
    const unsigned bo = lds_addr(smem) + ABYTES + (g >> 1) * 6144 + (g & 1) * 1024 + q4 * 128 + p4 * 8;
    const unsigned bo0 = bo + (((wn * 2 + 0) ^ fl) << 5), bo1 = bo + (((wn * 2 + 1) ^ fl) << 5);
    const unsigned ar = lds_addr(smem) + (wm * 64 + fr) * 128;
    const unsigned aK0 = ar + ((g ^ (fr & 7)) << 4), aK1 = ar + (((4 + g) ^ (fr & 7)) << 4);
    if (first) {
      const int row = m0 + hp * 8 + w * 2 + (lane >> 5);
      static_assert(HH || (OFF_SP == 122880 && OFF_KS + 12288 <= OFF_JK), "addresses used at the top of the kernel");
      asm volatile("" :: "v"(aK0), "v"(aK1), "v"(bo0), "v"(bo1));
      unsigned go = (lane & 31) == 0 ? (unsigned)(WS_G0 + row * 16) : 0x7fff0000u;
      unsigned ho = WS_HL + (unsigned)(((rt & 7) * 8 + (hp >> 1)) * 16384 + (hp & 1) * 4096 + tid * 16 + (rt >> 3) * 8192);
      asm volatile("" : "+v"(go), "+v"(ho));
      WAITV(0);
      u32x4 xs[4];
      const u32x2 ev = *(const u32x2*)(smem + OFF_E);
#pragma unroll
      for (int i = 0; i < 4; ++i) xs[i] = *(const u32x4*)(smem + OFF_SP + w * 4096 + (lane >> 5) * 2048 + (lane & 31) * 16 + i * 512);
      typedef __attribute__((ext_vector_type(2))) __bf16 bf2;
      const bf2 one2 = __builtin_bit_cast(bf2, 0x3F803F80u);
      float sa[2] = {zero1(), zero1()}, sq[2] = {zero1(), zero1()};
#pragma unroll
      for (int i = 0; i < 16; ++i) {
        const unsigned u = xs[i >> 2][i & 3];
        const bf2 t = __builtin_bit_cast(bf2, u);
        sa[i & 1] = __builtin_amdgcn_fdot2_f32_bf16(t, one2, sa[i & 1], false);
        sq[i & 1] = __builtin_amdgcn_fdot2_f32_bf16(t, t, sq[i & 1], false);
      }
      float sm = r16_sum(sa[0] + sa[1]), m2 = r16_sum(sq[0] + sq[1]);
      {
        auto r = __builtin_amdgcn_permlane16_swap(__float_as_uint(sm), __float_as_uint(sm), false, false);
        auto q = __builtin_amdgcn_permlane16_swap(__float_as_uint(m2), __float_as_uint(m2), false, false);
        sm = __uint_as_float(r[0]) + __uint_as_float(r[1]); m2 = __uint_as_float(q[0]) + __uint_as_float(q[1]);
      }
      const float mean = sm * (1.0f / 1024.0f);
      m2 = fmaxf(m2 - sm * mean, 0.f);
      const unsigned tag = ev[0] + 1u;
      u32x4 gw = {__float_as_uint(-mean), tag, __float_as_uint(__builtin_amdgcn_rsqf(m2 * (1.0f / 1024.0f) + 1e-5f)), tag};
      __builtin_amdgcn_raw_buffer_store_b128(gw, rsW, go, 0, 16);
      __builtin_amdgcn_raw_buffer_store_b128(empty4(), rsW, ho, 0, 16);
      if (bid < 64 && tid == 0) __builtin_amdgcn_raw_buffer_store_b32(tag, rsW, WS_PL + (unsigned)((bid >> 5) * 8192 + (bid & 31) * 256), 0, 16);
      E = uni2(ev[0], ev[1]);
    }
    f32x32 ac0;
    f32x16 ac1;
    f32x8 av[2];
    asm volatile("" :: "v"(aK0), "v"(aK1), "v"(bo0), "v"(bo1));
    if (first) BAR;
#define G1_OUT
#define G1_T(a)
#define G1_TEND
#define G1_TCL
    asm volatile(G1_MACROS
        ".p2align 3\n"
        "G1RDB 0, %[b0], %[b1], 0\n G1RDA %[a0], %[a1], 0\n G1RDB 1, %[b0], %[b1], 0\n"
        "ds_read_b128 v[64:67], %[xa]\n ds_read_b128 v[68:71], %[xa] offset:2048\n"
        "ds_read_b128 v[72:75], %[xa] offset:4096\n ds_read_b128 v[76:79], %[xa] offset:6144\n"
        "s_cmp_eq_u32 %[lay], 0\n s_waitcnt lgkmcnt(12)\n"
        "G1SG 0, 1, 1, %[a0], %[a1], %[b0], %[b1], 0\n"
        "s_mov_b32 vcc_lo, 16\n"
        "G1SG 1, 1, 1, %[a0], %[a1], %[b0], %[b1], 40960\n"
        "G1SG 2, 1, 2, %[a0], %[a1], %[b0], %[b1], 40960\n"
        "s_cbranch_scc1 .Lg1a%=\n"
        "s_nop 0\n"
        G1_T(88)
        "s_barrier\n"
        G1_T(92)
        "G1RDA %[a0], %[a1], 40960\n"
        "s_waitcnt lgkmcnt(4)\n"
        "s_branch .Lg1b%=\n"
        ".Lg1a%=:\n"
        G1_T(88)
        "s_barrier\n"
        G1_T(92)
        "G1RDB 0, %[b0], %[b1], 40960\n G1RDA %[a0], %[a1], 40960\n G1RDB 1, %[b0], %[b1], 40960\n"
        ".p2align 3\n s_nop 0\n"
        ".Lg1b%=:\n"
        "s_waitcnt lgkmcnt(8)\n"
        "G1SG 0, 0, 1, %[a0], %[a1], %[b0], %[b1], 40960\n"
        "s_barrier\n"
        "G1SG 1, 0, 1, %[a2], %[a3], %[b2], %[b3], 0\n"
        "G1SG 2, 0, 1, %[a2], %[a3], %[b2], %[b3], 0\n"
        "s_lshr_b32 vcc_lo, vcc_lo, 1\n s_waitcnt vmcnt(0) lgkmcnt(8)\n"
        "G1SG 0, 0, 1, %[a2], %[a3], %[b2], %[b3], 0\n"
        "s_barrier\n"
        "G1SG 1, 0, 1, %[a0], %[a1], %[b0], %[b1], 0\n"
        "G1SG 2, 0, 1, %[a0], %[a1], %[b0], %[b1], 0\n"
        "s_cbranch_scc0 .Lg1c%=\n s_waitcnt lgkmcnt(8)\n"
        "G1SG 0, 0, 1, %[a0], %[a1], %[b0], %[b1], 0\n"
        "s_barrier\n"
        "G1SG 1, 0, 1, %[a0], %[a1], %[b0], %[b1], 40960\n"
        "G1SG 2, 0, 1, %[a0], %[a1], %[b0], %[b1], 40960\n"
        "s_branch .Lg1b%=\n"
        ".Lg1c%=:\n"
        "s_waitcnt lgkmcnt(8)\n"
        "G1END 1, 0, 8, 0, %[xs], %[b0], %[b1]\n"
        "s_waitcnt lgkmcnt(8)\n s_nop 0\n"
        "G1END 1, 8, 8, 0, %[xs], %[b0], %[b1]\n"
        "s_waitcnt lgkmcnt(0)\n s_nop 0\n"
        "G1END 1, 16, 8, 0, %[xs], %[b0], %[b1]\n"
        ".if %[hh] == 0\n s_nop 0\n s_barrier\n .endif\n"
        G1_T(96)
        ".if %[hh]\n G1END 0, 0, 20, 0xffff76103298, %[xs], %[b0], %[b1]\n .else\n G1END 0, 0, 18, 0xba5498327610, %[xs], %[b0], %[b1]\n .endif\n"
        "s_waitcnt lgkmcnt(0)\n s_barrier\n"
        ".if %[hh]\n G1END 0, 20, 24-20, 0xffffffffffff, %[xs], %[b0], %[b1]\n .else\n G1END 0, 18, 24-18, 0xffffffffffff, %[xs], %[b0], %[b1]\n .endif\n"
        "s_nop 7\n"
        G1_TEND G1_PURGE
        : "=&{v[160:191]}"(ac0), "=&{v[192:207]}"(ac1), "=&{v[64:79]}"(xq), "=&{v[224:231]}"(av[0]), "=&{v[248:255]}"(av[1]),
          "+{v[48:63]}"(pa), "+{v[36:39]}"(bias[0]), "+{v[40:43]}"(bias[1]), "+{v[44:47]}"(bias[2]) G1_OUT
        : [a0] "v"(aK0), [a1] "v"(aK1), [a2] "v"(aK0 + 2 * STAGE), [a3] "v"(aK1 + 2 * STAGE), [b0] "v"(bo0), [b1] "v"(bo1),
          [b2] "v"(bo0 + 2 * STAGE), [b3] "v"(bo1 + 2 * STAGE), [xs] "v"(lds_addr(smem) + XS + lane * 16), [xa] "v"(wn ? aK1 : aK0),
          [lay] "s"(layer), [hh] "n"(MODE)
        : "memory", "scc", "vcc", G1_TCL G1_CLOBBER);
    acc[0][0] = quad<0>(ac0); acc[0][1] = quad<1>(ac0); acc[0][2] = quad<2>(ac0); acc[0][3] = quad<3>(ac0); acc[0][4] = quad<4>(ac0); acc[0][5] = quad<5>(ac0);
    acc[1][0] = quad<6>(ac0); acc[1][1] = quad<7>(ac0); acc[1][2] = quad<0>(ac1); acc[1][3] = quad<1>(ac1); acc[1][4] = quad<2>(ac1); acc[1][5] = quad<3>(ac1);
    if constexpr (HH) {
      const float cs[2] = {lds<float>(pcv), lds<float>(pcv + 64)};
      f32x4 rm[4], rr[4];
#pragma unroll
      for (int rb = 0; rb < 4; ++rb) { rm[rb] = lds<f32x4>(prv + rb * 64); rr[rb] = lds<f32x4>(prv + 512 + rb * 64); }
#pragma unroll
      for (int h = 0; h < 2; ++h) {
        f32x2 f[8];
#pragma unroll
        for (int i = 0; i < 8; ++i) {
          const int c = i >> 2, rb = (i >> 1) & 1, e = (i & 1) * 2;
          const f32x4 a = h == 0 ? acc[rb][4 + c] : c ? quad<1>(av[rb]) : quad<0>(av[rb]);
          f[i] = __builtin_elementwise_fma(f32x2{rm[2 * h + rb][e], rm[2 * h + rb][e + 1]}, f32x2{cs[c], cs[c]}, f32x2{a[e], a[e + 1]});
        }
        asm volatile("" : "+v"(f[0]), "+v"(f[1]), "+v"(f[2]), "+v"(f[3]), "+v"(f[4]), "+v"(f[5]), "+v"(f[6]), "+v"(f[7]));
#pragma unroll
        for (int i = 0; i < 8; ++i) f[i] *= f32x2{rr[2 * h + ((i >> 1) & 1)][(i & 1) * 2], rr[2 * h + ((i >> 1) & 1)][(i & 1) * 2 + 1]};
        asm volatile("" : "+v"(f[0]), "+v"(f[1]), "+v"(f[2]), "+v"(f[3]), "+v"(f[4]), "+v"(f[5]), "+v"(f[6]), "+v"(f[7]));
#pragma unroll
        for (int c = 0; c < 2; ++c) lds<u32x4>(pvf + h * 8192 + c * 1024) = u32x4{pk2(f[4 * c][0], f[4 * c][1]), pk2(f[4 * c + 1][0], f[4 * c + 1][1]), pk2(f[4 * c + 2][0], f[4 * c + 2][1]), pk2(f[4 * c + 3][0], f[4 * c + 3][1])};
      }
    }
  }
  ST(layer * 24 + 4)
  {
  const int lane = lane_id(), tid = w * 64 + lane, fr = lane & 15, g = lane >> 4;
  (void)fr; (void)g;
  auto o_put = [&](int rb, const f32x4& a, const f32x4& b) {
    const u32x4 v = {pk2(a[0], a[1]), pk2(a[2], a[3]), pk2(b[0], b[1]), pk2(b[2], b[3])};
    __builtin_amdgcn_raw_buffer_store_b128(v, rsW, ogo + rb * 1024, 0, 16);
    return v;
  };
  auto marks = [&]() {
    const u32x4 pz = empty4();
    __builtin_amdgcn_raw_buffer_store_b128(pz, rsW, dm, 0, 16);
    if (w < 2 || (!HH && w >= 4)) __builtin_amdgcn_raw_buffer_store_b128(pz, rsW, dm2, 0, 16);
  };
  if (first) { P = __builtin_amdgcn_readfirstlane(*(const unsigned*)(smem + OFF_E + 256)); ogo ^= P; hof ^= P; }
  f32x4 csq[2], csk[2], rm[2], rr[2];
  float csv[2];
  f32x2 st2[2] = {lds<f32x2>(prs), lds<f32x2>(prs + 128)};
#pragma unroll
  for (int c = 0; c < 2; ++c) {
    csk[c] = lds<f32x4>(pcs + 256 + c * 64);
    if (!HH) { csv[c] = lds<float>(pcv + c * 64); rm[1 - c] = lds<f32x4>(prv + (1 - c) * 64); rr[1 - c] = lds<f32x4>(prv + 512 + (1 - c) * 64); }
  }
#pragma unroll
  for (int c = 0; c < 2; ++c) csq[c] = lds<f32x4>(pcs + c * 64);
  if constexpr (!HH) {
    auto fold = [&](const f32x4& a, const f32x4& cs, const f32x2& st, bool scale) {
      f32x2 lo = __builtin_elementwise_fma(f32x2{cs[0], cs[1]}, f32x2{st[0], st[0]}, f32x2{a[0], a[1]});
      f32x2 hi = __builtin_elementwise_fma(f32x2{cs[2], cs[3]}, f32x2{st[0], st[0]}, f32x2{a[2], a[3]});
      if (scale) { lo *= f32x2{st[1], st[1]}; hi *= f32x2{st[1], st[1]}; }
      return u32x2{pk2(lo[0], lo[1]), pk2(hi[0], hi[1])};
    };
    auto cat2 = [](u32x2 a, u32x2 b) { return __builtin_bit_cast(bf16x8, u32x4{a[0], a[1], b[0], b[1]}); };
    auto foldv = [&](int rb, int c) {
      const f32x4& a = acc[rb][4 + c];
      const f32x2 lo = __builtin_elementwise_fma(f32x2{rm[rb][0], rm[rb][1]}, f32x2{csv[c], csv[c]}, f32x2{a[0], a[1]}) * f32x2{rr[rb][0], rr[rb][1]};
      const f32x2 hi = __builtin_elementwise_fma(f32x2{rm[rb][2], rm[rb][3]}, f32x2{csv[c], csv[c]}, f32x2{a[2], a[3]}) * f32x2{rr[rb][2], rr[rb][3]};
      return u32x2{pk2(lo[0], lo[1]), pk2(hi[0], hi[1])};
    };
    bf16x8 qf[2], kf[2];
    u32x2 vt[3][2];
#pragma unroll
    for (int rb = 1; rb >= 0; --rb) {
      kf[rb] = cat2(fold(acc[rb][2], csk[0], st2[rb], true), fold(acc[rb][3], csk[1], st2[rb], true));
      const u32x2 va0 = vt[rb + 1][0] = foldv(rb, 0), va1 = vt[rb + 1][1] = foldv(rb, 1);
      if (rb == 1) {
        if (R0 == 96) {
          u32x4 hk = __builtin_bit_cast(u32x4, kf[1]), hv = {va0[0], va0[1], va1[0], va1[1]};
#pragma unroll
          for (int e = 0; e < 4; ++e) { hk[e] = hk[e] < EMPTY - 1u ? hk[e] : EMPTY - 1u; hv[e] = hv[e] < EMPTY - 1u ? hv[e] : EMPTY - 1u; }
          __builtin_amdgcn_raw_buffer_store_b128(hk, rsW, hof, 0, 16);
          __builtin_amdgcn_raw_buffer_store_b128(hv, rsW, hof + 1024, 0, 16);
        } else {
          lds<bf16x8>(pkw + 4096) = kf[1];
          lds<u32x2>(pkw + 5120) = va0; lds<u32x2>(pkw + 5128) = va1;
        }
        asm volatile("" : "+v"(st2[0]), "+v"(st2[1]), "+v"(csv[0]), "+v"(csv[1]));
      }
    }
#pragma unroll
    for (int rb = 0; rb < 2; ++rb) qf[rb] = cat2(fold(acc[rb][0], csq[0], st2[rb], false), fold(acc[rb][1], csq[1], st2[rb], false));
    f32x4 sc[2][2];
    sc[1][0] = MFMA(kf[0], qf[1], bias[0]);
    sc[1][1] = MFMA(kf[1], qf[1], bias[1]);
    sc[0][1] = MFMA(kf[0], qf[0], bias[1]);
    constexpr float SCL = 0.17677669529663687f * 1.4426950408889634f;
    const bf16x8 ones = ones8();
    auto attn = [&](int rb) {
      const f32x4 z4 = {0, 0, 0, 0};
      const float scl = st2[rb][1] * SCL;
      asm volatile("s_nop 7" : "+v"(sc[rb][0]), "+v"(sc[rb][1]));
      unsigned m = __float_as_uint(vmax3f_n(vmax3f(sc[rb][0][0], sc[rb][0][1], sc[rb][0][2]), vmax3f(sc[rb][0][3], sc[rb][1][0], sc[rb][1][1]), vmaxf(sc[rb][1][2], sc[rb][1][3])));
      auto r = __builtin_amdgcn_permlane16_swap(m, m, false, false);
      m = __float_as_uint(vmaxf_n(__uint_as_float(r[0]), __uint_as_float(r[1])));
      auto r2 = __builtin_amdgcn_permlane32_swap(m, m, false, false);
      const float mxs = -vmaxf(__uint_as_float(r2[0]), __uint_as_float(r2[1])) * scl;
      u32x4 pv;
#pragma unroll
      for (int i = 0; i < 4; ++i) {
        const f32x2 t = __builtin_elementwise_fma(f32x2{sc[rb][i >> 1][(i & 1) * 2], sc[rb][i >> 1][(i & 1) * 2 + 1]}, f32x2{scl, scl}, f32x2{mxs, mxs});
        pv[i] = pk2(__builtin_amdgcn_exp2f(t[0]), __builtin_amdgcn_exp2f(t[1]));
      }
      const bf16x8 pf = __builtin_bit_cast(bf16x8, pv);
      const f32x4 den = MFMA(ones, pf, z4);
      const f32x4 o0 = MFMA(cat2(vt[rb][0], vt[rb + 1][0]), pf, z4), o1 = MFMA(cat2(vt[rb][1], vt[rb + 1][1]), pf, z4);
      const float inv = __builtin_amdgcn_rcpf(den[0]);
      return o_put(rb, o0 * inv, o1 * inv);
    };
    u32x4 hk, hv;
    auto ld_h = [&]() {
      asm volatile("s_nop 7\n\tbuffer_load_dwordx4 %0, %2, %3, 0 offen sc1\n\tbuffer_load_dwordx4 %1, %2, %3, 0 offen offset:1024 sc1" : "=&v"(hk), "=&v"(hv) : "v"(hof), "s"(rsW) : "memory");
    };
    asm volatile("" : "+v"(sc[0][1]));
    if (R0 == 0) ld_h();
    const u32x4 ov = attn(1);
    if (R0 == 0) {
      WAITL0; BAR;
      WAITV(1);
      _Pragma("unroll 1") for (unsigned spins = 0; spins < (1u << 18); ++spins) {
        __builtin_amdgcn_sched_barrier(0); asm volatile("" : "+v"(hk), "+v"(hv));
        unsigned mxw = 0;
#pragma unroll
        for (int e = 0; e < 4; ++e) { mxw = hk[e] > mxw ? hk[e] : mxw; mxw = hv[e] > mxw ? hv[e] : mxw; }
        if (__all(mxw != EMPTY)) break;
        ld_h();
        WAITV(0);
      }
      asm volatile("" : "+v"(hk), "+v"(hv));
      if (first) marks();
    } else {
      if (first) marks();
      if (first && tid >= 128 && tid < 176) __builtin_amdgcn_raw_buffer_store_b128(*(const u32x4*)(smem + OFF_CS + (tid - 128) * 16), rsW, WS_CSA + (unsigned)((rt * 16 + hp) * 1024 + (tid - 128) * 16), 0, 16);
      WAITL0; BAR;
      asm volatile("" ::: "memory");
      hk = lds<u32x4>(pkw); hv = lds<u32x4>(pkw + 1024);
    }
    ST(layer * 24 + 5)
    asm volatile("" ::: "memory");
    vt[0][0] = u32x2{hv[0], hv[1]}; vt[0][1] = u32x2{hv[2], hv[3]};
    sc[0][0] = MFMA(__builtin_bit_cast(bf16x8, hk), qf[0], bias[2]);
    lds<u32x4>(opo + 1024) = ov;
    lds<u32x4>(opo) = attn(0);
  } else {
    bf16x8 wf[2];
#pragma unroll
    for (int fb = 0; fb < 2; ++fb) wf[fb] = cat8(tr_read(pwf + fb * 32), tr_read(pwf + fb * 32 + 16 * 64));
    __builtin_amdgcn_sched_barrier(0);
    if (layer == 1) marks();
    const f32x4 z4 = {0, 0, 0, 0};
    constexpr float L2E = 1.4426950408889634f;
    const unsigned kvo = WS_KV + (layer >> 1) * KVLB, kfo = WS_KF + (layer >> 1) * KFLB + hp * 16384, kzo = WS_KF + (layer >> 1) * KFLB + 16 * 16384 + hp * 256;
    bf16x8 qp[2][2];
    auto phi2 = [&](auto qktag) {
      constexpr int QK = decltype(qktag)::value & 1;
      constexpr int RB0 = (decltype(qktag)::value >> 1) & 1, RB1 = (decltype(qktag)::value & 4) ? RB0 + 1 : 2;
      float pv[2][8];
      f32x4 pm[2][2];
      f32x2 f[2][4];
#pragma unroll
      for (int rb = RB0; rb < RB1; ++rb) {
        asm volatile("" : "+v"(st2[rb]));
        const f32x2 nm = {st2[rb][0], st2[rb][0]};
#pragma unroll
        for (int i = 0; i < 4; ++i) {
          const f32x4& a = acc[rb][2 * QK + (i >> 1)];
          const f32x4& cs = QK ? csk[i >> 1] : csq[i >> 1];
          f[rb][i] = __builtin_elementwise_fma(f32x2{cs[(i & 1) * 2], cs[(i & 1) * 2 + 1]}, nm, f32x2{a[(i & 1) * 2], a[(i & 1) * 2 + 1]});
        }
      }
#pragma unroll
      for (int rb = RB0; rb < RB1; ++rb) asm volatile("" : "+v"(f[rb][0]), "+v"(f[rb][1]), "+v"(f[rb][2]), "+v"(f[rb][3]));
#pragma unroll
      for (int rb = RB0; rb < RB1; ++rb) {
        const bf16x8 xf = __builtin_bit_cast(bf16x8, u32x4{pk2(f[rb][0][0], f[rb][0][1]), pk2(f[rb][1][0], f[rb][1][1]), pk2(f[rb][2][0], f[rb][2][1]), pk2(f[rb][3][0], f[rb][3][1])});
        pm[rb][0] = MFMA(wf[0], xf, z4); pm[rb][1] = MFMA(wf[1], xf, z4);
      }
      if (QK) asm volatile("s_nop 7" : "+v"(pm[0][0]), "+v"(pm[0][1]), "+v"(pm[1][0]), "+v"(pm[1][1]));
      else asm volatile("s_nop 7" : "+v"(pm[RB0][0]), "+v"(pm[RB0][1]));
#pragma unroll
      for (int rb = RB0; rb < RB1; ++rb)
#pragma unroll
        for (int i = 0; i < 4; ++i) { pv[rb][i] = pm[rb][0][i]; pv[rb][4 + i] = pm[rb][1][i]; }
      float mx[2], mn[2];
#pragma unroll
      for (int u = RB0; u < RB1; ++u) max8pn(pm[u][0], pm[u][1], mx[u], mn[u]);
      if (RB1 - RB0 == 2) xr4<true>(mx[0], mn[0], mx[1], mn[1]); else xr2<true>(mx[RB0], mn[RB0]);
      float ep[2][8], en[2][8], sp[2], sn[2];
#pragma unroll
      for (int u = RB0; u < RB1; ++u) {
        const float c = st2[u][1] * L2E, mxs = -mx[u] * c, mns = -mn[u] * c;
#pragma unroll
        for (int i = 0; i < 4; ++i) {
          const f32x2 p2 = {pv[u][2 * i], pv[u][2 * i + 1]};
          const f32x2 tp = __builtin_elementwise_fma(p2, f32x2{c, c}, f32x2{mxs, mxs});
          const f32x2 tn = __builtin_elementwise_fma(-p2, f32x2{c, c}, f32x2{mns, mns});
          ep[u][2 * i] = __builtin_amdgcn_exp2f(tp[0]); ep[u][2 * i + 1] = __builtin_amdgcn_exp2f(tp[1]);
          en[u][2 * i] = __builtin_amdgcn_exp2f(tn[0]); en[u][2 * i + 1] = __builtin_amdgcn_exp2f(tn[1]);
        }
        const f32x2 a2 = (f32x2{ep[u][0], ep[u][1]} + f32x2{ep[u][2], ep[u][3]}) + (f32x2{ep[u][4], ep[u][5]} + f32x2{ep[u][6], ep[u][7]});
        const f32x2 b2 = (f32x2{en[u][0], en[u][1]} + f32x2{en[u][2], en[u][3]}) + (f32x2{en[u][4], en[u][5]} + f32x2{en[u][6], en[u][7]});
        sp[u] = a2[0] + a2[1]; sn[u] = b2[0] + b2[1];
      }
      if (RB1 - RB0 == 2) xr4<false>(sp[0], sn[0], sp[1], sn[1]); else xr2<false>(sp[RB0], sn[RB0]);
#pragma unroll
      for (int rb = RB0; rb < RB1; ++rb) {
        float ip = __builtin_amdgcn_rcpf(sp[rb]), in = __builtin_amdgcn_rcpf(sn[rb]);
        u32x2 o0, o1, o2, o3;
        o0[0] = pk2(ep[rb][0] * ip, ep[rb][1] * ip); o0[1] = pk2(ep[rb][2] * ip, ep[rb][3] * ip);
        o1[0] = pk2(ep[rb][4] * ip, ep[rb][5] * ip); o1[1] = pk2(ep[rb][6] * ip, ep[rb][7] * ip);
        o2[0] = pk2(en[rb][0] * in, en[rb][1] * in); o2[1] = pk2(en[rb][2] * in, en[rb][3] * in);
        o3[0] = pk2(en[rb][4] * in, en[rb][5] * in); o3[1] = pk2(en[rb][6] * in, en[rb][7] * in);
        if (QK == 0) {
          qp[rb][0] = __builtin_bit_cast(bf16x8, (u32x4){o0[0], o0[1], o1[0], o1[1]});
          qp[rb][1] = __builtin_bit_cast(bf16x8, (u32x4){o2[0], o2[1], o3[0], o3[1]});
          asm volatile("" : "+v"(qp[rb][0]), "+v"(qp[rb][1]));
        } else {
          const unsigned dst = ppk + rb * 16 * PSTR;
          lds<u32x2>(dst) = o0; lds<u32x2>(dst + 32) = o1; lds<u32x2>(dst + 64) = o2; lds<u32x2>(dst + 96) = o3;
        }
      }
    };
    phi2(std::integral_constant<int, 1>{});
    bf16x8 one8 = ones8();
    asm volatile("" : "+v"(one8));
    WAITL0; BAR;
    ST(layer * 24 + 5)
    {
      bf16x8 af[4], v0[4], v1[4];
      asm volatile("" ::: "memory");
#pragma unroll
      for (int sw = 0; sw < 4; ++sw) {
        af[sw] = cat8(tr_read(ppa + sw * (64 * PSTR)), tr_read(ppa + sw * (64 * PSTR) + 16 * PSTR));
        v0[sw] = lds<bf16x8>(pvr + sw * 4096);
        v1[sw] = lds<bf16x8>(pvr + sw * 4096 + 1024);
      }
      __builtin_amdgcn_sched_barrier(0);
      f32x4 kv[3] = {z4, z4, z4};
#pragma unroll
      for (int sw = 0; sw < 4; ++sw) {
        kv[0] = MFMA(af[sw], v0[sw], kv[0]);
        kv[1] = MFMA(af[sw], v1[sw], kv[1]);
        kv[2] = MFMA(af[sw], one8, kv[2]);
      }
      auto pc = [](float x, float y) { const unsigned t = pk2(x, y); return t < EMPTY - 1u ? t : EMPTY - 1u; };
      __builtin_amdgcn_raw_buffer_store_b128(u32x4{pc(kv[0][0], kv[0][1]), pc(kv[0][2], kv[0][3]), pc(kv[1][0], kv[1][1]), pc(kv[1][2], kv[1][3])}, rsW, gkv, 0, 16);
      __builtin_amdgcn_raw_buffer_store_b64(u32x2{pc(kv[2][0], kv[2][1]), pc(kv[2][2], kv[2][3])}, rsW, gkz, 0, 16);
      __builtin_amdgcn_sched_barrier(0);
      if (kg) {
#pragma unroll
        for (int i = 0; i < 4; ++i) acc[i >> 1][i & 1] = lds<f32x4>(sb + XS + lane * 16 + ((i >> 1) * 8 + (i & 1)) * 1024);
      }
    }
    ST(layer * 24 + 6)
    u32x4 l1[8];
    const bool red = w == 7 || (w == 6 && (rt & 7) == 0);
    unsigned lo = kvo + (unsigned)((hp + (lane >> 5) * 128) * 16384) + ((((rt >> 3) ^ (lane >> 5) ^ 1) & 1) * 8192 ^ P) +
                  (w == 7 ? (unsigned)((rt & 4) * 512 + (g & 1) * 1024 + (rt & 3) * 256 + fr * 16) : (unsigned)(4096 + (lane & 7) * 16));
    unsigned fs = w == 7 ? kfo + (unsigned)((rt >> 2) * 2048 + (lane >> 5) * 1024 + (rt & 3) * 256 + fr * 16 + (g & 1) * 8)
                : (lane & 24) ? 0x7fff0000u : kzo + (unsigned)((rt >> 3) * 128 + (lane & 4) * 16 + (lane & 1) * 32 + (lane >> 5) * 16 + (lane & 2) * 4);
    auto l1_load = [&]() {
      asm volatile("" ::: "memory");
#pragma unroll
      for (int p = 0; p < 8; ++p) l1[p] = __builtin_amdgcn_raw_buffer_load_b128(rsW, lo, p * (16 * 16384), 16);
      __builtin_amdgcn_sched_barrier(0);
    };
    if (red) asm volatile("" : "+v"(lo), "+v"(fs));
    BAR;
    if (red) {
      typedef __attribute__((ext_vector_type(2))) __bf16 bf2;
      unsigned c0 = 0x00003F80u, c1 = 0x3F800000u;
      asm volatile("" : "+v"(c0), "+v"(c1));
      __builtin_amdgcn_s_setprio(3);
      __builtin_amdgcn_s_sleep(5);
      _Pragma("unroll 1") for (unsigned spins = 0; spins < (1u << 18); ++spins) {
        l1_load();
        unsigned mxw = 0;
#pragma unroll
        for (int p = 0; p < 8; ++p)
#pragma unroll
          for (int e = 0; e < 4; ++e) mxw = l1[p][e] > mxw ? l1[p][e] : mxw;
        if (__all(mxw != EMPTY)) break;
      }
      float s4[4];
      float a[8] = {zero1(), zero1(), zero1(), zero1(), zero1(), zero1(), zero1(), zero1()};
#pragma unroll
      for (int p = 0; p < 8; ++p)
#pragma unroll
        for (int e = 0; e < 4; ++e) {
          const unsigned u = l1[p][e];
          a[2 * e] = __builtin_amdgcn_fdot2_f32_bf16(__builtin_bit_cast(bf2, u), __builtin_bit_cast(bf2, c0), a[2 * e], false);
          a[2 * e + 1] = __builtin_amdgcn_fdot2_f32_bf16(__builtin_bit_cast(bf2, u), __builtin_bit_cast(bf2, c1), a[2 * e + 1], false);
        }
#pragma unroll
      for (int i = 0; i < 4; ++i) {
        auto r = __builtin_amdgcn_permlane32_swap(__float_as_uint(a[i]), __float_as_uint(a[i + 4]), false, false);
        s4[i] = __uint_as_float(r[0]) + __uint_as_float(r[1]);
      }
      unsigned o0 = pk2(s4[0], s4[1]), o1 = pk2(s4[2], s4[3]);
      o0 = o0 < EMPTY - 1u ? o0 : EMPTY - 1u; o1 = o1 < EMPTY - 1u ? o1 : EMPTY - 1u;
      __builtin_amdgcn_raw_buffer_store_b64(u32x2{o0, o1}, rsW, fs, 0, 16);
      __builtin_amdgcn_raw_buffer_store_b64(u32x2{o0, o1}, rsW, fs, 8192, 16);
    }
    phi2(std::integral_constant<int, 4>{});
    ST(layer * 24 + 7)
    phi2(std::integral_constant<int, 6>{});
    __builtin_amdgcn_s_setprio(0);
    ST(layer * 24 + 8)
    ST(layer * 24 + 9)
    {
      unsigned pfo = OFF_KV + wn * 4096 + lane * 16, pzo = OFF_KV + 8192 + wn * 128 + g * 16;
      asm volatile("" : "+v"(pfo), "+v"(pzo));
      if (w < 3) {
        u32x4 fa[3];
        unsigned fo = kfo + (unsigned)(w * 3072 + lane * 16) + ((rt >> 3) * 8192 ^ P);
        unsigned fo2 = w < 2 ? fo + 2048 : kzo + (unsigned)((lane & 15) * 16) + ((rt >> 3) * 8192 ^ P);
        unsigned lo1 = sb + OFF_KV + w * 3072 + lane * 16, lo2 = w < 2 ? lo1 + 2048 : sb + OFF_KV + 8192 + (lane & 15) * 16;
        __builtin_amdgcn_s_sleep(8);
        _Pragma("unroll 1") for (unsigned spins = 0; spins < (1u << 18); ++spins) {
          asm volatile("" ::: "memory");
          fa[0] = __builtin_amdgcn_raw_buffer_load_b128(rsW, fo, 0, 16);
          fa[1] = __builtin_amdgcn_raw_buffer_load_b128(rsW, fo + 1024, 0, 16);
          fa[2] = __builtin_amdgcn_raw_buffer_load_b128(rsW, fo2, 0, 16);
          unsigned mxw = 0;
#pragma unroll
          for (int e = 0; e < 12; ++e) mxw = fa[e >> 2][e & 3] > mxw ? fa[e >> 2][e & 3] : mxw;
          if (__all(mxw != EMPTY)) break;
          __builtin_amdgcn_s_sleep(1);
        }
        lds<u32x4>(lo1) = fa[0]; lds<u32x4>(lo1 + 1024) = fa[1]; lds<u32x4>(lo2) = fa[2];
      }
      ST(layer * 24 + 10)
      WAITL0; BAR;
      ST(layer * 24 + 11)
      bf16x8 kvf[3][2];
#pragma unroll
      for (int ks = 0; ks < 2; ++ks) {
        kvf[0][ks] = *(const bf16x8*)(smem + pfo + ks * 2048);
        kvf[1][ks] = *(const bf16x8*)(smem + pfo + ks * 2048 + 1024);
        kvf[2][ks] = *(const bf16x8*)(smem + pzo + ks * 64);
      }
#pragma unroll
      for (int rb = 0; rb < 2; ++rb) {
        f32x4 oa[3];
#pragma unroll
        for (int nb = 2; nb >= 0; --nb) {
          oa[nb] = MFMA(kvf[nb][0], qp[rb][0], z4);
          oa[nb] = MFMA(kvf[nb][1], qp[rb][1], oa[nb]);
        }
        float den = oa[2][0];
        den += 1e-6f;
        float inv = __builtin_amdgcn_rcpf(den);
        lds<u32x4>(opo + rb * 1024) = o_put(rb, oa[0] * inv, oa[1] * inv);
      }
    }
  }
  ST(layer * 24 + 12)
  if (w >= 6) {
    const unsigned v0 = (unsigned)((((w & 1) * 32 + (lane >> 3)) * DM + hp * 64 + ((lane & 7) ^ ((lane >> 4) << 1)) * 8) * 2);
    const u64 ko = korder(bid & 3, hp);
#pragma unroll
    for (int t = 6; t < 12; ++t) glds4v(wo + (size_t)knib(ko, t) * 64 * DM, v0, v0 + 15360u, v0 + 2 * 15360u, v0 + 3 * 15360u, sb + OFF_WO + t * 8192 + (w & 1) * 4096);
    WAITV(24); WAITL0; BAR;
  } else if (w >= 4) { WAITV(0); WAITL0; BAR; }
  if (first && w >= 4 && w < 7) {
    typedef __attribute__((ext_vector_type(2))) __bf16 bf2;
    unsigned c0 = 0x00003F80u, c1 = 0x3F800000u;
    asm volatile("" : "+v"(c0), "+v"(c1));
    const bf2 lo1 = __builtin_bit_cast(bf2, c0), hi1 = __builtin_bit_cast(bf2, c1);
    float a[8] = {zero1(), zero1(), zero1(), zero1(), zero1(), zero1(), zero1(), zero1()};
#pragma unroll
    for (int i = 0; i < 8; ++i) {
      asm volatile("" : "+v"(pw[i]));
#pragma unroll
      for (int e = 0; e < 4; ++e) {
        const unsigned u = pw[i][e];
        a[2 * e] = __builtin_amdgcn_fdot2_f32_bf16(__builtin_bit_cast(bf2, u), lo1, a[2 * e], false);
        a[2 * e + 1] = __builtin_amdgcn_fdot2_f32_bf16(__builtin_bit_cast(bf2, u), hi1, a[2 * e + 1], false);
      }
    }
#pragma unroll
    for (int i = 0; i < 4; ++i) {
      auto r = __builtin_amdgcn_permlane32_swap(__float_as_uint(a[i]), __float_as_uint(a[i + 4]), false, false);
      a[i] = __uint_as_float(r[0]) + __uint_as_float(r[1]);
    }
#pragma unroll
    for (int i = 0; i < 2; ++i) {
      auto r = __builtin_amdgcn_permlane16_swap(__float_as_uint(a[i]), __float_as_uint(a[i + 2]), false, false);
      a[i] = __uint_as_float(r[0]) + __uint_as_float(r[1]);
      a[i] += dpp_ror<8>(a[i]);
    }
    if (!(lane & 8))
      __builtin_amdgcn_raw_buffer_store_b64(u32x2{__float_as_uint(a[0]), __float_as_uint(a[1])}, rsW,
          (unsigned)(WS_CSP + ((((hp + (1 << (w - 4))) & 15) * 16 + rt) * 192 + (w - 4) * 64 + (lane & 7) * 8 + (lane >> 5) * 4 + ((lane >> 4) & 1) * 2) * 4), 0, 16);
  }
  }
}

#undef pcs
#undef prs
#undef opo
#undef ogo
#undef pkw
#undef hof
#undef dm
#undef dm2
#undef pcv
#undef pwf
#undef prv
#undef pvf
#undef ppk
#undef ppa
#undef pvr
#undef gkv
#undef gkz

__device__ __forceinline__ void g2_phase(char* smem, char* ws, unsigned P, u32x4& nv, u64& nk, u64 E, int w, f32x16& xq, int layer, const u16* __restrict__ x0,
                                         const u16* __restrict__ wo, const u16* __restrict__ wnx, u16* __restrict__ out) {
  IDS
  wnx = uni(wnx);
  const unsigned sb = lds_addr(smem);
  const int lw = w & 3, k2 = 16 * lw + (lane >> 3);
  const unsigned vW0 = (unsigned)((k2 * DM + hp * 64 + ((lane & 7) ^ ((lane >> 4) << 1)) * 8) * 2), vW1 = vW0 + 8 * DM * 2 - 1024u;
  const int r0 = w * 16 + (lane >> 3);
  unsigned pxr = sb + r0 * 128 + (lane & 7) * 16;
  const unsigned rmo = (unsigned)(((m0 + r0) * DM + hp * 64 + ((lane & 7) ^ (lane >> 3)) * 8) * 2);
  const unsigned imo = SX + (unsigned)(hp * 32768 + (w >> 2) * 8192 + r0 * 128 + (lane & 7) * 16);
  constexpr int S0 = 0, S1 = 16384, S2 = 32768, S3 = 147456, S4 = OFF_WO, S5 = 98304, S6 = 114688;
  constexpr int AS[16] = {S0, S1, S2, S3, S0, S1, S4, S2, S3, S0, S1, S4, S5, S3, S0, S6};
  constexpr int OFF_C = 0;
  auto bs = [](int t) { return OFF_WO + (t < 12 ? t : t - 10) * 8192; };
  if (w >= 4) {
    constexpr int WT[15] = {0, 8, 12, 14, 16, 18, 20, 18, 16, 14, 12, 12, 10, 10, 8};
    const char* o = ws + WS_O + layer * (SEQ * DM * 2) + SX;
    const unsigned vA = (unsigned)((lw >> 1) * 16384 + (lw & 1) * 4096 + lane * 16);
    const u64 kord = korder(bid & 3, hp);
    auto ldA = [&](int s) { glds4c(o + knib(kord, s) * 32768, vA, sb + AS[s] + lw * 4096); };
    auto ldW = [&](int t) { glds2v(wo + (size_t)knib(kord, t) * 64 * DM, vW0, vW1, sb + bs(t) + lw * 2048); };
    const int fB = (k2 >> 1) & 1;
    unsigned vB0 = (unsigned)((k2 * 3072 + hp * 64 + (((((lane & 7) >> 1) ^ fB) << 1) | (lane & 1)) * 8) * 2);
    unsigned vB1 = (unsigned)(((k2 + 8) * 3072 + hp * 64 + (((((lane & 7) >> 1) ^ (fB | 2)) << 1) | (lane & 1)) * 8) * 2) - 1024u;
    nk = korder(kv1(bid), hp);
    const int kn1 = knib(nk, 1);
    nv = u32x4{vB0, vB1, SX + vA, lane < 16 && ((kfirst(nk) >> lane) & 1) ? (WS_FLAGS + (unsigned)(256 + rt * 16 + lane) * FLS) ^ P : 0u};
    asm volatile("" : "+v"(nv));
    asm volatile("" : "+s"(nk));
    vB0 = nv[0]; vB1 = nv[1];
    auto ldB = [&](int s, int part) {
      const u16* b = wnx + (size_t)(s ? kn1 : hp) * 64 * 3072;
      if (part) glds4v(b, vB0, vB1, vB0, vB1, sb + s * 40960 + 16384 + lw * 6144);
      else glds2v(b + 2048, vB0, vB1, sb + s * 40960 + 16384 + lw * 6144 + 4096);
    };
    BAR;
    ldA(1); ldA(2); ldA(3); ldA(4);
    WAITV(12);
    BAR;
    auto step = [&](auto ktc) {
      constexpr int kt = decltype(ktc)::value;
      WAITVN(WT[kt]);
      BAR;
      if constexpr (kt == 1) { ldA(5); ldA(6); }
      if constexpr (kt >= 2 && kt <= 10) ldA(kt + 5);
      if constexpr (kt >= 2 && kt <= 5) ldW(kt + 10);
      if constexpr (kt >= 11) ldB((kt - 11) >> 1, (kt - 11) & 1);
      if constexpr (kt >= 8) { __builtin_amdgcn_s_sleep(kt >= 11 ? 2 : 1); if (layer == 1) __builtin_amdgcn_s_sleep(1); }
    };
    step(IC<1>{}); step(IC<2>{}); step(IC<3>{}); step(IC<4>{}); step(IC<5>{}); step(IC<6>{}); step(IC<7>{});
    step(IC<8>{}); step(IC<9>{}); step(IC<10>{}); step(IC<11>{}); step(IC<12>{}); step(IC<13>{}); step(IC<14>{});
  } else {
    const int wm = w >> 1, wn = w & 1;
    f32x4 acc[4][2];
    unsigned co[2];
#pragma unroll
    for (int cb = 0; cb < 2; ++cb) co[cb] = sb + OFF_C + (unsigned)((wm * 64 + fr) * 128 + (((wn * 4 + cb * 2 + (g >> 1)) ^ (fr & 7)) << 4) + (g & 1) * 8);
    const int fl = (q4 >> 1) | ((g & 1) << 1);
    const int bb0 = (4 * g + q4) * 128 + p4 * 8 + (((wn * 2) ^ fl) << 5), bb1 = (4 * g + q4) * 128 + p4 * 8 + (((wn * 2 + 1) ^ fl) << 5);
    const int abase = wm * 4096 + lane * 16;
    bf16x8 fa[2][2][4], fb[2][2][2];
    unsigned ra[3], rb0[2], rb1[2];
#pragma unroll
    for (int i = 0; i < 3; ++i) { ra[i] = abase + i * 65536; asm volatile("" : "+v"(ra[i])); }
#pragma unroll
    for (int i = 0; i < 2; ++i) { rb0[i] = bb0 + OFF_WO + i * 49152; rb1[i] = bb1 + OFF_WO + i * 49152; asm volatile("" : "+v"(rb0[i]), "+v"(rb1[i])); }
    auto loadf = [&](int kt, bf16x8 (&a)[2][4], bf16x8 (&b)[2][2]) {
#pragma unroll
      for (int s = 0; s < 2; ++s) {
        const char* sA = smem + ra[AS[kt] >> 16] + (AS[kt] & 65535) + s * 8192;
        const int hi = bs(kt) >= OFF_WO + 49152, ob = bs(kt) - OFF_WO - hi * 49152 + s * 4096;
        b[s][0] = cat8(tr_read(smem + rb0[hi] + ob), tr_read(smem + rb0[hi] + ob + 2048));
        b[s][1] = cat8(tr_read(smem + rb1[hi] + ob), tr_read(smem + rb1[hi] + ob + 2048));
#pragma unroll
        for (int rb = 0; rb < 4; ++rb) a[s][rb] = *(const bf16x8*)(sA + rb * 1024);
      }
    };
    auto mma = [&](bf16x8 (&a)[2][4], bf16x8 (&b)[2][2]) {
#pragma unroll
      for (int i = 0; i < 16; ++i) {
        const int rb = i >> 2, cb = ((i >> 1) ^ rb) & 1, s = ((i + 1) >> 1) & 1;
        acc[rb][cb] = MFMA(b[s][cb], a[s][rb], acc[rb][cb]);
      }
    };
    unsigned fo = lane < 16 && lane != hp ? (WS_FLAGS + (unsigned)(rt * 16 + lane) * FLS) ^ P : 0u;
    const u64 ko = korder(bid & 3, hp);
    unsigned kf = kfirst(ko), kf2 = kfirst(ko >> 16) | (1u << knib(ko, 9));
    unsigned jo = (unsigned)((((rt & 7) * 128 + (w - 1) * 64 + lane) * 3072 + (hp & 3) * 1024 + (hp | 3) * 64) * 2);
    unsigned fo1 = ((kf >> (lane & 31)) & 1) ? fo : 0u, fo2 = ((kf2 >> (lane & 31)) & 1) ? fo : 0u;
    fo = (((kf | kf2) >> (lane & 31)) & 1) ? 0u : fo;
    asm volatile("" : "+v"(fo), "+v"(fo1), "+v"(fo2));
    asm volatile("" : "+s"(kf), "+s"(kf2));
    publish(rsW, tid, rt * 16 + hp, E + (unsigned)(layer + 1), P);
    ST(layer * 24 + 13)
    u64 fl1 = 0;
    if (w == 0) asm volatile("global_load_dwordx2 %0, %1, %2 sc1" : "+v"(fl1) : "v"(fo1), "s"(ws) : "memory");
    if (layer == 0) {
      const unsigned xo = (unsigned)(((m0 + wm * 64 + fr) * DM + hp * 64 + wn * 32 + g * 8) * 2);
      asm volatile("global_load_dwordx4 v[64:67], %1, %3\n\tglobal_load_dwordx4 v[68:71], %1, %4\n\tglobal_load_dwordx4 v[72:75], %2, %3\n\tglobal_load_dwordx4 v[76:79], %2, %4"
                   : "+{v[64:79]}"(xq) : "v"(xo), "v"(xo + 32 * DM * 2), "s"(x0), "s"(x0 + 16 * DM) : "memory");
    }
    loadf(0, fa[0], fb[0]);
    unsigned junk = lane;
    if (layer < 3 && (hp & 7) < 3 && (w == 1 || w == 2))
      asm volatile("global_load_dword %0, %1, %2" : "+v"(junk) : "v"(jo), "s"(wnx) : "memory");
    if (layer == 0 && w == 3) {
      CODE_TOUCH(2, ".Lmk_g1b", ".Lmk_g1a")
      asm volatile("global_load_dword %0, %1, %2" : "+v"(junk) : "v"(v), "s"(pc) : "memory");
    }
    if (layer == 1 && w == 3) {
      CODE_TOUCH(3, ".Lmk_f0", ".Lmk_f1")
      asm volatile("global_load_dword %0, %1, %2" : "+v"(junk) : "v"(v), "s"(pc) : "memory");
    }
    __builtin_amdgcn_sched_barrier(0);
    bf16x8 id[2];
#pragma unroll
    for (int cb = 0; cb < 2; ++cb) {
      const unsigned one = (g >> 1) == cb && (g & 1) == (fr >> 3) ? 0x3F80u << ((fr & 1) * 16) : 0u;
      u32x4 t;
#pragma unroll
      for (int j = 0; j < 4; ++j) t[j] = ((fr >> 1) & 3) == j ? one : 0u;
      id[cb] = __builtin_bit_cast(bf16x8, t);
      asm volatile("" : "+v"(id[cb]));
    }
    asm volatile("" : "+v"(co[0]), "+v"(co[1]));
#pragma unroll
    for (int rb = 0; rb < 4; ++rb) { acc[rb][0] = f32x4{0, 0, 0, 0}; acc[rb][1] = f32x4{0, 0, 0, 0}; }
    mma(fa[0], fb[0]);
    __builtin_amdgcn_sched_barrier(0);
    WAITL0;
    u64 miss = 0;
    if (w == 0) { ARRIVED("+v"(fl1)); if (__ballot(fl1 != E + (unsigned)(layer + 1)) & kf) poll_eq(ws, fo1, E + (unsigned)(layer + 1), kf, kf); miss = 0xffffu & ~(kf | kf2 | (1u << hp)); }
    ST(layer * 24 + 14)
    __builtin_amdgcn_sched_barrier(0);
    BAR;
    __builtin_amdgcn_sched_barrier(0);
    asm volatile("" ::: "memory");
    if (layer == 0) WAITV(0);
    __builtin_amdgcn_sched_barrier(0);
    asm volatile("" : "+{v[64:79]}"(xq));
    const bf16x8 xf[4] = {__builtin_bit_cast(bf16x8, quad<0>(xq)), __builtin_bit_cast(bf16x8, quad<1>(xq)), __builtin_bit_cast(bf16x8, quad<2>(xq)), __builtin_bit_cast(bf16x8, quad<3>(xq))};
#pragma unroll
    for (int rb = 0; rb < 4; ++rb) { acc[rb][0] = MFMA(id[0], xf[rb], acc[rb][0]); acc[rb][1] = MFMA(id[1], xf[rb], acc[rb][1]); }
    if (w == 0) poll_eq(ws, fo2, E + (unsigned)(layer + 1), kf2, kf2);
    u64 fl2 = 0;
    auto step = [&](auto ktc) {
      constexpr int kt = decltype(ktc)::value;
      __builtin_amdgcn_sched_barrier(0);
      if (kt == 5 && miss) {
        ARRIVED("+v"(fl2));
        if (__ballot(fl2 != E + (unsigned)(layer + 1)) & miss) poll_eq(ws, fo, E + (unsigned)(layer + 1), miss, ~0ull);
      }
      WAITL0;
      BAR;
      if (kt == 2 && miss) asm volatile("global_load_dwordx2 %0, %1, %2 sc1" : "+v"(fl2) : "v"(fo), "s"(ws) : "memory");
      if (kt == 0) { ST(layer * 24 + 15) }
      __builtin_amdgcn_sched_barrier(0);
      loadf(kt + 1, fa[(kt + 1) & 1], fb[(kt + 1) & 1]);
      if (kt == 0) return;
      mma(fa[kt & 1], fb[kt & 1]);
#pragma unroll
      for (int i = 0; i < 4; ++i) {
        __builtin_amdgcn_sched_group_barrier(0x008, 1, 0);
        __builtin_amdgcn_sched_group_barrier(0x100, 1, 0);
        __builtin_amdgcn_sched_group_barrier(0x008, 1, 0);
        __builtin_amdgcn_sched_group_barrier(0x100, 3, 0);
      }
#pragma unroll
      for (int i = 0; i < 8; ++i) __builtin_amdgcn_sched_group_barrier(0x008, 1, 0);
    };
    step(IC<0>{}); step(IC<1>{}); step(IC<2>{}); step(IC<3>{}); step(IC<4>{}); step(IC<5>{}); step(IC<6>{}); step(IC<7>{});
    step(IC<8>{}); step(IC<9>{}); step(IC<10>{}); step(IC<11>{}); step(IC<12>{}); step(IC<13>{}); step(IC<14>{});
    WAITV(0);
    asm volatile("" :: "v"(junk));
    __builtin_amdgcn_sched_barrier(0);
    auto put = [&](int rb) {
#pragma unroll
      for (int cb = 0; cb < 2; ++cb) lds<u32x2>(co[cb] + rb * 2048) = u32x2{pk2(acc[rb][cb][0], acc[rb][cb][1]), pk2(acc[rb][cb][2], acc[rb][cb][3])};
    };
#pragma unroll
    for (int rb = 0; rb < 4; ++rb)
#pragma unroll
      for (int cb = 0; cb < 2; ++cb) acc[rb][cb] = MFMA(fb[1][0][cb], fa[1][0][rb], acc[rb][cb]);
#pragma unroll
    for (int rb = 0; rb < 4; ++rb) {
      __builtin_amdgcn_sched_barrier(0);
#pragma unroll
      for (int cb = 0; cb < 2; ++cb) acc[rb][cb] = MFMA(fb[1][1][cb], fa[1][1][rb], acc[rb][cb]);
      __builtin_amdgcn_sched_barrier(0);
      if (rb >= 2) put(rb - 2);
    }
    __builtin_amdgcn_sched_barrier(0);
    ST(layer * 24 + 16)
    put(2); put(3);
  }
  WAITL0; BAR;
  asm volatile("" ::: "memory");
  u32x4 xn[2];
#pragma unroll
  for (int i = 0; i < 2; ++i) xn[i] = lds<u32x4>(pxr + OFF_C + i * 1024);
  if (layer == 3) {
    const __amdgpu_buffer_rsrc_t rsO = __builtin_amdgcn_make_buffer_rsrc((void*)uni(out), (short)0, SEQ * DM * 2, 0x00020000);
    __builtin_amdgcn_raw_buffer_store_b128(xn[0], rsO, rmo, 0, 16);
    __builtin_amdgcn_raw_buffer_store_b128(xn[1], rsO, rmo + 8 * DM * 2, 0, 16);
  } else {
    __builtin_amdgcn_raw_buffer_store_b128(xn[0], rsW, WS_X + layer * (SEQ * DM * 2) + imo, 0, 16);
    __builtin_amdgcn_raw_buffer_store_b128(xn[1], rsW, WS_X + layer * (SEQ * DM * 2) + imo + 1024, 0, 16);
    float mean[2], m2[2];
#pragma unroll
    for (int i = 0; i < 2; ++i) {
      typedef __attribute__((ext_vector_type(2))) __bf16 bf2;
      const bf2 one2 = __builtin_bit_cast(bf2, 0x3F803F80u);
      float sa = zero1(), sq = zero1();
#pragma unroll
      for (int e = 0; e < 4; ++e) {
        const unsigned u = xn[i][e];
        const bf2 t = __builtin_bit_cast(bf2, u);
        sa = __builtin_amdgcn_fdot2_f32_bf16(t, one2, sa, false);
        sq = __builtin_amdgcn_fdot2_f32_bf16(t, t, sq, false);
      }
      mean[i] = sa;
      m2[i] = sq;
    }
#define DPA(c) "v_add_f32_dpp %0, %0, %0 " c "\n\tv_add_f32_dpp %1, %1, %1 " c "\n\tv_add_f32_dpp %2, %2, %2 " c "\n\tv_add_f32_dpp %3, %3, %3 " c "\n\t"
    asm volatile("s_nop 3\n\t" DPA("quad_perm:[1,0,3,2] row_mask:0xf bank_mask:0xf") DPA("quad_perm:[2,3,0,1] row_mask:0xf bank_mask:0xf")
                 DPA("row_half_mirror row_mask:0xf bank_mask:0xf") "s_nop 0" : "+v"(mean[0]), "+v"(m2[0]), "+v"(mean[1]), "+v"(m2[1]));
    if ((lane & 7) < 2) {
      const int i = lane & 1;
      const u32x2 sv = {__float_as_uint(i ? mean[1] : mean[0]), __float_as_uint(i ? m2[1] : m2[0])};
      __builtin_amdgcn_raw_buffer_store_b64(sv, rsW, WS_ST + layer * (SEQ * 128) + (unsigned)((rt & 7) * 32768 + (hp >> 3) * 16384 + (hp & 7) * 1024 + (r0 + i * 8) * 8) + ((rt >> 3) * 8192 ^ P), 0, 16);
    }
  }
  ST(layer * 24 + 17)
  ST(layer * 24 + 18)
}

__global__ __launch_bounds__(512, 2) void mega_kernel(const u16* __restrict__ x0, const u16* __restrict__ wqkvA,
                                                      const u16* __restrict__ woA, const u16* __restrict__ wqkvB,
                                                      const u16* __restrict__ wphi, const u16* __restrict__ woB,
                                                      u16* __restrict__ out, char* __restrict__ ws) {
  unsigned P = 0;
  extern __shared__ __attribute__((aligned(16))) char smem[];
  const int w = __builtin_amdgcn_readfirstlane(threadIdx.x >> 6);
  u64 E = 0;
  {
    const int bid = blockIdx.x, lane = threadIdx.x & 63;
    const int rt = 2 * (bid & 4) + ((bid >> 3) & 7), hp = 4 * (bid & 3) + (bid >> 6), m0 = rt * 128;
    if (w < 4) {
      if (w == 3) {
        unsigned long long pc;
        unsigned len;
        asm volatile("s_getpc_b64 %0\n.Lmk_pc:\n\ts_mov_b32 %1, .Lmk_end-.Lmk_pc" : "=s"(pc), "=s"(len));
        const unsigned per = ((len >> 10) + 1) * 128, off = ((bid >> 3) & 7) * per + (lane * 128 < per ? lane * 128 : 0);
        asm volatile("s_mov_b32 m0, %2\n\ts_nop 0\n\tglobal_load_lds_dword %0, %1" :: "v"(off < len - 4 ? off : 0u), "s"(pc), "s"(lds_addr(smem) + LDS_TOTAL - 1024) : "memory", "m0");
      }
      const unsigned xo = (unsigned)((m0 + hp * 8 + w * 2) * (DM * 2) + lane * 16);
      glds4v(x0, xo, xo, xo, xo, lds_addr(smem) + 122880 + w * 4096);
      asm volatile("s_mov_b32 m0, %2\n\ts_nop 0\n\tglobal_load_lds_dword %0, %1" :: "v"(lane * 4), "s"(ws), "s"(lds_addr(smem) + OFF_E) : "memory", "m0");
    } else {
      const int lw = w & 3;
      const int kB = 16 * lw + (lane >> 3), fB = (kB >> 1) & 1;
      const unsigned vB0 = (unsigned)((kB * 3072 + hp * 64 + (((((lane & 7) >> 1) ^ fB) << 1) | (lane & 1)) * 8) * 2);
      const unsigned vB1 = (unsigned)(((kB + 8) * 3072 + hp * 64 + (((((lane & 7) >> 1) ^ (fB | 2)) << 1) | (lane & 1)) * 8) * 2) - 1024u;
      const unsigned vA = (unsigned)(((m0 + lw * 32 + (lane >> 3)) * DM + ((lane & 7) ^ (lane >> 3)) * 8) * 2);
#pragma unroll
      for (int s = 0; s < 1; ++s) {
        const int kt = (rt & 8) + s;
        const unsigned sA = lds_addr(smem) + s * 40960, sB = sA + 16384;
        glds4v(x0 + kt * 64, vA, vA + 15360u, vA + 2 * 15360u, vA + 3 * 15360u, sA + lw * 4096);
        const u16* b = wqkvA + (size_t)kt * 64 * 3072;
        glds4v(b, vB0, vB1, vB0, vB1, sB + lw * 6144);
        glds2v(b + 2048, vB0, vB1, sB + lw * 6144 + 4096);
      }
    }
  }
  f32x16 xq;
  u32x4 nv;
  u64 nk;
  asm volatile("" : "=v"(nv));
  asm volatile("" : "=s"(nk));
#pragma unroll 1
  for (int layer = 0; layer < 4; ++layer) {
    if (layer & 1) g1_phase<1>(smem, ws, P, nv, nk, E, w, xq, layer, x0, wqkvB, woB, wphi, nullptr, nullptr);
    else g1_phase<0>(smem, ws, P, nv, nk, E, w, xq, layer, x0, wqkvA, woA, nullptr, wqkvB, woB);
    g2_phase(smem, ws, P, nv, nk, E, w, xq, layer, x0, (layer & 1) ? woB : woA, (layer & 1) ? wqkvA : wqkvB, out);
  }
  asm volatile(".Lmk_end:");
  WAITV(0);
  if (blockIdx.x == 0 && w == 0 && lane_id() == 0) __hip_atomic_store((gu64*)ws, E + 16u, __ATOMIC_RELAXED, __HIP_MEMORY_SCOPE_AGENT);
}

extern "C" void kernel_launch(void* const* d_in, const int* in_sizes, int n_in,
                              void* d_out, int out_size, void* d_ws, size_t ws_size,
                              hipStream_t stream) {
  hipFuncSetAttribute((const void*)mega_kernel, hipFuncAttributeMaxDynamicSharedMemorySize, LDS_TOTAL);
  mega_kernel<<<256, 512, LDS_TOTAL, stream>>>((const u16*)d_in[0], (const u16*)d_in[1], (const u16*)d_in[2], (const u16*)d_in[4],
                                               (const u16*)d_in[5], (const u16*)d_in[6], (u16*)d_out, (char*)d_ws);
}
```

```cpp
#include <hip/hip_runtime.h>
#include <hip/hip_bf16.h>
#include <stdint.h>
#include <type_traits>
#pragma clang diagnostic ignored "-Winline-asm"
#pragma clang diagnostic ignored "-Wunused-value"

typedef unsigned short u16;
typedef __attribute__((ext_vector_type(8))) short bf16x8;
typedef __attribute__((ext_vector_type(4))) short bf16x4;
typedef __attribute__((ext_vector_type(4))) float f32x4;
typedef __attribute__((ext_vector_type(4))) unsigned u32x4;
typedef __attribute__((ext_vector_type(2))) unsigned u32x2;
typedef __attribute__((ext_vector_type(8))) float f32x8;
typedef __attribute__((ext_vector_type(16))) unsigned u32x16;
typedef __attribute__((ext_vector_type(16))) float f32x16;
typedef __attribute__((ext_vector_type(32))) float f32x32;
template <int I, typename T> __device__ __forceinline__ f32x4 quad(const T& v) { return __builtin_shufflevector(v, v, 4 * I, 4 * I + 1, 4 * I + 2, 4 * I + 3); }

constexpr int SEQ = 2048, DM = 1024, NH = 32;
#define ST(i)

typedef __attribute__((ext_vector_type(2))) float f32x2;
typedef __attribute__((ext_vector_type(2))) __bf16 bf16x2_t;
__device__ __forceinline__ unsigned pk2(float a, float b) {
  f32x2 v = {a, b};
  bf16x2_t r = __builtin_convertvector(v, bf16x2_t);
  return __builtin_bit_cast(unsigned, r);
}

__device__ __forceinline__ bf16x4 tr_read(const char* p) {
  return __builtin_amdgcn_ds_read_tr16_b64_v4i16((__attribute__((address_space(3))) bf16x4*)p);
}
template <typename T> __device__ __forceinline__ __attribute__((address_space(3))) T& lds(unsigned a) { return *(__attribute__((address_space(3))) T*)(size_t)a; }
__device__ __forceinline__ bf16x4 tr_read(unsigned a) { return __builtin_amdgcn_ds_read_tr16_b64_v4i16(&lds<bf16x4>(a)); }
__device__ __forceinline__ bf16x8 cat8(bf16x4 a, bf16x4 b) {
  bf16x8 f = {a[0], a[1], a[2], a[3], b[0], b[1], b[2], b[3]};
  return f;
}
__device__ __forceinline__ void glds16s(const void* sbase, unsigned voff, unsigned lds_base) {
  asm volatile("s_mov_b32 m0, %2\n\ts_nop 0\n\tglobal_load_lds_dwordx4 %0, %1" :: "v"(voff), "s"(sbase), "s"(lds_base) : "memory", "m0");
}
__device__ __forceinline__ void glds16c(const void* sbase, unsigned voff, unsigned lds_base) {
  asm volatile("s_mov_b32 m0, %2\n\ts_nop 0\n\tglobal_load_lds_dwordx4 %0, %1 sc1" :: "v"(voff), "s"(sbase), "s"(lds_base) : "memory", "m0");
}
#define GL_ "\n\tglobal_load_lds_dwordx4 "
__device__ __forceinline__ void glds4c(const void* sbase, unsigned v, unsigned lds_base) {
  asm volatile("s_mov_b32 m0, %2\n\ts_nop 0" GL_ "%0, %1 sc1" GL_ "%0, %1 offset:1024 sc1" GL_ "%0, %1 offset:2048 sc1" GL_ "%0, %1 offset:3072 sc1"
               :: "v"(v), "s"(sbase), "s"(lds_base) : "memory", "m0");
}
__device__ __forceinline__ void glds4v(const void* sbase, unsigned v0, unsigned v1, unsigned v2, unsigned v3, unsigned lds_base) {
  asm volatile("s_mov_b32 m0, %5\n\ts_nop 0" GL_ "%0, %4" GL_ "%1, %4 offset:1024" GL_ "%2, %4 offset:2048" GL_ "%3, %4 offset:3072"
               :: "v"(v0), "v"(v1), "v"(v2), "v"(v3), "s"(sbase), "s"(lds_base) : "memory", "m0");
}
__device__ __forceinline__ void glds2v(const void* sbase, unsigned v0, unsigned v1, unsigned lds_base) {
  asm volatile("s_mov_b32 m0, %3\n\ts_nop 0" GL_ "%0, %2" GL_ "%1, %2 offset:1024" :: "v"(v0), "v"(v1), "s"(sbase), "s"(lds_base) : "memory", "m0");
}
__device__ __forceinline__ void gld16(u32x4& d, unsigned voff, const void* sbase) {
  asm volatile("global_load_dwordx4 %0, %1, %2" : "=v"(d) : "v"(voff), "s"(sbase) : "memory");
}
__device__ __forceinline__ unsigned lds_addr(const void* p) { return (unsigned)(size_t)p; }
#define WAITV(n) asm volatile("s_waitcnt vmcnt(" #n ")" ::: "memory")
#define WAITVN(n) asm volatile("s_waitcnt vmcnt(%0)" :: "n"(n) : "memory")
#define WAITL0 do { asm volatile("" ::: "memory"); __builtin_amdgcn_s_waitcnt(0xC07F); asm volatile("" ::: "memory"); } while (0)
#define BAR __builtin_amdgcn_s_barrier()
#define ARRIVED(...) do { WAITV(0); __builtin_amdgcn_sched_barrier(0); asm volatile("" : __VA_ARGS__); } while (0)

__device__ __forceinline__ float vmaxf(float a, float b) { float r; asm("v_max_f32 %0, %1, %2" : "=v"(r) : "v"(a), "v"(b)); return r; }
template <int N> __device__ __forceinline__ float dpp_ror(float x) {
  return __uint_as_float(__builtin_amdgcn_update_dpp(0u, __float_as_uint(x), 0x120 + N, 0xf, 0xf, false));
}
__device__ __forceinline__ float r16_sum(float x) { x += dpp_ror<8>(x); x += dpp_ror<4>(x); x += dpp_ror<2>(x); return x + dpp_ror<1>(x); }
__device__ __forceinline__ float vmax3f(float a, float b, float c) { float r; asm("v_max3_f32 %0, %1, %2, %3" : "=v"(r) : "v"(a), "v"(b), "v"(c)); return r; }
__device__ __forceinline__ void max8pn(const f32x4& a, const f32x4& b, float& mx, float& mn) {
  asm("v_max3_f32 %0, %2, %3, %4\n\tv_max3_f32 %1, -%2, -%3, -%4\n\tv_max3_f32 %0, %0, %5, %6\n\tv_max3_f32 %1, %1, -%5, -%6\n\t"
      "v_max3_f32 %0, %0, %7, %8\n\tv_max3_f32 %1, %1, -%7, -%8\n\tv_max_f32 %0, %0, %9\n\tv_max_f32 %1, %1, -%9\n\ts_nop 0"
      : "=&v"(mx), "=&v"(mn) : "v"(a[0]), "v"(a[1]), "v"(a[2]), "v"(a[3]), "v"(b[0]), "v"(b[1]), "v"(b[2]), "v"(b[3]));
}
__device__ __forceinline__ float vmaxf_n(float a, float b) { float r; asm("v_max_f32 %0, %1, %2\n\ts_nop 0" : "=v"(r) : "v"(a), "v"(b)); return r; }
__device__ __forceinline__ float vmax3f_n(float a, float b, float c) { float r; asm("v_max3_f32 %0, %1, %2, %3\n\ts_nop 0" : "=v"(r) : "v"(a), "v"(b), "v"(c)); return r; }
template <bool MAXOP> __device__ __forceinline__ float rop(float a, float b) { return MAXOP ? vmaxf_n(a, b) : a + b; }
template <bool MAXOP> __device__ __forceinline__ void xr2(float& a, float& b) {
  auto r = __builtin_amdgcn_permlane16_swap(__float_as_uint(a), __float_as_uint(b), false, false);
  unsigned u = __float_as_uint(rop<MAXOP>(__uint_as_float(r[0]), __uint_as_float(r[1])));
  auto r2 = __builtin_amdgcn_permlane32_swap(u, u, false, false);
  u = __float_as_uint(rop<MAXOP>(__uint_as_float(r2[0]), __uint_as_float(r2[1])));
  auto r3 = __builtin_amdgcn_permlane16_swap(u, u, false, false);
  a = __uint_as_float(r3[0]); b = __uint_as_float(r3[1]);
}
template <bool MAXOP> __device__ __forceinline__ void xr4(float& a, float& b, float& c, float& d) {
  auto r = __builtin_amdgcn_permlane16_swap(__float_as_uint(a), __float_as_uint(b), false, false);
  auto q = __builtin_amdgcn_permlane16_swap(__float_as_uint(c), __float_as_uint(d), false, false);
  const unsigned ab = __float_as_uint(rop<MAXOP>(__uint_as_float(r[0]), __uint_as_float(r[1])));
  const unsigned cd = __float_as_uint(rop<MAXOP>(__uint_as_float(q[0]), __uint_as_float(q[1])));
  auto t = __builtin_amdgcn_permlane32_swap(ab, cd, false, false);
  const unsigned u = __float_as_uint(rop<MAXOP>(__uint_as_float(t[0]), __uint_as_float(t[1])));
  auto h = __builtin_amdgcn_permlane32_swap(u, u, false, false);
  auto p0 = __builtin_amdgcn_permlane16_swap(h[0], h[0], false, false);
  auto p1 = __builtin_amdgcn_permlane16_swap(h[1], h[1], false, false);
  a = __uint_as_float(p0[0]); b = __uint_as_float(p0[1]); c = __uint_as_float(p1[0]); d = __uint_as_float(p1[1]);
}

__device__ __forceinline__ unsigned xs4(float a, float b, float c, float d) {
  auto r = __builtin_amdgcn_permlane16_swap(__float_as_uint(a), __float_as_uint(b), false, false);
  auto q = __builtin_amdgcn_permlane16_swap(__float_as_uint(c), __float_as_uint(d), false, false);
  auto t = __builtin_amdgcn_permlane32_swap(__float_as_uint(__uint_as_float(r[0]) + __uint_as_float(r[1])), __float_as_uint(__uint_as_float(q[0]) + __uint_as_float(q[1])), false, false);
  return __float_as_uint(__uint_as_float(t[0]) + __uint_as_float(t[1]));
}

#define MFMA(a, b, c) __builtin_amdgcn_mfma_f32_16x16x32_bf16(a, b, c, 0, 0, 0)

typedef unsigned long long u64;
typedef __attribute__((address_space(1))) unsigned long long gu64;
constexpr unsigned WS_PL = 0x1a0000, WS_PR = 1024;
constexpr unsigned WS_FLAGS = 16384;
constexpr int FLS = 64;
constexpr unsigned WS_G0 = 65536;
constexpr unsigned WS_CSP = WS_G0 + SEQ * 16;
constexpr unsigned WS_ST = WS_CSP + 16 * 16 * 768;
constexpr unsigned WS_X = 1u << 21;
constexpr unsigned WS_O = WS_X + 3u * SEQ * DM * 2;
constexpr unsigned EMPTY = 0xffffffffu;
constexpr unsigned WS_KV = WS_O + 4u * SEQ * DM * 2;
constexpr unsigned KVLB = 256u * 16384;
constexpr unsigned WS_KF = 0x110000, KFLB = 17u * 16384;
constexpr unsigned WS_HL = WS_KV + 2u * KVLB;
static_assert(WS_ST % 16384 == 0 && WS_ST + 3u * SEQ * 128 <= WS_KF && WS_KF + 2 * KFLB <= WS_PL && WS_PL + 16384 <= WS_X, "first 2 MB");
static_assert((WS_X >> 21 & 1) && (WS_O >> 21 & 1) && (WS_KV >> 21 & 1) && (WS_HL >> 21 & 1) && WS_HL % (1u << 21) == 0, "odd multiples of 2 MB");
constexpr unsigned WS_CSA = WS_HL + 512u * 4096;
constexpr unsigned WS_CPA = WS_CSA + 256u * 1024;
constexpr unsigned WS_END = WS_CPA + 16 * 16 * 768;
constexpr int LDS_TOTAL = 163840;
constexpr int OFF_E = 139264;
constexpr int OFF_WO = 49152;
template <int V> using IC = std::integral_constant<int, V>;
__device__ __forceinline__ bf16x8 ones8() { unsigned e = 0x3F803F80u; asm volatile("" : "+v"(e)); return __builtin_bit_cast(bf16x8, u32x4{e, e, e, e}); }
__device__ __forceinline__ float zero1() { float z; asm volatile("v_mov_b32 %0, 0" : "=v"(z)); return z; }
__device__ __forceinline__ u32x4 empty4() { unsigned e = EMPTY; asm volatile("" : "+v"(e)); return u32x4{e, e, e, e}; }
__device__ __forceinline__ u64 korder(int v, int own) {
  const u64 t = v == 0 ? 0x3bf7210edca98654ull : v == 1 ? 0xb37f654210edca98ull : v == 2 ? 0x3bf7a98654210edcull : 0xb37fedca98654210ull;
  const u64 inv = v == 0 ? 0xd876e543c210fba9ull : v == 1 ? 0xc543f210dba9e876ull : v == 2 ? 0xd210eba9c876f543ull : 0xcba9f876d543e210ull;
  const u64 low = (1ull << (4 * ((inv >> (4 * own)) & 15))) - 1ull;
  return (u64)own | ((t & low) << 4) | (t & ~((low << 4) | 15ull));
}
__device__ __forceinline__ u64 korder0(int rt) {
  const int r = rt & 7;
  const u64 own = (0x765432765432ull >> (8 * ((0x4924 >> (2 * r)) & 3))) & 0xffffffull, oth = (0x7654321076543210ull >> (4 * r)) & 0xffffffffull;
  const u64 t = 0x10ull | (oth << 8) | (own << 40);
  return t | (rt < 8 ? 0x0000008888888800ull : 0x8888880000000088ull);
}
__device__ __forceinline__ int knib(u64 t, int s) { return (int)(t >> (4 * s)) & 15; }
__device__ __forceinline__ unsigned kfirst(u64 t) { return (1u << knib(t, 1)) | (1u << knib(t, 2)) | (1u << knib(t, 3)) | (1u << knib(t, 4)); }
__device__ __forceinline__ int kv1(int bid) { return (bid + (0x4924 >> ((bid >> 2) & 14))) & 3; }

template <typename T> __device__ __forceinline__ T* uni(T* p) {
  const unsigned long long v = (unsigned long long)p;
  const unsigned lo = __builtin_amdgcn_readfirstlane((unsigned)v), hi = __builtin_amdgcn_readfirstlane((unsigned)(v >> 32));
  return (T*)(((unsigned long long)hi << 32) | lo);
}
__device__ __forceinline__ u64 uni2(unsigned lo, unsigned hi) {
  lo = __builtin_amdgcn_readfirstlane(lo); hi = __builtin_amdgcn_readfirstlane(hi);
  return ((u64)hi << 32) | lo;
}
__device__ __forceinline__ int lane_id() {
  int l;
  asm volatile("v_mbcnt_lo_u32_b32 %0, -1, 0\n\tv_mbcnt_hi_u32_b32 %0, -1, %0" : "=v"(l));
  __builtin_assume(l >= 0 && l < 64);
  return l;
}
#define CODE_TOUCH(n, from, to) \
  unsigned long long pc; unsigned v; \
  { unsigned o, len; \
    asm volatile("s_getpc_b64 %0\n.Lmk_t" #n ":\n\ts_mov_b32 %1, " from "-.Lmk_t" #n "+0x40000\n\ts_mov_b32 %2, " to "-" from : "=s"(pc), "=s"(o), "=s"(len)); \
    pc -= 0x40000; v = (unsigned)(((bid >> 3) & 7) + lane * 8) * 128; v = o + ((int)v < (int)len ? v : 0u); }
#define IDS \
  const int lane = lane_id(); \
  asm volatile("" : "+s"(w)); \
  const int tid = w * 64 + lane; \
  int bid = blockIdx.x; asm volatile("" : "+s"(bid)); \
  ws = uni(ws); asm volatile("" : "+s"(ws)); \
  layer = __builtin_amdgcn_readfirstlane(layer); x0 = uni(x0); wo = uni(wo); \
    \
  const int rt = 2 * (bid & 4) + ((bid >> 3) & 7); \
  const int hp = 4 * (bid & 3) + (bid >> 6); \
  const int m0 = rt * 128; \
  P = __builtin_amdgcn_readfirstlane(P); \
  const unsigned SX = (unsigned)((rt & 7) * 524288) + ((((rt >> 3) ^ (rt >> 2) ^ 1) & 1) * 8192 ^ P); (void)SX; \
  const int fr = lane & 15, g = lane >> 4; \
  const int q4 = (lane & 15) >> 2, p4 = lane & 3; \
  const __amdgpu_buffer_rsrc_t rsW = __builtin_amdgcn_make_buffer_rsrc((void*)ws, (short)0, (int)WS_END, 0x00020000); \
  (void)m0; (void)fr; (void)q4; (void)p4; (void)rsW; (void)g;

__device__ __forceinline__ void publish(__amdgpu_buffer_rsrc_t rsW, int tid, unsigned idx, u64 v, unsigned P) {
  unsigned off = (WS_FLAGS + idx * FLS) ^ P;
  asm volatile("" : "+v"(off), "+v"(v));
  WAITV(0);
  WAITL0; BAR;
  if (tid == 192) __builtin_amdgcn_raw_buffer_store_b64(__builtin_bit_cast(u32x2, v), rsW, off, 0, 16);
  asm volatile("" ::: "memory");
}
__device__ __forceinline__ void poll_flags(char* ws, unsigned P, u64 E, int set, int rt, int hp, int lane, unsigned n, int grp) {
  const int l = lane & 15, g = lane >> 4;
  const int fi = set * 256 + (g == 2 ? l * 16 + hp : rt * 16 + l);
  const unsigned need = g == grp ? n : 0u;
  const unsigned off = need ? (WS_FLAGS + fi * FLS) ^ P : 0u;
  _Pragma("unroll 1") for (unsigned spins = 0; spins < (1u << 18); ++spins) {
    u64 d;
    asm volatile("global_load_dwordx2 %0, %1, %2 sc1\n\ts_waitcnt vmcnt(0)" : "=v"(d) : "v"(off), "s"(ws) : "memory");
    d -= E;
    const bool ok = (need == 0u) || ((unsigned)(d >> 32) == 0u && (unsigned)d >= need && (unsigned)d <= 16u);
    if (__all(ok)) break;
    __builtin_amdgcn_s_sleep(1);
  }
}

__device__ __forceinline__ u64 poll_eq(const char* ws, unsigned off, u64 want, u64 mask, u64 now) {
  u64 miss;
  _Pragma("unroll 1") for (unsigned spins = 0; spins < (1u << 18); ++spins) {
    u64 d;
    asm volatile("global_load_dwordx2 %0, %1, %2 sc1\n\ts_waitcnt vmcnt(0)" : "=v"(d) : "v"(off), "s"(ws) : "memory");
    miss = __ballot(d != want) & mask;
    if (!(miss & now)) break;
    __builtin_amdgcn_s_sleep(1);
  }
  return miss;
}

#define G1_MACROS \
  ".macro G1M rb, j, s, z\n" \
  " .set g1d, 160+24*(\\rb)+4*(\\j)\n .set g1a, 80+40*(\\s)+4*(\\rb)\n .set g1b, 96+40*(\\s)+4*(\\j)\n" \
  " .if (\\j) >= 4\n  .set g1t, g1a\n  .set g1a, g1b\n  .set g1b, g1t\n .endif\n" \
  " .if \\z\n  v_mfma_f32_16x16x32_bf16 v[g1d:g1d+3], v[g1b:g1b+3], v[g1a:g1a+3], 0\n" \
  " .else\n  v_mfma_f32_16x16x32_bf16 v[g1d:g1d+3], v[g1b:g1b+3], v[g1a:g1a+3], v[g1d:g1d+3]\n .endif\n" \
  ".endm\n" \
  ".macro G1RA rb, s, ra, oa\n" \
  " ds_read_b128 v[80+40*(\\s)+4*(\\rb):83+40*(\\s)+4*(\\rb)], \\ra offset:(\\oa)+(((\\rb)*2048)^(4096*(1-%[hh])))\n" \
  ".endm\n" \
  ".macro G1RB k, s, r0, r1, ob\n" \
  " .if (\\k) & 2\n  ds_read_b64_tr_b16 v[96+40*(\\s)+2*(\\k):97+40*(\\s)+2*(\\k)], \\r1 offset:(\\ob)+((\\k)>>2)*2048+((\\k)&1)*512\n" \
  " .else\n  ds_read_b64_tr_b16 v[96+40*(\\s)+2*(\\k):97+40*(\\s)+2*(\\k)], \\r0 offset:(\\ob)+((\\k)>>2)*2048+((\\k)&1)*512\n .endif\n" \
  ".endm\n" \
    \
  ".macro G1BK i, g, r0, r1, ob\n G1RB 4*(\\g)+((\\i)&3), (\\i)>>2, \\r0, \\r1, (\\ob)+((\\i)>>2)*12288\n.endm\n" \
  ".macro G1AK rb, s, a0, a1, oa\n .if \\s\n  G1RA \\rb, 1, \\a1, \\oa\n .else\n  G1RA \\rb, 0, \\a0, \\oa\n .endif\n.endm\n" \
  ".macro G1RDB g, r0, r1, ob\n .set g1i, 0\n .rept 8\n  G1BK g1i, \\g, \\r0, \\r1, \\ob\n  .set g1i, g1i+1\n .endr\n.endm\n" \
  ".macro G1RDA a0, a1, oa\n .set g1i, 0\n .rept 8\n  G1AK (g1i>>1)^2, g1i&1, \\a0, \\a1, \\oa\n  .set g1i, g1i+1\n .endr\n.endm\n" \
    \
  ".macro G1Q g, rb, q, i, z\n G1M \\rb, 2*(\\g)+((((\\i)>>1)^(\\q))&1), (((\\i)+1)>>1)&1, (\\z)&&(((\\i)&1)==0)\n.endm\n" \
    \
  ".macro G1SG g, z, rd, a0, a1, r0, r1, o\n" \
  " .set g1i, 0\n .rept 16\n" \
  "  .if ((\\g) == 0) && (g1i == 8) && (\\rd)\n   s_waitcnt lgkmcnt(7)\n   s_nop 0\n  .endif\n" \
  "  .if ((\\g) == 0) && (g1i == 15)\n   s_waitcnt lgkmcnt(0)\n  .endif\n" \
  "  G1Q \\g, (g1i>>2)^(2-((\\g)&1)), g1i>>2, g1i&3, \\z\n" \
  "  .if \\rd\n" \
  "   .if (\\g) == 0\n    .if g1i < 7\n     G1BK g1i, 2, \\r0, \\r1, \\o\n    .elseif g1i == 8\n     G1BK 7, 2, \\r0, \\r1, \\o\n    .endif\n" \
  "   .elseif (\\g) == 1\n    .if g1i < 8\n     G1BK g1i, 0, \\r0, \\r1, \\o\n    .endif\n" \
  "   .elseif (g1i & 2) == 0\n    G1BK (g1i&1)+2*(g1i>>2), 1, \\r0, \\r1, \\o\n" \
  "   .elseif (\\rd) == 1\n    G1AK (g1i>>2)^2, 1-(g1i&1), \\a0, \\a1, \\o\n   .endif\n" \
  "  .endif\n" \
  "  .set g1i, g1i+1\n .endr\n" \
  ".endm\n" \
    \
    \
  ".macro G1END h, i0, n, l, xs, r0, r1\n" \
  " .set g1i, \\i0\n .rept \\n\n" \
  "  .if (\\l) && (g1i-(\\i0) < 12)\n   .set g1x, ((\\l)>>(4*(g1i-(\\i0))))&15\n" \
  "   .if g1x < 15\n    ds_write_b128 \\xs, v[208+4*g1x:211+4*g1x] offset:(g1x+2*%[hh]*(g1x/6))*1024\n   .endif\n  .endif\n" \
  "  G1Q g1i>>3, 2*(\\h)+(((g1i>>2)^(g1i>>3))&1), g1i>>2, g1i&3, 0\n" \
  "  .if ((\\l) == 0) && (g1i < 8)\n   G1BK g1i, 2, \\r0, \\r1, 0\n  .endif\n" \
  "  .set g1i, g1i+1\n .endr\n" \
  ".endm\n"
#define G1_PURGE \
  ".purgem G1M\n .purgem G1RA\n .purgem G1RB\n .purgem G1BK\n .purgem G1AK\n .purgem G1RDB\n .purgem G1RDA\n .purgem G1Q\n .purgem G1SG\n .purgem G1END\n"
#define G1_CLOBBER \
  "v80", "v81", "v82", "v83", "v84", "v85", "v86", "v87", "v88", "v89", "v90", "v91", "v92", "v93", "v94", "v95", "v96", "v97", "v98", "v99", "v100", \
  "v101", "v102", "v103", "v104", "v105", "v106", "v107", "v108", "v109", "v110", "v111", "v112", "v113", "v114", "v115", "v116", "v117", "v118", \
  "v119", "v120", "v121", "v122", "v123", "v124", "v125", "v126", "v127", "v128", "v129", "v130", "v131", "v132", "v133", "v134", "v135", "v136", \
  "v137", "v138", "v139", "v140", "v141", "v142", "v143", "v144", "v145", "v146", "v147", "v148", "v149", "v150", "v151", "v152", "v153", "v154", \
  "v155", "v156", "v157", "v158", "v159", "v208", "v209", "v210", "v211", "v212", "v213", "v214", "v215", "v216", "v217", "v218", "v219", "v220", \
  "v221", "v222", "v223", "v232", "v233", "v234", "v235", "v236", "v237", "v238", "v239", "v240", "v241", "v242", "v243", "v244", "v245", "v246", "v247"

template <int MODE>
__device__ __forceinline__ void g1_phase(char* smem, char* ws, unsigned& P, const u32x4& nv, u64 nk, u64& E, int w, f32x16& xq, int layer, const u16* __restrict__ x0, const u16* __restrict__ W,
                                         const u16* __restrict__ wo, const u16* __restrict__ wphi,
                                         const u16* __restrict__ wn1, const u16* __restrict__ wn2) {
  constexpr bool HH = (MODE == 1);
  constexpr int ABYTES = 128 * 128;
  constexpr int STAGE = ABYTES + 3 * 8192;
  constexpr int OFF_KS = 147456;
  constexpr int OFF_SP = 3 * STAGE;
  constexpr int OFF_RS = !HH ? OFF_SP : OFF_SP + 16384;
  constexpr int OFF_CS = OFF_RS + 1152;
  constexpr int OFF_WP = OFF_CS + 1536;
  constexpr int OFF_JK = LDS_TOTAL - 1024;
  constexpr int OFF_PK = 0;
  constexpr int OFF_VF = OFF_WP + 4096;
  constexpr int OFF_KV = 16384;
  constexpr int OFF_R2 = 135168;
  const int XS = HH ? 98304 + ((w >> 1) & 1) * 16384 + (w & 1) * 4096 : (w & 3) * 12288;
  constexpr int PSTR = 144;
  if constexpr (HH) asm volatile(".Lmk_g1b:"); else asm volatile(".Lmk_g1a:");
  IDS
  W = uni(W); wphi = uni(wphi); wn1 = uni(wn1); wn2 = uni(wn2);
  const int kg = w >> 2, wm = (w >> 1) & 1, wn = w & 1;
  const bool first = !HH && layer == 0;
  f32x4 acc[2][6];
  u32x4 pw[8];
  ST(layer * 24 + 0)
  const int R0 = wm * 64 + (HH ? kg : kg ^ 1) * 32;
  u32x16 pa;
#define pcs pa[0]
#define prs pa[1]
#define opo pa[2]
#define ogo pa[3]
#define pkw pa[5]
#define hof pa[7]
#define dm pa[8]
#define dm2 pa[9]
#define pcv pa[4]
#define pwf pa[5]
#define prv pa[6]
#define pvf pa[7]
#define ppk pa[10]
#define ppa pa[11]
#define pvr pa[12]
#define gkv pa[13]
#define gkz pa[14]
  f32x4 bias[3];
  const unsigned sb = lds_addr(smem);
  auto prep = [&](int part = 3) {
    if (part & 1) {
    pcs = sb + OFF_CS + wn * 128 + g * 16;
    prs = sb + OFF_RS + (R0 + fr) * 8;
    opo = (unsigned)(wn * 8192 + R0 * 64 + lane * 16);
    ogo = opo + WS_O + layer * (SEQ * DM * 2) + SX + (unsigned)(wn * 8192 + hp * 32768);
    opo += sb;
    pcv = sb + OFF_CS + (wn * 32 + 128 + fr) * 4;
    prv = sb + OFF_R2 + (R0 + 4 * g) * 4;
    if (layer < 2) {
      constexpr unsigned kvn = WS_KV + MODE * KVLB;
      dm = kvn + (unsigned)((rt * 16 + hp) * 16384 + (w >> 2) * 8192 + (tid & 255) * 16);
      dm2 = w >= 4 ? WS_HL + (unsigned)((1 << 20) + ((rt & 7) * 8 + (hp >> 1)) * 16384 + (rt >> 3) * 8192 + (hp & 1) * 4096 + (tid - 256) * 16)
          : tid < 16 ? kvn + (unsigned)((rt * 16 + hp) * 16384 + (tid >> 3) * 8192 + 4096 + (tid & 7) * 16)
          : tid < 18 ? WS_KF + MODE * KFLB + (unsigned)(16 * 16384 + (tid & 1) * 8192 + hp * 256 + rt * 16)
          : tid >= 64 && tid < 128 ? WS_KF + MODE * KFLB + (unsigned)(hp * 16384 + (tid & 32) * 256 + rt * 512 + (tid & 31) * 16) : 0x7fff0000u;
    }
    }
    if (!(part & 2)) {
    } else if constexpr (!HH) {
      pkw = sb + OFF_KS - 4096 + R0 * 128 + wn * 2048 + lane * 16;
      const int rr = R0 ? rt : rt - 1;
      hof = m0 + R0 == 0 ? 0x7fff0000u : WS_HL + (unsigned)(layer * 524288 + ((rr & 7) * 8 + (hp >> 1)) * 16384 + (hp & 1) * 4096 + wn * 2048 + lane * 16) + ((((rr + 1) >> 3 ^ 1) & 1) * 8192 ^ P);
      const unsigned out = ~((0x5555u << 2 << fr) >> (4 * g));
#pragma unroll
      for (int i = 0; i < 8; ++i) {
        unsigned t;
        asm("v_bfe_i32 %0, %1, %2, 1" : "=v"(t) : "v"(out), "n"((i >> 2) * 16 + (i & 3)));
        bias[i >> 2][i & 3] = __uint_as_float(t & 0xff61b1e6u);
      }
      bias[2] = m0 + R0 == 0 ? f32x4{-3.0e38f, -3.0e38f, -3.0e38f, -3.0e38f} : bias[0];
    } else {
      const int u = wm * 2 + kg;
      pwf = sb + OFF_WP + wn * 2048 + (4 * g + q4) * 64 + p4 * 8;
      pvf = sb + OFF_VF + w * 2048 + lane * 16;
      ppk = sb + OFF_PK + w * (32 * PSTR) + fr * PSTR + g * 8;
      ppa = sb + OFF_PK + wn * (32 * PSTR) + (4 * g + q4) * PSTR + u * 32 + p4 * 8;
      pvr = sb + OFF_VF + wn * 2048 + lane * 16;
      const unsigned doff = WS_KV + (layer >> 1) * KVLB + (unsigned)((rt * 16 + hp) * 16384) + (((wn ^ (rt >> 3) ^ 1) & 1) * 8192 ^ P);
      gkv = doff + u * 1024 + lane * 16;
      gkz = fr == 0 ? doff + 4096 + u * 32 + g * 8 : 0x7fff0000u;
    }
    if (part & 2) asm volatile("" : "+{v[48:63]}"(pa), "+{v[36:39]}"(bias[0]), "+{v[40:43]}"(bias[1]), "+{v[44:47]}"(bias[2]));
    else asm volatile("" : "+{v[48:63]}"(pa));
  };

  if (kg == 1) {
    const int lw = w & 3;
    const int kB = 16 * lw + (lane >> 3);
    const int fB = (kB >> 1) & 1;
    const int cB0 = ((((lane & 7) >> 1) ^ fB) << 1) | (lane & 1), cB1 = ((((lane & 7) >> 1) ^ (fB | 2)) << 1) | (lane & 1);

    unsigned vW0, vW1;
    u64 ko2;
    auto run = [&](auto fst) {
      constexpr bool FIRST = decltype(fst)::value;
      if constexpr (!HH) { if constexpr (FIRST) asm volatile(".Lmk_f1:"); else asm volatile(".Lmk_f0:"); }
      constexpr int N = 10;
      const unsigned vB0 = FIRST ? (unsigned)((kB * 3072 + hp * 64 + cB0 * 8) * 2) : nv[0];
      const unsigned vB1 = FIRST ? (unsigned)(((kB + 8) * 3072 + hp * 64 + cB1 * 8) * 2) - 1024u : nv[1];
      const unsigned vA = FIRST ? (unsigned)(((m0 + lw * 32 + (lane >> 3)) * DM + ((lane & 7) ^ (lane >> 3)) * 8) * 2)
                                : nv[2];
      const char* xa = FIRST ? (const char*)x0 : ws + WS_X + (layer - 1) * (SEQ * DM * 2);
      constexpr int KS = FIRST ? 128 : 32768;
      const u64 kord = FIRST ? korder0(rt) : uni2((unsigned)nk, (unsigned)(nk >> 32));
      auto ktile = [&](int ks) { return knib(kord, ks); };
      auto stageA = [&](int ks, int boff) {
        const int kt = ktile(ks);
        const unsigned sA = lds_addr(smem) + boff;
        if (FIRST) glds4v(xa + kt * KS, vA, vA + 15360u, vA + 2 * 15360u, vA + 3 * 15360u, sA + lw * 4096);
        else glds4c(xa + kt * KS, vA, sA + lw * 4096);
      };
      auto stageB = [&](const u16* Wp, int ks, int boff) {
        const int kt = ktile(ks);
        const unsigned sB = lds_addr(smem) + boff + ABYTES;
        const u16* b = Wp + (size_t)kt * 64 * 3072;
        glds4v(b, vB0, vB1, vB0, vB1, sB + lw * 6144);
        glds2v(b + 2048, vB0, vB1, sB + lw * 6144 + 4096);
      };
      bf16x8 sel[3];
      unsigned cso[3], cst;
      if (FIRST) {
        stageA(1, STAGE); stageB(W, 1, STAGE);
        __builtin_amdgcn_sched_barrier(0);
        WAITVN(N);
        BAR;
        asm volatile("" ::: "memory");
        const u32x2 ev = *(const u32x2*)(smem + OFF_E);
        stageA(2, 2 * STAGE); stageB(W, 2, 2 * STAGE);
        __builtin_amdgcn_sched_barrier(0);
        const int fl = (q4 >> 1) | ((g & 1) << 1), j = 3 * wm + (fr < 3 ? fr : 0);
#pragma unroll
        for (int i = 0; i < 3; ++i) {
          const unsigned o = fr == i ? 0x3F803F80u : 0u;
          sel[i] = __builtin_bit_cast(bf16x8, u32x4{o, o, o, o});
          cso[i] = (unsigned)(2 * STAGE + ABYTES + (g >> 1) * 6144 + (g & 1) * 1024 + q4 * 128 + p4 * 8 + (((wn * 2 + ((wm + i) & 1)) ^ fl) << 5) + ((3 * wm + i) >> 1) * 2048);
          asm volatile("" : "+v"(sel[i]), "+v"(cso[i]));
        }
        cst = fr < 3 ? WS_CPA + (unsigned)(((hp * 16 + rt) * 192 + (j >> 1) * 64 + wn * 32 + (j & 1) * 16 + 4 * g) * 4) : 0x7fff0000u;
        asm volatile("" : "+v"(cst));
        E = uni2(ev[0], ev[1]);
      }
      u64 fall, fal2;
      unsigned fao, fa2, vP, vS, vC, vX, dS, dC, dR;
      auto ld_pw = [&](int i0) {
#pragma unroll
        for (int i = i0; i < i0 + 4; ++i) gld16(pw[i], vP, wn1 + i * 8 * 3072);
      };
      auto setup = [&]() {
        fao = (unsigned)((256 + rt * 16 + (lane & 15)) * FLS) ^ P;
        fa2 = (unsigned)(((FIRST ? 512 : 256) + (lane & 15) * 16 + (FIRST ? hp : (hp - ((1 << (lane >> 4)) >> 1)) & 15)) * FLS) ^ P;
        vP = (unsigned)((rt * 64 + (lane >> 3)) * 6144 + lw * 2048 + ((hp + (1 << lw)) & 15) * 128 + (lane & 7) * 16);
        vS = FIRST ? (unsigned)(WS_G0 + (m0 + lw * 32 + (lane & 31)) * 16)
                   : WS_ST + (unsigned)((layer - 1) * (SEQ * 128) + (rt & 7) * 32768 + g * 1024 + lw * 256 + fr * 16) + ((rt >> 3) * 8192 ^ P);
        vC = (FIRST ? WS_CPA : WS_CSP) + (unsigned)(hp * 12288 + g * 768 + lw * 192 + (fr < 12 ? fr : 0) * 16);
        if (!FIRST) vX = HH ? (unsigned)(hp * 4096 + lw * 1024 + lane * 16) : (unsigned)((rt * 16 + hp) * 1024 + lane * 16);
        vW0 = (unsigned)((kB * DM + hp * 64 + ((lane & 7) ^ ((lane >> 4) << 1)) * 8) * 2); vW1 = vW0 + 8 * DM * 2 - 1024u;
        dS = OFF_RS + lw * 256 + (FIRST ? (lane & 31) * 8 : fr * 16 + (g & 1) * 8); dC = OFF_CS + lw * 192 + (fr < 12 ? fr : 0) * 16 + g * 4;
        ko2 = korder(bid & 3, hp);
        dR = OFF_R2 + lw * 128 + (FIRST ? (lane & 31) * 4 : fr * 8 + (g & 1) * 4);
        asm volatile("" : "+v"(dR));
        asm volatile("" : "+v"(vS), "+v"(vX), "+v"(vW0), "+v"(vW1), "+v"(dS));
        asm volatile("" : "+s"(ko2));
        if (FIRST || HH) asm volatile("" : "+v"(vC), "+v"(dC), "+v"(fa2));
        if (FIRST) asm volatile("" : "+v"(vP)); else asm volatile("" : "+v"(fao));
      };
      if (!FIRST) {
        const char* xa1 = xa + ktile(1) * KS;
        const char* xa2 = xa + ktile(2) * KS;
        const u16* b2 = W + (size_t)ktile(2) * 64 * 3072;
        unsigned sA = lds_addr(smem) + STAGE + lw * 4096, sB = lds_addr(smem) + 2 * STAGE + ABYTES + lw * 6144;
        const unsigned fo = nv[3];
        WAITV(0);
        BAR;
        u64 fl1;
        __builtin_amdgcn_s_sleep(6);
        asm volatile("global_load_dwordx2 %0, %1, %2 sc1" : "=v"(fl1) : "v"(fo), "s"(ws) : "memory");
        glds4v(b2, vB0, vB1, vB0, vB1, sB);
        glds2v(b2 + 2048, vB0, vB1, sB + 4096);
        asm volatile("" : "+s"(xa1), "+s"(xa2), "+s"(sA));
        setup();
        {
          WAITV(6);
          __builtin_amdgcn_sched_barrier(0);
          asm volatile("" : "+v"(fl1));
          const u64 m = __ballot(fo != 0u);
          if (__ballot(fl1 != E + (unsigned)layer) & m) poll_eq(ws, fo, E + (unsigned)layer, m, ~0ull);
        }
        glds4c(xa1, vA, sA); glds4c(xa2, vA, sA + STAGE);
        __builtin_amdgcn_sched_barrier(0);
        prep();
      }
      bf16x8 cf[6];
      f32x4 csum = {0, 0, 0, 0};
      if (FIRST) { fall = E + 1u; vX = (unsigned)(bid * 8192 + lw * 2048 + lane * 16); asm volatile("" : "+v"(fall), "+v"(vX)); }
#pragma unroll
      for (int kt = 0; kt < 13; ++kt) {
        const int cur = (kt % 3) * STAGE;
        if (!FIRST && kt == 0) { WAITV(4); }
        else if (FIRST && kt == 4 && lw == 1) { WAITVN(N + 2); }
        else if ((!FIRST && kt == 1 && lw == 3) || ((FIRST || HH) && kt == 10 && lw == 3) || (FIRST && (kt == 2 || kt == 4 || kt == 5 || (kt == 11 && lw == 0)))) { WAITVN(N + 1); }
        else { WAITVN(N); }
        if (!FIRST && kt == 2 && lw == 3) {
          asm volatile("" : "+v"(fall));
          if (__any(fall != E + (unsigned)layer)) poll_flags(ws, P, E, 1, rt, hp, lane, layer, 0);
        }
        if (HH && kt == 11 && lw == 3) {
          asm volatile("" : "+v"(fal2));
          if (__any(fal2 != E + (unsigned)layer)) {
            _Pragma("unroll 1") for (int j = 0; j < 4; ++j) poll_flags(ws, P, E, 1, rt, (hp - ((1 << j) >> 1)) & 15, lane, layer, 2);
          }
        }
        if (FIRST && kt == 11 && lw == 3) {
          asm volatile("" : "+v"(fal2));
          if (__any(fal2 != E + 1u)) poll_flags(ws, P, E, 2, rt, hp, lane, 1u, 2);
        }
        BAR;
        if (FIRST && kt == 1) {
          asm volatile("" ::: "memory");
#pragma unroll
          for (int i = 0; i < 6; ++i) { const char* t = smem + cso[i >> 1] + (i & 1) * 12288; cf[i] = cat8(tr_read(t), tr_read(t + 512)); }
        }
        if (FIRST && kt == 2 && lw == 2 && bid < 64 && (bid & 7) == ((bid >> 3) & 3)) {
          u64 t0, t1; unsigned d;
          asm volatile("s_memtime %0\n\ts_load_dword %2, %3, 0x0\n\ts_waitcnt lgkmcnt(0)\n\ts_memtime %1\n\ts_waitcnt lgkmcnt(0)"
                       : "=&s"(t0), "=&s"(t1), "=&s"(d) : "s"(ws + WS_PL + (bid >> 5) * 8192 + (bid & 31) * 256) : "memory");
          d = (unsigned)(t1 - t0);
          const u64 v = ((E + 1u) << 16) | (d < 65535u ? d : 65535u);
          __builtin_amdgcn_raw_buffer_store_b64(__builtin_bit_cast(u32x2, v), rsW, lane == 0 ? WS_PR + (unsigned)(bid & 56) : 0x7fff0000u, 0, 16);
        }
        if (FIRST && kt == 12 && lw == 2) asm volatile("global_load_dwordx2 %0, %1, %2 sc1" : "=v"(fal2) : "v"((lane & 7) * 8), "s"(ws + WS_PR) : "memory");
        if (FIRST && kt >= 2 && kt < 6 && lw == kt - 2) setup();
        if (FIRST && kt >= 6 && kt < 10 && (lw >> 1) == ((kt - 6) >> 1)) prep(1 + (kt & 1));
        stageA(kt + 3, cur);
        if (FIRST && kt == 1) {
          asm volatile("" : "+v"(sel[0]));
#pragma unroll
          for (int i = 0; i < 6; ++i) csum = MFMA(cf[i], sel[i >> 1], csum);
          asm volatile("" : "+v"(csum));
        }
        stageB(W, kt + 3, cur);
        if (FIRST) {
          if (kt == 1) __builtin_amdgcn_raw_buffer_store_b128(__builtin_bit_cast(u32x4, csum), rsW, cst, 0, 16);
          if (kt == 3 && tid == 320) __builtin_amdgcn_raw_buffer_store_b64(__builtin_bit_cast(u32x2, fall), rsW, (WS_FLAGS + (unsigned)(512 + rt * 16 + hp) * FLS) ^ P, 0, 16);
          if (kt == 3 || kt == 4) glds16s(wo + (kt - 3) * 512, vX, lds_addr(smem) + OFF_JK);
        }
        if constexpr (!HH) if (FIRST && kt == 10 && lw == 0) {
          CODE_TOUCH(1, ".Lmk_ep0", ".Lmk_end")
          asm volatile("s_mov_b32 m0, %2\n\ts_nop 0\n\tglobal_load_lds_dword %0, %1" :: "v"(v), "s"(pc), "s"(lds_addr(smem) + OFF_JK) : "memory", "m0");
        }
        if (!FIRST && kt == 0 && lw == 3) asm volatile("global_load_dwordx2 %0, %1, %2 sc1" : "=v"(fall) : "v"(fao), "s"(ws + WS_FLAGS) : "memory");
        if ((FIRST || HH) && kt == 9 && lw == 3) asm volatile("global_load_dwordx2 %0, %1, %2 sc1" : "=v"(fal2) : "v"(fa2), "s"(ws + WS_FLAGS) : "memory");
      }
      if (FIRST && lw < 3) { ld_pw(0); WAITVN(N + 4); } else { WAITVN(N); }
      BAR;
      u32x4 sv[4], cv[4];
      auto ld_c = [&](u32x4& d, unsigned v, int so) { asm volatile("buffer_load_dwordx4 %0, %1, %2, %3 offen sc1" : "=v"(d) : "v"(v), "s"(rsW), "s"(so) : "memory"); };
      auto wo_tile = [&](int t) { glds2v(wo + (size_t)knib(ko2, t) * 64 * DM, vW0, vW1, lds_addr(smem) + OFF_WO + t * 8192 + lw * 2048); };
#pragma unroll
      for (int i = 0; i < (FIRST ? 1 : 4); ++i) ld_c(sv[i], vS, (i & 1) * 4096 + (i >> 1) * 16384);
      if (FIRST || HH) {
#pragma unroll
        for (int i = 0; i < 4; ++i) ld_c(cv[i], vC, i * 3072);
      }
      if (HH) glds16s(wphi, vX, lds_addr(smem) + OFF_WP + lw * 1024);
      else if (!FIRST) glds16c(ws + WS_CSA, vX, lds_addr(smem) + OFF_CS);
      wo_tile(0); wo_tile(1);
      if (HH || (FIRST && lw < 3)) { WAITV(13); } else { WAITV(9); }
      BAR;
      wo_tile(2); wo_tile(3); wo_tile(4); wo_tile(5);
      if (FIRST) {
        glds16s(wn2, vX, lds_addr(smem) + OFF_JK); glds16s(wn2 + 512, vX, lds_addr(smem) + OFF_JK);
        WAITV(14);
        if (lw < 3) ld_pw(4);
      } else {
        WAITV(12);
      }
      auto fin = [&]() {
        if (FIRST) {
          const unsigned tag = (unsigned)E + 1u;
          asm volatile("" : "+v"(sv[0]));
          _Pragma("unroll 1") for (unsigned spins = 0; spins < (1u << 18); ++spins) {
            if (__all(sv[0][1] == tag && sv[0][3] == tag)) break;
            __builtin_amdgcn_s_sleep(1);
            asm volatile("buffer_load_dwordx4 %0, %1, %2, 0 offen sc1\n\ts_waitcnt vmcnt(0)" : "=v"(sv[0]) : "v"(vS), "s"(rsW) : "memory");
          }
          *(u32x2*)(smem + dS) = u32x2{sv[0][0], sv[0][2]};
          { unsigned* p = (unsigned*)(smem + dR); p[0] = sv[0][0]; p[128] = sv[0][2]; }
        } else {
          asm volatile("" : "+v"(sv[0]), "+v"(sv[1]), "+v"(sv[2]), "+v"(sv[3]));
          const f32x4 t = (__builtin_bit_cast(f32x4, sv[0]) + __builtin_bit_cast(f32x4, sv[1])) + (__builtin_bit_cast(f32x4, sv[2]) + __builtin_bit_cast(f32x4, sv[3]));
          const unsigned u = xs4(t[0], t[2], t[1], t[3]);
          auto h = __builtin_amdgcn_permlane32_swap(u, u, false, false);
          const float mean = __uint_as_float(h[0]) * (1.0f / 1024.0f);
          const float var = fmaxf(__uint_as_float(h[1]) * (1.0f / 1024.0f) - mean * mean, 0.f);
          const float rs = __builtin_amdgcn_rsqf(var + 1e-5f);
          *(float2*)(smem + dS) = make_float2(-mean, rs);
          { float* p = (float*)(smem + dR); p[0] = -mean; p[128] = rs; }
        }
      };
      auto cfin = [&]() {
        if (FIRST || HH) {
          asm volatile("" : "+v"(cv[0]), "+v"(cv[1]), "+v"(cv[2]), "+v"(cv[3]));
          const f32x4 t = (__builtin_bit_cast(f32x4, cv[0]) + __builtin_bit_cast(f32x4, cv[1])) + (__builtin_bit_cast(f32x4, cv[2]) + __builtin_bit_cast(f32x4, cv[3]));
          *(unsigned*)(smem + dC) = xs4(t[0], t[1], t[2], t[3]);
        }
      };
      cfin();
      if (FIRST && lw == 2) {
        asm volatile("" : "+v"(fal2));
        _Pragma("unroll 1") for (unsigned spins = 0; spins < (1u << 18); ++spins) {
          if (__all(((fal2 ^ ((E + 1u) << 16)) >> 16) == 0)) break;
          __builtin_amdgcn_s_sleep(1);
          asm volatile("global_load_dwordx2 %0, %1, %2 sc1\n\ts_waitcnt vmcnt(0)" : "=v"(fal2) : "v"((lane & 7) * 8), "s"(ws + WS_PR) : "memory");
        }
        unsigned d = (unsigned)fal2 & 0xffffu;
        d = min(d, (unsigned)__builtin_amdgcn_update_dpp(0u, d, 0xb1, 0xf, 0xf, false));
        d = min(d, (unsigned)__builtin_amdgcn_update_dpp(0u, d, 0x4e, 0xf, 0xf, false));
        *(unsigned*)(smem + OFF_E + 256) = __builtin_amdgcn_readlane(d, 4) < __builtin_amdgcn_readlane(d, 0) ? 8192u : 0u;
      }
      if (!HH) BAR;
      fin();
      WAITL0;
    };
    typedef std::integral_constant<bool, true> T_;
    typedef std::integral_constant<bool, false> F_;
    __builtin_amdgcn_s_setprio(3);
    if (first) run(T_{}); else run(F_{});
    __builtin_amdgcn_s_setprio(0);
    BAR;
    if constexpr (!HH) asm volatile(".Lmk_ep0:");
    {
      const char* xr = smem + XS + lane * 16;
#pragma unroll
      for (int i = 0; i < 2; ++i)
#pragma unroll
        for (int j = HH ? 2 : 0; j < (HH ? 4 : 6); ++j) acc[i][j] = *(const f32x4*)(xr + (i * (HH ? 8 : 6) + j) * 1024);
      if (HH) asm volatile("" : "=v"(acc[0][0]), "=v"(acc[0][1]), "=v"(acc[1][0]), "=v"(acc[1][1]));
    }
    asm volatile("" : "={v[64:79]}"(xq));
  } else {
    prep(1);
    if (!first) publish(rsW, tid, 256 + rt * 16 + hp, E + (unsigned)layer, P);
    prep(2);
    const int fl = (q4 >> 1) | ((g & 1) << 1);
    const unsigned bo = lds_addr(smem) + ABYTES + (g >> 1) * 6144 + (g & 1) * 1024 + q4 * 128 + p4 * 8;
    const unsigned bo0 = bo + (((wn * 2 + 0) ^ fl) << 5), bo1 = bo + (((wn * 2 + 1) ^ fl) << 5);
    const unsigned ar = lds_addr(smem) + (wm * 64 + fr) * 128;
    const unsigned aK0 = ar + ((g ^ (fr & 7)) << 4), aK1 = ar + (((4 + g) ^ (fr & 7)) << 4);
    if (first) {
      const int row = m0 + hp * 8 + w * 2 + (lane >> 5);
      static_assert(HH || (OFF_SP == 122880 && OFF_KS + 12288 <= OFF_JK), "addresses used at the top of the kernel");
      asm volatile("" :: "v"(aK0), "v"(aK1), "v"(bo0), "v"(bo1));
      unsigned go = (lane & 31) == 0 ? (unsigned)(WS_G0 + row * 16) : 0x7fff0000u;
      unsigned ho = WS_HL + (unsigned)(((rt & 7) * 8 + (hp >> 1)) * 16384 + (hp & 1) * 4096 + tid * 16 + (rt >> 3) * 8192);
      asm volatile("" : "+v"(go), "+v"(ho));
      WAITV(0);
      u32x4 xs[4];
      const u32x2 ev = *(const u32x2*)(smem + OFF_E);
#pragma unroll
      for (int i = 0; i < 4; ++i) xs[i] = *(const u32x4*)(smem + OFF_SP + w * 4096 + (lane >> 5) * 2048 + (lane & 31) * 16 + i * 512);
      typedef __attribute__((ext_vector_type(2))) __bf16 bf2;
      const bf2 one2 = __builtin_bit_cast(bf2, 0x3F803F80u);
      float sa[2] = {zero1(), zero1()}, sq[2] = {zero1(), zero1()};
#pragma unroll
      for (int i = 0; i < 16; ++i) {
        const unsigned u = xs[i >> 2][i & 3];
        const bf2 t = __builtin_bit_cast(bf2, u);
        sa[i & 1] = __builtin_amdgcn_fdot2_f32_bf16(t, one2, sa[i & 1], false);
        sq[i & 1] = __builtin_amdgcn_fdot2_f32_bf16(t, t, sq[i & 1], false);
      }
      float sm = r16_sum(sa[0] + sa[1]), m2 = r16_sum(sq[0] + sq[1]);
      {
        auto r = __builtin_amdgcn_permlane16_swap(__float_as_uint(sm), __float_as_uint(sm), false, false);
        auto q = __builtin_amdgcn_permlane16_swap(__float_as_uint(m2), __float_as_uint(m2), false, false);
        sm = __uint_as_float(r[0]) + __uint_as_float(r[1]); m2 = __uint_as_float(q[0]) + __uint_as_float(q[1]);
      }
      const float mean = sm * (1.0f / 1024.0f);
      m2 = fmaxf(m2 - sm * mean, 0.f);
      const unsigned tag = ev[0] + 1u;
      u32x4 gw = {__float_as_uint(-mean), tag, __float_as_uint(__builtin_amdgcn_rsqf(m2 * (1.0f / 1024.0f) + 1e-5f)), tag};
      __builtin_amdgcn_raw_buffer_store_b128(gw, rsW, go, 0, 16);
      __builtin_amdgcn_raw_buffer_store_b128(empty4(), rsW, ho, 0, 16);
      if (bid < 64 && tid == 0) __builtin_amdgcn_raw_buffer_store_b32(tag, rsW, WS_PL + (unsigned)((bid >> 5) * 8192 + (bid & 31) * 256), 0, 16);
      E = uni2(ev[0], ev[1]);
    }
    f32x32 ac0;
    f32x16 ac1;
    f32x8 av[2];
    asm volatile("" :: "v"(aK0), "v"(aK1), "v"(bo0), "v"(bo1));
    if (first) BAR;
#define G1_OUT
#define G1_T(a)
#define G1_TEND
#define G1_TCL
    asm volatile(G1_MACROS
        ".p2align 3\n"
        "G1RDB 0, %[b0], %[b1], 0\n G1RDA %[a0], %[a1], 0\n G1RDB 1, %[b0], %[b1], 0\n"
        "ds_read_b128 v[64:67], %[xa]\n ds_read_b128 v[68:71], %[xa] offset:2048\n"
        "ds_read_b128 v[72:75], %[xa] offset:4096\n ds_read_b128 v[76:79], %[xa] offset:6144\n"
        "s_cmp_eq_u32 %[lay], 0\n s_waitcnt lgkmcnt(12)\n"
        "G1SG 0, 1, 1, %[a0], %[a1], %[b0], %[b1], 0\n"
        "s_mov_b32 vcc_lo, 16\n"
        "G1SG 1, 1, 1, %[a0], %[a1], %[b0], %[b1], 40960\n"
        "G1SG 2, 1, 2, %[a0], %[a1], %[b0], %[b1], 40960\n"
        "s_cbranch_scc1 .Lg1a%=\n"
        "s_nop 0\n"
        G1_T(88)
        "s_barrier\n"
        G1_T(92)
        "G1RDA %[a0], %[a1], 40960\n"
        "s_waitcnt lgkmcnt(4)\n"
        "s_branch .Lg1b%=\n"
        ".Lg1a%=:\n"
        G1_T(88)
        "s_barrier\n"
        G1_T(92)
        "G1RDB 0, %[b0], %[b1], 40960\n G1RDA %[a0], %[a1], 40960\n G1RDB 1, %[b0], %[b1], 40960\n"
        ".p2align 3\n s_nop 0\n"
        ".Lg1b%=:\n"
        "s_waitcnt lgkmcnt(8)\n"
        "G1SG 0, 0, 1, %[a0], %[a1], %[b0], %[b1], 40960\n"
        "s_barrier\n"
        "G1SG 1, 0, 1, %[a2], %[a3], %[b2], %[b3], 0\n"
        "G1SG 2, 0, 1, %[a2], %[a3], %[b2], %[b3], 0\n"
        "s_lshr_b32 vcc_lo, vcc_lo, 1\n s_waitcnt vmcnt(0) lgkmcnt(8)\n"
        "G1SG 0, 0, 1, %[a2], %[a3], %[b2], %[b3], 0\n"
        "s_barrier\n"
        "G1SG 1, 0, 1, %[a0], %[a1], %[b0], %[b1], 0\n"
        "G1SG 2, 0, 1, %[a0], %[a1], %[b0], %[b1], 0\n"
        "s_cbranch_scc0 .Lg1c%=\n s_waitcnt lgkmcnt(8)\n"
        "G1SG 0, 0, 1, %[a0], %[a1], %[b0], %[b1], 0\n"
        "s_barrier\n"
        "G1SG 1, 0, 1, %[a0], %[a1], %[b0], %[b1], 40960\n"
        "G1SG 2, 0, 1, %[a0], %[a1], %[b0], %[b1], 40960\n"
        "s_branch .Lg1b%=\n"
        ".Lg1c%=:\n"
        "s_waitcnt lgkmcnt(8)\n"
        "G1END 1, 0, 8, 0, %[xs], %[b0], %[b1]\n"
        "s_waitcnt lgkmcnt(8)\n s_nop 0\n"
        "G1END 1, 8, 8, 0, %[xs], %[b0], %[b1]\n"
        "s_waitcnt lgkmcnt(0)\n s_nop 0\n"
        "G1END 1, 16, 8, 0, %[xs], %[b0], %[b1]\n"
        ".if %[hh] == 0\n s_nop 0\n s_barrier\n .endif\n"
        G1_T(96)
        ".if %[hh]\n G1END 0, 0, 20, 0xffff76103298, %[xs], %[b0], %[b1]\n .else\n G1END 0, 0, 18, 0xba5498327610, %[xs], %[b0], %[b1]\n .endif\n"
        "s_waitcnt lgkmcnt(0)\n s_barrier\n"
        ".if %[hh]\n G1END 0, 20, 24-20, 0xffffffffffff, %[xs], %[b0], %[b1]\n .else\n G1END 0, 18, 24-18, 0xffffffffffff, %[xs], %[b0], %[b1]\n .endif\n"
        "s_nop 7\n"
        G1_TEND G1_PURGE
        : "=&{v[160:191]}"(ac0), "=&{v[192:207]}"(ac1), "=&{v[64:79]}"(xq), "=&{v[224:231]}"(av[0]), "=&{v[248:255]}"(av[1]),
          "+{v[48:63]}"(pa), "+{v[36:39]}"(bias[0]), "+{v[40:43]}"(bias[1]), "+{v[44:47]}"(bias[2]) G1_OUT
        : [a0] "v"(aK0), [a1] "v"(aK1), [a2] "v"(aK0 + 2 * STAGE), [a3] "v"(aK1 + 2 * STAGE), [b0] "v"(bo0), [b1] "v"(bo1),
          [b2] "v"(bo0 + 2 * STAGE), [b3] "v"(bo1 + 2 * STAGE), [xs] "v"(lds_addr(smem) + XS + lane * 16), [xa] "v"(wn ? aK1 : aK0),
          [lay] "s"(layer), [hh] "n"(MODE)
        : "memory", "scc", "vcc", G1_TCL G1_CLOBBER);
    acc[0][0] = quad<0>(ac0); acc[0][1] = quad<1>(ac0); acc[0][2] = quad<2>(ac0); acc[0][3] = quad<3>(ac0); acc[0][4] = quad<4>(ac0); acc[0][5] = quad<5>(ac0);
    acc[1][0] = quad<6>(ac0); acc[1][1] = quad<7>(ac0); acc[1][2] = quad<0>(ac1); acc[1][3] = quad<1>(ac1); acc[1][4] = quad<2>(ac1); acc[1][5] = quad<3>(ac1);
    if constexpr (HH) {
      const float cs[2] = {lds<float>(pcv), lds<float>(pcv + 64)};
      f32x4 rm[4], rr[4];
#pragma unroll
      for (int rb = 0; rb < 4; ++rb) { rm[rb] = lds<f32x4>(prv + rb * 64); rr[rb] = lds<f32x4>(prv + 512 + rb * 64); }
#pragma unroll
      for (int h = 0; h < 2; ++h) {
        f32x2 f[8];
#pragma unroll
        for (int i = 0; i < 8; ++i) {
          const int c = i >> 2, rb = (i >> 1) & 1, e = (i & 1) * 2;
          const f32x4 a = h == 0 ? acc[rb][4 + c] : c ? quad<1>(av[rb]) : quad<0>(av[rb]);
          f[i] = __builtin_elementwise_fma(f32x2{rm[2 * h + rb][e], rm[2 * h + rb][e + 1]}, f32x2{cs[c], cs[c]}, f32x2{a[e], a[e + 1]});
        }
        asm volatile("" : "+v"(f[0]), "+v"(f[1]), "+v"(f[2]), "+v"(f[3]), "+v"(f[4]), "+v"(f[5]), "+v"(f[6]), "+v"(f[7]));
#pragma unroll
        for (int i = 0; i < 8; ++i) f[i] *= f32x2{rr[2 * h + ((i >> 1) & 1)][(i & 1) * 2], rr[2 * h + ((i >> 1) & 1)][(i & 1) * 2 + 1]};
        asm volatile("" : "+v"(f[0]), "+v"(f[1]), "+v"(f[2]), "+v"(f[3]), "+v"(f[4]), "+v"(f[5]), "+v"(f[6]), "+v"(f[7]));
#pragma unroll
        for (int c = 0; c < 2; ++c) lds<u32x4>(pvf + h * 8192 + c * 1024) = u32x4{pk2(f[4 * c][0], f[4 * c][1]), pk2(f[4 * c + 1][0], f[4 * c + 1][1]), pk2(f[4 * c + 2][0], f[4 * c + 2][1]), pk2(f[4 * c + 3][0], f[4 * c + 3][1])};
      }
    }
  }
  ST(layer * 24 + 4)
  {
  const int lane = lane_id(), tid = w * 64 + lane, fr = lane & 15, g = lane >> 4;
  (void)fr; (void)g;
  auto o_put = [&](int rb, const f32x4& a, const f32x4& b) {
    const u32x4 v = {pk2(a[0], a[1]), pk2(a[2], a[3]), pk2(b[0], b[1]), pk2(b[2], b[3])};
    __builtin_amdgcn_raw_buffer_store_b128(v, rsW, ogo + rb * 1024, 0, 16);
    return v;
  };
  auto marks = [&]() {
    const u32x4 pz = empty4();
    __builtin_amdgcn_raw_buffer_store_b128(pz, rsW, dm, 0, 16);
    if (w < 2 || (!HH && w >= 4)) __builtin_amdgcn_raw_buffer_store_b128(pz, rsW, dm2, 0, 16);
  };
  if (first) { P = __builtin_amdgcn_readfirstlane(*(const unsigned*)(smem + OFF_E + 256)); ogo ^= P; hof ^= P; }
  f32x4 csq[2], csk[2], rm[2], rr[2];
  float csv[2];
  f32x2 st2[2] = {lds<f32x2>(prs), lds<f32x2>(prs + 128)};
#pragma unroll
  for (int c = 0; c < 2; ++c) {
    csk[c] = lds<f32x4>(pcs + 256 + c * 64);
    if (!HH) { csv[c] = lds<float>(pcv + c * 64); rm[1 - c] = lds<f32x4>(prv + (1 - c) * 64); rr[1 - c] = lds<f32x4>(prv + 512 + (1 - c) * 64); }
  }
#pragma unroll
  for (int c = 0; c < 2; ++c) csq[c] = lds<f32x4>(pcs + c * 64);
  if constexpr (!HH) {
    auto fold = [&](const f32x4& a, const f32x4& cs, const f32x2& st, bool scale) {
      f32x2 lo = __builtin_elementwise_fma(f32x2{cs[0], cs[1]}, f32x2{st[0], st[0]}, f32x2{a[0], a[1]});
      f32x2 hi = __builtin_elementwise_fma(f32x2{cs[2], cs[3]}, f32x2{st[0], st[0]}, f32x2{a[2], a[3]});
      if (scale) { lo *= f32x2{st[1], st[1]}; hi *= f32x2{st[1], st[1]}; }
      return u32x2{pk2(lo[0], lo[1]), pk2(hi[0], hi[1])};
    };
    auto cat2 = [](u32x2 a, u32x2 b) { return __builtin_bit_cast(bf16x8, u32x4{a[0], a[1], b[0], b[1]}); };
    auto foldv = [&](int rb, int c) {
      const f32x4& a = acc[rb][4 + c];
      const f32x2 lo = __builtin_elementwise_fma(f32x2{rm[rb][0], rm[rb][1]}, f32x2{csv[c], csv[c]}, f32x2{a[0], a[1]}) * f32x2{rr[rb][0], rr[rb][1]};
      const f32x2 hi = __builtin_elementwise_fma(f32x2{rm[rb][2], rm[rb][3]}, f32x2{csv[c], csv[c]}, f32x2{a[2], a[3]}) * f32x2{rr[rb][2], rr[rb][3]};
      return u32x2{pk2(lo[0], lo[1]), pk2(hi[0], hi[1])};
    };
    bf16x8 qf[2], kf[2];
    u32x2 vt[3][2];
#pragma unroll
    for (int rb = 1; rb >= 0; --rb) {
      kf[rb] = cat2(fold(acc[rb][2], csk[0], st2[rb], true), fold(acc[rb][3], csk[1], st2[rb], true));
      const u32x2 va0 = vt[rb + 1][0] = foldv(rb, 0), va1 = vt[rb + 1][1] = foldv(rb, 1);
      if (rb == 1) {
        if (R0 == 96) {
          u32x4 hk = __builtin_bit_cast(u32x4, kf[1]), hv = {va0[0], va0[1], va1[0], va1[1]};
#pragma unroll
          for (int e = 0; e < 4; ++e) { hk[e] = hk[e] < EMPTY - 1u ? hk[e] : EMPTY - 1u; hv[e] = hv[e] < EMPTY - 1u ? hv[e] : EMPTY - 1u; }
          __builtin_amdgcn_raw_buffer_store_b128(hk, rsW, hof, 0, 16);
          __builtin_amdgcn_raw_buffer_store_b128(hv, rsW, hof + 1024, 0, 16);
        } else {
          lds<bf16x8>(pkw + 4096) = kf[1];
          lds<u32x2>(pkw + 5120) = va0; lds<u32x2>(pkw + 5128) = va1;
        }
        asm volatile("" : "+v"(st2[0]), "+v"(st2[1]), "+v"(csv[0]), "+v"(csv[1]));
      }
    }
#pragma unroll
    for (int rb = 0; rb < 2; ++rb) qf[rb] = cat2(fold(acc[rb][0], csq[0], st2[rb], false), fold(acc[rb][1], csq[1], st2[rb], false));
    f32x4 sc[2][2];
    sc[1][0] = MFMA(kf[0], qf[1], bias[0]);
    sc[1][1] = MFMA(kf[1], qf[1], bias[1]);
    sc[0][1] = MFMA(kf[0], qf[0], bias[1]);
    constexpr float SCL = 0.17677669529663687f * 1.4426950408889634f;
    const bf16x8 ones = ones8();
    auto attn = [&](int rb) {
      const f32x4 z4 = {0, 0, 0, 0};
      const float scl = st2[rb][1] * SCL;
      asm volatile("s_nop 7" : "+v"(sc[rb][0]), "+v"(sc[rb][1]));
      unsigned m = __float_as_uint(vmax3f_n(vmax3f(sc[rb][0][0], sc[rb][0][1], sc[rb][0][2]), vmax3f(sc[rb][0][3], sc[rb][1][0], sc[rb][1][1]), vmaxf(sc[rb][1][2], sc[rb][1][3])));
      auto r = __builtin_amdgcn_permlane16_swap(m, m, false, false);
      m = __float_as_uint(vmaxf_n(__uint_as_float(r[0]), __uint_as_float(r[1])));
      auto r2 = __builtin_amdgcn_permlane32_swap(m, m, false, false);
      const float mxs = -vmaxf(__uint_as_float(r2[0]), __uint_as_float(r2[1])) * scl;
      u32x4 pv;
#pragma unroll
      for (int i = 0; i < 4; ++i) {
        const f32x2 t = __builtin_elementwise_fma(f32x2{sc[rb][i >> 1][(i & 1) * 2], sc[rb][i >> 1][(i & 1) * 2 + 1]}, f32x2{scl, scl}, f32x2{mxs, mxs});
        pv[i] = pk2(__builtin_amdgcn_exp2f(t[0]), __builtin_amdgcn_exp2f(t[1]));
      }
      const bf16x8 pf = __builtin_bit_cast(bf16x8, pv);
      const f32x4 den = MFMA(ones, pf, z4);
      const f32x4 o0 = MFMA(cat2(vt[rb][0], vt[rb + 1][0]), pf, z4), o1 = MFMA(cat2(vt[rb][1], vt[rb + 1][1]), pf, z4);
      const float inv = __builtin_amdgcn_rcpf(den[0]);
      return o_put(rb, o0 * inv, o1 * inv);
    };
    u32x4 hk, hv;
    auto ld_h = [&]() {
      asm volatile("s_nop 7\n\tbuffer_load_dwordx4 %0, %2, %3, 0 offen sc1\n\tbuffer_load_dwordx4 %1, %2, %3, 0 offen offset:1024 sc1" : "=&v"(hk), "=&v"(hv) : "v"(hof), "s"(rsW) : "memory");
    };
    asm volatile("" : "+v"(sc[0][1]));
    if (R0 == 0) ld_h();
    const u32x4 ov = attn(1);
    if (R0 == 0) {
      WAITL0; BAR;
      WAITV(1);
      _Pragma("unroll 1") for (unsigned spins = 0; spins < (1u << 18); ++spins) {
        __builtin_amdgcn_sched_barrier(0); asm volatile("" : "+v"(hk), "+v"(hv));
        unsigned mxw = 0;
#pragma unroll
        for (int e = 0; e < 4; ++e) { mxw = hk[e] > mxw ? hk[e] : mxw; mxw = hv[e] > mxw ? hv[e] : mxw; }
        if (__all(mxw != EMPTY)) break;
        ld_h();
        WAITV(0);
      }
      asm volatile("" : "+v"(hk), "+v"(hv));
      if (first) marks();
    } else {
      if (first) marks();
      if (first && tid >= 128 && tid < 176) __builtin_amdgcn_raw_buffer_store_b128(*(const u32x4*)(smem + OFF_CS + (tid - 128) * 16), rsW, WS_CSA + (unsigned)((rt * 16 + hp) * 1024 + (tid - 128) * 16), 0, 16);
      WAITL0; BAR;
      asm volatile("" ::: "memory");
      hk = lds<u32x4>(pkw); hv = lds<u32x4>(pkw + 1024);
    }
    ST(layer * 24 + 5)
    asm volatile("" ::: "memory");
    vt[0][0] = u32x2{hv[0], hv[1]}; vt[0][1] = u32x2{hv[2], hv[3]};
    sc[0][0] = MFMA(__builtin_bit_cast(bf16x8, hk), qf[0], bias[2]);
    lds<u32x4>(opo + 1024) = ov;
    lds<u32x4>(opo) = attn(0);
  } else {
    bf16x8 wf[2];
#pragma unroll
    for (int fb = 0; fb < 2; ++fb) wf[fb] = cat8(tr_read(pwf + fb * 32), tr_read(pwf + fb * 32 + 16 * 64));
    __builtin_amdgcn_sched_barrier(0);
    if (layer == 1) marks();
    const f32x4 z4 = {0, 0, 0, 0};
    constexpr float L2E = 1.4426950408889634f;
    const unsigned kvo = WS_KV + (layer >> 1) * KVLB, kfo = WS_KF + (layer >> 1) * KFLB + hp * 16384, kzo = WS_KF + (layer >> 1) * KFLB + 16 * 16384 + hp * 256;
    bf16x8 qp[2][2];
    auto phi2 = [&](auto qktag) {
      constexpr int QK = decltype(qktag)::value & 1;
      constexpr int RB0 = (decltype(qktag)::value >> 1) & 1, RB1 = (decltype(qktag)::value & 4) ? RB0 + 1 : 2;
      float pv[2][8];
      f32x4 pm[2][2];
      f32x2 f[2][4];
#pragma unroll
      for (int rb = RB0; rb < RB1; ++rb) {
        asm volatile("" : "+v"(st2[rb]));
        const f32x2 nm = {st2[rb][0], st2[rb][0]};
#pragma unroll
        for (int i = 0; i < 4; ++i) {
          const f32x4& a = acc[rb][2 * QK + (i >> 1)];
          const f32x4& cs = QK ? csk[i >> 1] : csq[i >> 1];
          f[rb][i] = __builtin_elementwise_fma(f32x2{cs[(i & 1) * 2], cs[(i & 1) * 2 + 1]}, nm, f32x2{a[(i & 1) * 2], a[(i & 1) * 2 + 1]});
        }
      }
#pragma unroll
      for (int rb = RB0; rb < RB1; ++rb) asm volatile("" : "+v"(f[rb][0]), "+v"(f[rb][1]), "+v"(f[rb][2]), "+v"(f[rb][3]));
#pragma unroll
      for (int rb = RB0; rb < RB1; ++rb) {
        const bf16x8 xf = __builtin_bit_cast(bf16x8, u32x4{pk2(f[rb][0][0], f[rb][0][1]), pk2(f[rb][1][0], f[rb][1][1]), pk2(f[rb][2][0], f[rb][2][1]), pk2(f[rb][3][0], f[rb][3][1])});
        pm[rb][0] = MFMA(wf[0], xf, z4); pm[rb][1] = MFMA(wf[1], xf, z4);
      }
      if (QK) asm volatile("s_nop 7" : "+v"(pm[0][0]), "+v"(pm[0][1]), "+v"(pm[1][0]), "+v"(pm[1][1]));
      else asm volatile("s_nop 7" : "+v"(pm[RB0][0]), "+v"(pm[RB0][1]));
#pragma unroll
      for (int rb = RB0; rb < RB1; ++rb)
#pragma unroll
        for (int i = 0; i < 4; ++i) { pv[rb][i] = pm[rb][0][i]; pv[rb][4 + i] = pm[rb][1][i]; }
      float mx[2], mn[2];
#pragma unroll
      for (int u = RB0; u < RB1; ++u) max8pn(pm[u][0], pm[u][1], mx[u], mn[u]);
      if (RB1 - RB0 == 2) xr4<true>(mx[0], mn[0], mx[1], mn[1]); else xr2<true>(mx[RB0], mn[RB0]);
      float ep[2][8], en[2][8], sp[2], sn[2];
#pragma unroll
      for (int u = RB0; u < RB1; ++u) {
        const float c = st2[u][1] * L2E, mxs = -mx[u] * c, mns = -mn[u] * c;
#pragma unroll
        for (int i = 0; i < 4; ++i) {
          const f32x2 p2 = {pv[u][2 * i], pv[u][2 * i + 1]};
          const f32x2 tp = __builtin_elementwise_fma(p2, f32x2{c, c}, f32x2{mxs, mxs});
          const f32x2 tn = __builtin_elementwise_fma(-p2, f32x2{c, c}, f32x2{mns, mns});
          ep[u][2 * i] = __builtin_amdgcn_exp2f(tp[0]); ep[u][2 * i + 1] = __builtin_amdgcn_exp2f(tp[1]);
          en[u][2 * i] = __builtin_amdgcn_exp2f(tn[0]); en[u][2 * i + 1] = __builtin_amdgcn_exp2f(tn[1]);
        }
        const f32x2 a2 = (f32x2{ep[u][0], ep[u][1]} + f32x2{ep[u][2], ep[u][3]}) + (f32x2{ep[u][4], ep[u][5]} + f32x2{ep[u][6], ep[u][7]});
        const f32x2 b2 = (f32x2{en[u][0], en[u][1]} + f32x2{en[u][2], en[u][3]}) + (f32x2{en[u][4], en[u][5]} + f32x2{en[u][6], en[u][7]});
        sp[u] = a2[0] + a2[1]; sn[u] = b2[0] + b2[1];
      }
      if (RB1 - RB0 == 2) xr4<false>(sp[0], sn[0], sp[1], sn[1]); else xr2<false>(sp[RB0], sn[RB0]);
#pragma unroll
      for (int rb = RB0; rb < RB1; ++rb) {
        float ip = __builtin_amdgcn_rcpf(sp[rb]), in = __builtin_amdgcn_rcpf(sn[rb]);
        u32x2 o0, o1, o2, o3;
        o0[0] = pk2(ep[rb][0] * ip, ep[rb][1] * ip); o0[1] = pk2(ep[rb][2] * ip, ep[rb][3] * ip);
        o1[0] = pk2(ep[rb][4] * ip, ep[rb][5] * ip); o1[1] = pk2(ep[rb][6] * ip, ep[rb][7] * ip);
        o2[0] = pk2(en[rb][0] * in, en[rb][1] * in); o2[1] = pk2(en[rb][2] * in, en[rb][3] * in);
        o3[0] = pk2(en[rb][4] * in, en[rb][5] * in); o3[1] = pk2(en[rb][6] * in, en[rb][7] * in);
        if (QK == 0) {
          qp[rb][0] = __builtin_bit_cast(bf16x8, (u32x4){o0[0], o0[1], o1[0], o1[1]});
          qp[rb][1] = __builtin_bit_cast(bf16x8, (u32x4){o2[0], o2[1], o3[0], o3[1]});
          asm volatile("" : "+v"(qp[rb][0]), "+v"(qp[rb][1]));
        } else {
          const unsigned dst = ppk + rb * 16 * PSTR;
          lds<u32x2>(dst) = o0; lds<u32x2>(dst + 32) = o1; lds<u32x2>(dst + 64) = o2; lds<u32x2>(dst + 96) = o3;
        }
      }
    };
    phi2(std::integral_constant<int, 1>{});
    bf16x8 one8 = ones8();
    asm volatile("" : "+v"(one8));
    WAITL0; BAR;
    ST(layer * 24 + 5)
    {
      bf16x8 af[4], v0[4], v1[4];
      asm volatile("" ::: "memory");
#pragma unroll
      for (int sw = 0; sw < 4; ++sw) {
        af[sw] = cat8(tr_read(ppa + sw * (64 * PSTR)), tr_read(ppa + sw * (64 * PSTR) + 16 * PSTR));
        v0[sw] = lds<bf16x8>(pvr + sw * 4096);
        v1[sw] = lds<bf16x8>(pvr + sw * 4096 + 1024);
      }
      __builtin_amdgcn_sched_barrier(0);
      f32x4 kv[3] = {z4, z4, z4};
#pragma unroll
      for (int sw = 0; sw < 4; ++sw) {
        kv[0] = MFMA(af[sw], v0[sw], kv[0]);
        kv[1] = MFMA(af[sw], v1[sw], kv[1]);
        kv[2] = MFMA(af[sw], one8, kv[2]);
      }
      auto pc = [](float x, float y) { const unsigned t = pk2(x, y); return t < EMPTY - 1u ? t : EMPTY - 1u; };
      __builtin_amdgcn_raw_buffer_store_b128(u32x4{pc(kv[0][0], kv[0][1]), pc(kv[0][2], kv[0][3]), pc(kv[1][0], kv[1][1]), pc(kv[1][2], kv[1][3])}, rsW, gkv, 0, 16);
      __builtin_amdgcn_raw_buffer_store_b64(u32x2{pc(kv[2][0], kv[2][1]), pc(kv[2][2], kv[2][3])}, rsW, gkz, 0, 16);
      __builtin_amdgcn_sched_barrier(0);
      if (kg) {
#pragma unroll
        for (int i = 0; i < 4; ++i) acc[i >> 1][i & 1] = lds<f32x4>(sb + XS + lane * 16 + ((i >> 1) * 8 + (i & 1)) * 1024);
      }
    }
    ST(layer * 24 + 6)
    u32x4 l1[8];
    const bool red = w == 7 || (w == 6 && (rt & 7) == 0);
    unsigned lo = kvo + (unsigned)((hp + (lane >> 5) * 128) * 16384) + ((((rt >> 3) ^ (lane >> 5) ^ 1) & 1) * 8192 ^ P) +
                  (w == 7 ? (unsigned)((rt & 4) * 512 + (g & 1) * 1024 + (rt & 3) * 256 + fr * 16) : (unsigned)(4096 + (lane & 7) * 16));
    unsigned fs = w == 7 ? kfo + (unsigned)((rt >> 2) * 2048 + (lane >> 5) * 1024 + (rt & 3) * 256 + fr * 16 + (g & 1) * 8)
                : (lane & 24) ? 0x7fff0000u : kzo + (unsigned)((rt >> 3) * 128 + (lane & 4) * 16 + (lane & 1) * 32 + (lane >> 5) * 16 + (lane & 2) * 4);
    auto l1_load = [&]() {
      asm volatile("" ::: "memory");
#pragma unroll
      for (int p = 0; p < 8; ++p) l1[p] = __builtin_amdgcn_raw_buffer_load_b128(rsW, lo, p * (16 * 16384), 16);
      __builtin_amdgcn_sched_barrier(0);
    };
    if (red) asm volatile("" : "+v"(lo), "+v"(fs));
    BAR;
    if (red) {
      typedef __attribute__((ext_vector_type(2))) __bf16 bf2;
      unsigned c0 = 0x00003F80u, c1 = 0x3F800000u;
      asm volatile("" : "+v"(c0), "+v"(c1));
      __builtin_amdgcn_s_setprio(3);
      __builtin_amdgcn_s_sleep(5);
      _Pragma("unroll 1") for (unsigned spins = 0; spins < (1u << 18); ++spins) {
        l1_load();
        unsigned mxw = 0;
#pragma unroll
        for (int p = 0; p < 8; ++p)
#pragma unroll
          for (int e = 0; e < 4; ++e) mxw = l1[p][e] > mxw ? l1[p][e] : mxw;
        if (__all(mxw != EMPTY)) break;
      }
      float s4[4];
      float a[8] = {zero1(), zero1(), zero1(), zero1(), zero1(), zero1(), zero1(), zero1()};
#pragma unroll
      for (int p = 0; p < 8; ++p)
#pragma unroll
        for (int e = 0; e < 4; ++e) {
          const unsigned u = l1[p][e];
          a[2 * e] = __builtin_amdgcn_fdot2_f32_bf16(__builtin_bit_cast(bf2, u), __builtin_bit_cast(bf2, c0), a[2 * e], false);
          a[2 * e + 1] = __builtin_amdgcn_fdot2_f32_bf16(__builtin_bit_cast(bf2, u), __builtin_bit_cast(bf2, c1), a[2 * e + 1], false);
        }
#pragma unroll
      for (int i = 0; i < 4; ++i) {
        auto r = __builtin_amdgcn_permlane32_swap(__float_as_uint(a[i]), __float_as_uint(a[i + 4]), false, false);
        s4[i] = __uint_as_float(r[0]) + __uint_as_float(r[1]);
      }
      unsigned o0 = pk2(s4[0], s4[1]), o1 = pk2(s4[2], s4[3]);
      o0 = o0 < EMPTY - 1u ? o0 : EMPTY - 1u; o1 = o1 < EMPTY - 1u ? o1 : EMPTY - 1u;
      __builtin_amdgcn_raw_buffer_store_b64(u32x2{o0, o1}, rsW, fs, 0, 16);
      __builtin_amdgcn_raw_buffer_store_b64(u32x2{o0, o1}, rsW, fs, 8192, 16);
    }
    phi2(std::integral_constant<int, 4>{});
    ST(layer * 24 + 7)
    phi2(std::integral_constant<int, 6>{});
    __builtin_amdgcn_s_setprio(0);
    ST(layer * 24 + 8)
    ST(layer * 24 + 9)
    {
      unsigned pfo = OFF_KV + wn * 4096 + lane * 16, pzo = OFF_KV + 8192 + wn * 128 + g * 16;
      asm volatile("" : "+v"(pfo), "+v"(pzo));
      if (w < 3) {
        u32x4 fa[3];
        unsigned fo = kfo + (unsigned)(w * 3072 + lane * 16) + ((rt >> 3) * 8192 ^ P);
        unsigned fo2 = w < 2 ? fo + 2048 : kzo + (unsigned)((lane & 15) * 16) + ((rt >> 3) * 8192 ^ P);
        unsigned lo1 = sb + OFF_KV + w * 3072 + lane * 16, lo2 = w < 2 ? lo1 + 2048 : sb + OFF_KV + 8192 + (lane & 15) * 16;
        __builtin_amdgcn_s_sleep(8);
        _Pragma("unroll 1") for (unsigned spins = 0; spins < (1u << 18); ++spins) {
          asm volatile("" ::: "memory");
          fa[0] = __builtin_amdgcn_raw_buffer_load_b128(rsW, fo, 0, 16);
          fa[1] = __builtin_amdgcn_raw_buffer_load_b128(rsW, fo + 1024, 0, 16);
          fa[2] = __builtin_amdgcn_raw_buffer_load_b128(rsW, fo2, 0, 16);
          unsigned mxw = 0;
#pragma unroll
          for (int e = 0; e < 12; ++e) mxw = fa[e >> 2][e & 3] > mxw ? fa[e >> 2][e & 3] : mxw;
          if (__all(mxw != EMPTY)) break;
          __builtin_amdgcn_s_sleep(1);
        }
        lds<u32x4>(lo1) = fa[0]; lds<u32x4>(lo1 + 1024) = fa[1]; lds<u32x4>(lo2) = fa[2];
      }
      ST(layer * 24 + 10)
      WAITL0; BAR;
      ST(layer * 24 + 11)
      bf16x8 kvf[3][2];
#pragma unroll
      for (int ks = 0; ks < 2; ++ks) {
        kvf[0][ks] = *(const bf16x8*)(smem + pfo + ks * 2048);
        kvf[1][ks] = *(const bf16x8*)(smem + pfo + ks * 2048 + 1024);
        kvf[2][ks] = *(const bf16x8*)(smem + pzo + ks * 64);
      }
#pragma unroll
      for (int rb = 0; rb < 2; ++rb) {
        f32x4 oa[3];
#pragma unroll
        for (int nb = 2; nb >= 0; --nb) {
          oa[nb] = MFMA(kvf[nb][0], qp[rb][0], z4);
          oa[nb] = MFMA(kvf[nb][1], qp[rb][1], oa[nb]);
        }
        float den = oa[2][0];
        den += 1e-6f;
        float inv = __builtin_amdgcn_rcpf(den);
        lds<u32x4>(opo + rb * 1024) = o_put(rb, oa[0] * inv, oa[1] * inv);
      }
    }
  }
  ST(layer * 24 + 12)
  if (w >= 6) {
    const unsigned v0 = (unsigned)((((w & 1) * 32 + (lane >> 3)) * DM + hp * 64 + ((lane & 7) ^ ((lane >> 4) << 1)) * 8) * 2);
    const u64 ko = korder(bid & 3, hp);
#pragma unroll
    for (int t = 6; t < 10; ++t) glds4v(wo + (size_t)knib(ko, t) * 64 * DM, v0, v0 + 15360u, v0 + 2 * 15360u, v0 + 3 * 15360u, sb + OFF_WO + t * 8192 + (w & 1) * 4096);
    WAITV(16); WAITL0; BAR;
    __builtin_amdgcn_s_sleep(2);
#pragma unroll
    for (int t = 10; t < 12; ++t) glds4v(wo + (size_t)knib(ko, t) * 64 * DM, v0, v0 + 15360u, v0 + 2 * 15360u, v0 + 3 * 15360u, sb + OFF_WO + t * 8192 + (w & 1) * 4096);
  } else if (w >= 4) { WAITV(0); WAITL0; BAR; }
  if (first && w >= 4 && w < 7) {
    typedef __attribute__((ext_vector_type(2))) __bf16 bf2;
    unsigned c0 = 0x00003F80u, c1 = 0x3F800000u;
    asm volatile("" : "+v"(c0), "+v"(c1));
    const bf2 lo1 = __builtin_bit_cast(bf2, c0), hi1 = __builtin_bit_cast(bf2, c1);
    float a[8] = {zero1(), zero1(), zero1(), zero1(), zero1(), zero1(), zero1(), zero1()};
#pragma unroll
    for (int i = 0; i < 8; ++i) {
      asm volatile("" : "+v"(pw[i]));
#pragma unroll
      for (int e = 0; e < 4; ++e) {
        const unsigned u = pw[i][e];
        a[2 * e] = __builtin_amdgcn_fdot2_f32_bf16(__builtin_bit_cast(bf2, u), lo1, a[2 * e], false);
        a[2 * e + 1] = __builtin_amdgcn_fdot2_f32_bf16(__builtin_bit_cast(bf2, u), hi1, a[2 * e + 1], false);
      }
    }
#pragma unroll
    for (int i = 0; i < 4; ++i) {
      auto r = __builtin_amdgcn_permlane32_swap(__float_as_uint(a[i]), __float_as_uint(a[i + 4]), false, false);
      a[i] = __uint_as_float(r[0]) + __uint_as_float(r[1]);
    }
#pragma unroll
    for (int i = 0; i < 2; ++i) {
      auto r = __builtin_amdgcn_permlane16_swap(__float_as_uint(a[i]), __float_as_uint(a[i + 2]), false, false);
      a[i] = __uint_as_float(r[0]) + __uint_as_float(r[1]);
      a[i] += dpp_ror<8>(a[i]);
    }
    if (!(lane & 8))
      __builtin_amdgcn_raw_buffer_store_b64(u32x2{__float_as_uint(a[0]), __float_as_uint(a[1])}, rsW,
          (unsigned)(WS_CSP + ((((hp + (1 << (w - 4))) & 15) * 16 + rt) * 192 + (w - 4) * 64 + (lane & 7) * 8 + (lane >> 5) * 4 + ((lane >> 4) & 1) * 2) * 4), 0, 16);
  }
  }
}

#undef pcs
#undef prs
#undef opo
#undef ogo
#undef pkw
#undef hof
#undef dm
#undef dm2
#undef pcv
#undef pwf
#undef prv
#undef pvf
#undef ppk
#undef ppa
#undef pvr
#undef gkv
#undef gkz

__device__ __forceinline__ void g2_phase(char* smem, char* ws, unsigned P, u32x4& nv, u64& nk, u64 E, int w, f32x16& xq, int layer, const u16* __restrict__ x0,
                                         const u16* __restrict__ wo, const u16* __restrict__ wnx, u16* __restrict__ out) {
  IDS
  wnx = uni(wnx);
  const unsigned sb = lds_addr(smem);
  const int lw = w & 3, k2 = 16 * lw + (lane >> 3);
  const unsigned vW0 = (unsigned)((k2 * DM + hp * 64 + ((lane & 7) ^ ((lane >> 4) << 1)) * 8) * 2), vW1 = vW0 + 8 * DM * 2 - 1024u;
  const int r0 = w * 16 + (lane >> 3);
  unsigned pxr = sb + r0 * 128 + (lane & 7) * 16;
  const unsigned rmo = (unsigned)(((m0 + r0) * DM + hp * 64 + ((lane & 7) ^ (lane >> 3)) * 8) * 2);
  const unsigned imo = SX + (unsigned)(hp * 32768 + (w >> 2) * 8192 + r0 * 128 + (lane & 7) * 16);
  constexpr int S0 = 0, S1 = 16384, S2 = 32768, S3 = 147456, S4 = OFF_WO, S5 = 98304, S6 = 114688;
  constexpr int AS[16] = {S0, S1, S2, S3, S0, S1, S4, S2, S3, S0, S1, S4, S5, S3, S0, S6};
  constexpr int OFF_C = 0;
  auto bs = [](int t) { return OFF_WO + (t < 12 ? t : t - 10) * 8192; };
  if (w >= 4) {
    constexpr int WT[15] = {0, 8, 12, 14, 16, 18, 20, 18, 16, 14, 12, 12, 10, 10, 8};
    const char* o = ws + WS_O + layer * (SEQ * DM * 2) + SX;
    const unsigned vA = (unsigned)((lw >> 1) * 16384 + (lw & 1) * 4096 + lane * 16);
    const u64 kord = korder(bid & 3, hp);
    auto ldA = [&](int s) { glds4c(o + knib(kord, s) * 32768, vA, sb + AS[s] + lw * 4096); };
    auto ldW = [&](int t) { glds2v(wo + (size_t)knib(kord, t) * 64 * DM, vW0, vW1, sb + bs(t) + lw * 2048); };
    const int fB = (k2 >> 1) & 1;
    unsigned vB0 = (unsigned)((k2 * 3072 + hp * 64 + (((((lane & 7) >> 1) ^ fB) << 1) | (lane & 1)) * 8) * 2);
    unsigned vB1 = (unsigned)(((k2 + 8) * 3072 + hp * 64 + (((((lane & 7) >> 1) ^ (fB | 2)) << 1) | (lane & 1)) * 8) * 2) - 1024u;
    nk = korder(kv1(bid), hp);
    const int kn1 = knib(nk, 1);
    nv = u32x4{vB0, vB1, SX + vA, lane < 16 && ((kfirst(nk) >> lane) & 1) ? (WS_FLAGS + (unsigned)(256 + rt * 16 + lane) * FLS) ^ P : 0u};
    asm volatile("" : "+v"(nv));
    asm volatile("" : "+s"(nk));
    vB0 = nv[0]; vB1 = nv[1];
    auto ldB = [&](int s, int part) {
      const u16* b = wnx + (size_t)(s ? kn1 : hp) * 64 * 3072;
      if (part) glds4v(b, vB0, vB1, vB0, vB1, sb + s * 40960 + 16384 + lw * 6144);
      else glds2v(b + 2048, vB0, vB1, sb + s * 40960 + 16384 + lw * 6144 + 4096);
    };
    BAR;
    ldA(1); ldA(2); ldA(3); ldA(4);
    WAITV(12);
    BAR;
    auto step = [&](auto ktc) {
      constexpr int kt = decltype(ktc)::value;
      WAITVN(WT[kt]);
      BAR;
      if constexpr (kt == 1) { ldA(5); ldA(6); }
      if constexpr (kt >= 2 && kt <= 10) ldA(kt + 5);
      if constexpr (kt >= 2 && kt <= 5) ldW(kt + 10);
      if constexpr (kt >= 11) ldB((kt - 11) >> 1, (kt - 11) & 1);
      if constexpr (kt >= 8) { __builtin_amdgcn_s_sleep(kt >= 11 ? 2 : 1); if (layer == 1) __builtin_amdgcn_s_sleep(1); }
    };
    step(IC<1>{}); step(IC<2>{}); step(IC<3>{}); step(IC<4>{}); step(IC<5>{}); step(IC<6>{}); step(IC<7>{});
    step(IC<8>{}); step(IC<9>{}); step(IC<10>{}); step(IC<11>{}); step(IC<12>{}); step(IC<13>{}); step(IC<14>{});
  } else {
    const int wm = w >> 1, wn = w & 1;
    f32x4 acc[4][2];
    unsigned co[2];
#pragma unroll
    for (int cb = 0; cb < 2; ++cb) co[cb] = sb + OFF_C + (unsigned)((wm * 64 + fr) * 128 + (((wn * 4 + cb * 2 + (g >> 1)) ^ (fr & 7)) << 4) + (g & 1) * 8);
    const int fl = (q4 >> 1) | ((g & 1) << 1);
    const int bb0 = (4 * g + q4) * 128 + p4 * 8 + (((wn * 2) ^ fl) << 5), bb1 = (4 * g + q4) * 128 + p4 * 8 + (((wn * 2 + 1) ^ fl) << 5);
    const int abase = wm * 4096 + lane * 16;
    bf16x8 fa[2][2][4], fb[2][2][2];
    unsigned ra[3], rb0[2], rb1[2];
#pragma unroll
    for (int i = 0; i < 3; ++i) { ra[i] = abase + i * 65536; asm volatile("" : "+v"(ra[i])); }
#pragma unroll
    for (int i = 0; i < 2; ++i) { rb0[i] = bb0 + OFF_WO + i * 49152; rb1[i] = bb1 + OFF_WO + i * 49152; asm volatile("" : "+v"(rb0[i]), "+v"(rb1[i])); }
    auto loadf = [&](int kt, bf16x8 (&a)[2][4], bf16x8 (&b)[2][2]) {
#pragma unroll
      for (int s = 0; s < 2; ++s) {
        const char* sA = smem + ra[AS[kt] >> 16] + (AS[kt] & 65535) + s * 8192;
        const int hi = bs(kt) >= OFF_WO + 49152, ob = bs(kt) - OFF_WO - hi * 49152 + s * 4096;
        b[s][0] = cat8(tr_read(smem + rb0[hi] + ob), tr_read(smem + rb0[hi] + ob + 2048));
        b[s][1] = cat8(tr_read(smem + rb1[hi] + ob), tr_read(smem + rb1[hi] + ob + 2048));
#pragma unroll
        for (int rb = 0; rb < 4; ++rb) a[s][rb] = *(const bf16x8*)(sA + rb * 1024);
      }
    };
    auto mma = [&](bf16x8 (&a)[2][4], bf16x8 (&b)[2][2]) {
#pragma unroll
      for (int i = 0; i < 16; ++i) {
        const int rb = i >> 2, cb = ((i >> 1) ^ rb) & 1, s = ((i + 1) >> 1) & 1;
        acc[rb][cb] = MFMA(b[s][cb], a[s][rb], acc[rb][cb]);
      }
    };
    unsigned fo = lane < 16 && lane != hp ? (WS_FLAGS + (unsigned)(rt * 16 + lane) * FLS) ^ P : 0u;
    const u64 ko = korder(bid & 3, hp);
    unsigned kf = kfirst(ko), kf2 = kfirst(ko >> 16) | (1u << knib(ko, 9));
    unsigned jo = (unsigned)((((rt & 7) * 128 + (w - 1) * 64 + lane) * 3072 + (hp & 3) * 1024 + (hp | 3) * 64) * 2);
    unsigned fo1 = ((kf >> (lane & 31)) & 1) ? fo : 0u, fo2 = ((kf2 >> (lane & 31)) & 1) ? fo : 0u;
    fo = (((kf | kf2) >> (lane & 31)) & 1) ? 0u : fo;
    asm volatile("" : "+v"(fo), "+v"(fo1), "+v"(fo2));
    asm volatile("" : "+s"(kf), "+s"(kf2));
    publish(rsW, tid, rt * 16 + hp, E + (unsigned)(layer + 1), P);
    ST(layer * 24 + 13)
    u64 fl1 = 0;
    if (w == 0) asm volatile("global_load_dwordx2 %0, %1, %2 sc1" : "+v"(fl1) : "v"(fo1), "s"(ws) : "memory");
    if (layer == 0) {
      const unsigned xo = (unsigned)(((m0 + wm * 64 + fr) * DM + hp * 64 + wn * 32 + g * 8) * 2);
      asm volatile("global_load_dwordx4 v[64:67], %1, %3\n\tglobal_load_dwordx4 v[68:71], %1, %4\n\tglobal_load_dwordx4 v[72:75], %2, %3\n\tglobal_load_dwordx4 v[76:79], %2, %4"
                   : "+{v[64:79]}"(xq) : "v"(xo), "v"(xo + 32 * DM * 2), "s"(x0), "s"(x0 + 16 * DM) : "memory");
    }
    loadf(0, fa[0], fb[0]);
    unsigned junk = lane;
    if (layer < 3 && (hp & 7) < 3 && (w == 1 || w == 2))
      asm volatile("global_load_dword %0, %1, %2" : "+v"(junk) : "v"(jo), "s"(wnx) : "memory");
    if (layer == 0 && w == 3) {
      CODE_TOUCH(2, ".Lmk_g1b", ".Lmk_g1a")
      asm volatile("global_load_dword %0, %1, %2" : "+v"(junk) : "v"(v), "s"(pc) : "memory");
    }
    if (layer == 1 && w == 3) {
      CODE_TOUCH(3, ".Lmk_f0", ".Lmk_f1")
      asm volatile("global_load_dword %0, %1, %2" : "+v"(junk) : "v"(v), "s"(pc) : "memory");
    }
    __builtin_amdgcn_sched_barrier(0);
    bf16x8 id[2];
#pragma unroll
    for (int cb = 0; cb < 2; ++cb) {
      const unsigned one = (g >> 1) == cb && (g & 1) == (fr >> 3) ? 0x3F80u << ((fr & 1) * 16) : 0u;
      u32x4 t;
#pragma unroll
      for (int j = 0; j < 4; ++j) t[j] = ((fr >> 1) & 3) == j ? one : 0u;
      id[cb] = __builtin_bit_cast(bf16x8, t);
      asm volatile("" : "+v"(id[cb]));
    }
    asm volatile("" : "+v"(co[0]), "+v"(co[1]));
#pragma unroll
    for (int rb = 0; rb < 4; ++rb) { acc[rb][0] = f32x4{0, 0, 0, 0}; acc[rb][1] = f32x4{0, 0, 0, 0}; }
    __builtin_amdgcn_sched_barrier(0);
    WAITL0;
    u64 miss = 0;
    if (w == 0) { ARRIVED("+v"(fl1)); if (__ballot(fl1 != E + (unsigned)(layer + 1)) & kf) poll_eq(ws, fo1, E + (unsigned)(layer + 1), kf, kf); miss = 0xffffu & ~(kf | kf2 | (1u << hp)); }
    ST(layer * 24 + 14)
    __builtin_amdgcn_sched_barrier(0);
    BAR;
    __builtin_amdgcn_sched_barrier(0);
    asm volatile("" ::: "memory");
    if (w == 0) asm volatile("global_load_dwordx2 %0, %1, %2 sc1" : "+v"(fl1) : "v"(fo2), "s"(ws) : "memory");
    mma(fa[0], fb[0]);
    __builtin_amdgcn_sched_barrier(0);
    if (layer == 0) WAITV(0);
    __builtin_amdgcn_sched_barrier(0);
    asm volatile("" : "+{v[64:79]}"(xq));
    const bf16x8 xf[4] = {__builtin_bit_cast(bf16x8, quad<0>(xq)), __builtin_bit_cast(bf16x8, quad<1>(xq)), __builtin_bit_cast(bf16x8, quad<2>(xq)), __builtin_bit_cast(bf16x8, quad<3>(xq))};
#pragma unroll
    for (int rb = 0; rb < 4; ++rb) { acc[rb][0] = MFMA(id[0], xf[rb], acc[rb][0]); acc[rb][1] = MFMA(id[1], xf[rb], acc[rb][1]); }
    if (w == 0) { ARRIVED("+v"(fl1)); if (__ballot(fl1 != E + (unsigned)(layer + 1)) & kf2) poll_eq(ws, fo2, E + (unsigned)(layer + 1), kf2, kf2); }
    u64 fl2 = 0;
    auto step = [&](auto ktc) {
      constexpr int kt = decltype(ktc)::value;
      __builtin_amdgcn_sched_barrier(0);
      if (kt == 5 && miss) {
        ARRIVED("+v"(fl2));
        if (__ballot(fl2 != E + (unsigned)(layer + 1)) & miss) poll_eq(ws, fo, E + (unsigned)(layer + 1), miss, ~0ull);
      }
      WAITL0;
      BAR;
      if (kt == 2 && miss) asm volatile("global_load_dwordx2 %0, %1, %2 sc1" : "+v"(fl2) : "v"(fo), "s"(ws) : "memory");
      if (kt == 0) { ST(layer * 24 + 15) }
      __builtin_amdgcn_sched_barrier(0);
      loadf(kt + 1, fa[(kt + 1) & 1], fb[(kt + 1) & 1]);
      if (kt == 0) return;
      mma(fa[kt & 1], fb[kt & 1]);
#pragma unroll
      for (int i = 0; i < 4; ++i) {
        __builtin_amdgcn_sched_group_barrier(0x008, 1, 0);
        __builtin_amdgcn_sched_group_barrier(0x100, 1, 0);
        __builtin_amdgcn_sched_group_barrier(0x008, 1, 0);
        __builtin_amdgcn_sched_group_barrier(0x100, 3, 0);
      }
#pragma unroll
      for (int i = 0; i < 8; ++i) __builtin_amdgcn_sched_group_barrier(0x008, 1, 0);
    };
    step(IC<0>{}); step(IC<1>{}); step(IC<2>{}); step(IC<3>{}); step(IC<4>{}); step(IC<5>{}); step(IC<6>{}); step(IC<7>{});
    step(IC<8>{}); step(IC<9>{}); step(IC<10>{}); step(IC<11>{}); step(IC<12>{}); step(IC<13>{}); step(IC<14>{});
    WAITV(0);
    asm volatile("" :: "v"(junk));
    __builtin_amdgcn_sched_barrier(0);
    auto put = [&](int rb) {
#pragma unroll
      for (int cb = 0; cb < 2; ++cb) lds<u32x2>(co[cb] + rb * 2048) = u32x2{pk2(acc[rb][cb][0], acc[rb][cb][1]), pk2(acc[rb][cb][2], acc[rb][cb][3])};
    };
#pragma unroll
    for (int rb = 0; rb < 4; ++rb)
#pragma unroll
      for (int cb = 0; cb < 2; ++cb) acc[rb][cb] = MFMA(fb[1][0][cb], fa[1][0][rb], acc[rb][cb]);
#pragma unroll
    for (int rb = 0; rb < 4; ++rb) {
      __builtin_amdgcn_sched_barrier(0);
#pragma unroll
      for (int cb = 0; cb < 2; ++cb) acc[rb][cb] = MFMA(fb[1][1][cb], fa[1][1][rb], acc[rb][cb]);
      __builtin_amdgcn_sched_barrier(0);
      if (rb >= 2) put(rb - 2);
    }
    __builtin_amdgcn_sched_barrier(0);
    ST(layer * 24 + 16)
    put(2); put(3);
  }
  WAITL0; BAR;
  asm volatile("" ::: "memory");
  u32x4 xn[2];
#pragma unroll
  for (int i = 0; i < 2; ++i) xn[i] = lds<u32x4>(pxr + OFF_C + i * 1024);
  if (layer == 3) {
    const __amdgpu_buffer_rsrc_t rsO = __builtin_amdgcn_make_buffer_rsrc((void*)uni(out), (short)0, SEQ * DM * 2, 0x00020000);
    __builtin_amdgcn_raw_buffer_store_b128(xn[0], rsO, rmo, 0, 16);
    __builtin_amdgcn_raw_buffer_store_b128(xn[1], rsO, rmo + 8 * DM * 2, 0, 16);
  } else {
    __builtin_amdgcn_raw_buffer_store_b128(xn[0], rsW, WS_X + layer * (SEQ * DM * 2) + imo, 0, 16);
    __builtin_amdgcn_raw_buffer_store_b128(xn[1], rsW, WS_X + layer * (SEQ * DM * 2) + imo + 1024, 0, 16);
    float mean[2], m2[2];
#pragma unroll
    for (int i = 0; i < 2; ++i) {
      typedef __attribute__((ext_vector_type(2))) __bf16 bf2;
      const bf2 one2 = __builtin_bit_cast(bf2, 0x3F803F80u);
      float sa = zero1(), sq = zero1();
#pragma unroll
      for (int e = 0; e < 4; ++e) {
        const unsigned u = xn[i][e];
        const bf2 t = __builtin_bit_cast(bf2, u);
        sa = __builtin_amdgcn_fdot2_f32_bf16(t, one2, sa, false);
        sq = __builtin_amdgcn_fdot2_f32_bf16(t, t, sq, false);
      }
      mean[i] = sa;
      m2[i] = sq;
    }
#define DPA(c) "v_add_f32_dpp %0, %0, %0 " c "\n\tv_add_f32_dpp %1, %1, %1 " c "\n\tv_add_f32_dpp %2, %2, %2 " c "\n\tv_add_f32_dpp %3, %3, %3 " c "\n\t"
    asm volatile("s_nop 3\n\t" DPA("quad_perm:[1,0,3,2] row_mask:0xf bank_mask:0xf") DPA("quad_perm:[2,3,0,1] row_mask:0xf bank_mask:0xf")
                 DPA("row_half_mirror row_mask:0xf bank_mask:0xf") "s_nop 0" : "+v"(mean[0]), "+v"(m2[0]), "+v"(mean[1]), "+v"(m2[1]));
    if ((lane & 7) < 2) {
      const int i = lane & 1;
      const u32x2 sv = {__float_as_uint(i ? mean[1] : mean[0]), __float_as_uint(i ? m2[1] : m2[0])};
      __builtin_amdgcn_raw_buffer_store_b64(sv, rsW, WS_ST + layer * (SEQ * 128) + (unsigned)((rt & 7) * 32768 + (hp >> 3) * 16384 + (hp & 7) * 1024 + (r0 + i * 8) * 8) + ((rt >> 3) * 8192 ^ P), 0, 16);
    }
  }
  ST(layer * 24 + 17)
  ST(layer * 24 + 18)
}

__global__ __launch_bounds__(512, 2) void mega_kernel(const u16* __restrict__ x0, const u16* __restrict__ wqkvA,
                                                      const u16* __restrict__ woA, const u16* __restrict__ wqkvB,
                                                      const u16* __restrict__ wphi, const u16* __restrict__ woB,
                                                      u16* __restrict__ out, char* __restrict__ ws) {
  unsigned P = 0;
  extern __shared__ __attribute__((aligned(16))) char smem[];
  const int w = __builtin_amdgcn_readfirstlane(threadIdx.x >> 6);
  u64 E = 0;
  {
    const int bid = blockIdx.x, lane = threadIdx.x & 63;
    const int rt = 2 * (bid & 4) + ((bid >> 3) & 7), hp = 4 * (bid & 3) + (bid >> 6), m0 = rt * 128;
    if (w < 4) {
      if (w == 3) {
        unsigned long long pc;
        unsigned len;
        asm volatile("s_getpc_b64 %0\n.Lmk_pc:\n\ts_mov_b32 %1, .Lmk_end-.Lmk_pc" : "=s"(pc), "=s"(len));
        const unsigned per = ((len >> 10) + 1) * 128, off = ((bid >> 3) & 7) * per + (lane * 128 < per ? lane * 128 : 0);
        asm volatile("s_mov_b32 m0, %2\n\ts_nop 0\n\tglobal_load_lds_dword %0, %1" :: "v"(off < len - 4 ? off : 0u), "s"(pc), "s"(lds_addr(smem) + LDS_TOTAL - 1024) : "memory", "m0");
      }
      const unsigned xo = (unsigned)((m0 + hp * 8 + w * 2) * (DM * 2) + lane * 16);
      glds4v(x0, xo, xo, xo, xo, lds_addr(smem) + 122880 + w * 4096);
      asm volatile("s_mov_b32 m0, %2\n\ts_nop 0\n\tglobal_load_lds_dword %0, %1" :: "v"(lane * 4), "s"(ws), "s"(lds_addr(smem) + OFF_E) : "memory", "m0");
    } else {
      const int lw = w & 3;
      const int kB = 16 * lw + (lane >> 3), fB = (kB >> 1) & 1;
      const unsigned vB0 = (unsigned)((kB * 3072 + hp * 64 + (((((lane & 7) >> 1) ^ fB) << 1) | (lane & 1)) * 8) * 2);
      const unsigned vB1 = (unsigned)(((kB + 8) * 3072 + hp * 64 + (((((lane & 7) >> 1) ^ (fB | 2)) << 1) | (lane & 1)) * 8) * 2) - 1024u;
      const unsigned vA = (unsigned)(((m0 + lw * 32 + (lane >> 3)) * DM + ((lane & 7) ^ (lane >> 3)) * 8) * 2);
#pragma unroll
      for (int s = 0; s < 1; ++s) {
        const int kt = (rt & 8) + s;
        const unsigned sA = lds_addr(smem) + s * 40960, sB = sA + 16384;
        glds4v(x0 + kt * 64, vA, vA + 15360u, vA + 2 * 15360u, vA + 3 * 15360u, sA + lw * 4096);
        const u16* b = wqkvA + (size_t)kt * 64 * 3072;
        glds4v(b, vB0, vB1, vB0, vB1, sB + lw * 6144);
        glds2v(b + 2048, vB0, vB1, sB + lw * 6144 + 4096);
      }
    }
  }
  f32x16 xq;
  u32x4 nv;
  u64 nk;
  asm volatile("" : "=v"(nv));
  asm volatile("" : "=s"(nk));
#pragma unroll 1
  for (int layer = 0; layer < 4; ++layer) {
    if (layer & 1) g1_phase<1>(smem, ws, P, nv, nk, E, w, xq, layer, x0, wqkvB, woB, wphi, nullptr, nullptr);
    else g1_phase<0>(smem, ws, P, nv, nk, E, w, xq, layer, x0, wqkvA, woA, nullptr, wqkvB, woB);
    g2_phase(smem, ws, P, nv, nk, E, w, xq, layer, x0, (layer & 1) ? woB : woA, (layer & 1) ? wqkvA : wqkvB, out);
  }
  asm volatile(".Lmk_end:");
  WAITV(0);
  if (blockIdx.x == 0 && w == 0 && lane_id() == 0) __hip_atomic_store((gu64*)ws, E + 16u, __ATOMIC_RELAXED, __HIP_MEMORY_SCOPE_AGENT);
}

extern "C" void kernel_launch(void* const* d_in, const int* in_sizes, int n_in,
                              void* d_out, int out_size, void* d_ws, size_t ws_size,
                              hipStream_t stream) {
  hipFuncSetAttribute((const void*)mega_kernel, hipFuncAttributeMaxDynamicSharedMemorySize, LDS_TOTAL);
  mega_kernel<<<256, 512, LDS_TOTAL, stream>>>((const u16*)d_in[0], (const u16*)d_in[1], (const u16*)d_in[2], (const u16*)d_in[4],
                                               (const u16*)d_in[5], (const u16*)d_in[6], (u16*)d_out, (char*)d_ws);
}
```

```cpp
#include <hip/hip_runtime.h>
#include <hip/hip_bf16.h>
#include <stdint.h>
#include <type_traits>
#pragma clang diagnostic ignored "-Winline-asm"
#pragma clang diagnostic ignored "-Wunused-value"

typedef unsigned short u16;
typedef __attribute__((ext_vector_type(8))) short bf16x8;
typedef __attribute__((ext_vector_type(4))) short bf16x4;
typedef __attribute__((ext_vector_type(4))) float f32x4;
typedef __attribute__((ext_vector_type(4))) unsigned u32x4;
typedef __attribute__((ext_vector_type(2))) unsigned u32x2;
typedef __attribute__((ext_vector_type(8))) float f32x8;
typedef __attribute__((ext_vector_type(16))) unsigned u32x16;
typedef __attribute__((ext_vector_type(16))) float f32x16;
typedef __attribute__((ext_vector_type(32))) float f32x32;
template <int I, typename T> __device__ __forceinline__ f32x4 quad(const T& v) { return __builtin_shufflevector(v, v, 4 * I, 4 * I + 1, 4 * I + 2, 4 * I + 3); }

constexpr int SEQ = 2048, DM = 1024, NH = 32;
#define ST(i)

typedef __attribute__((ext_vector_type(2))) float f32x2;
typedef __attribute__((ext_vector_type(2))) __bf16 bf16x2_t;
__device__ __forceinline__ unsigned pk2(float a, float b) {
  f32x2 v = {a, b};
  bf16x2_t r = __builtin_convertvector(v, bf16x2_t);
  return __builtin_bit_cast(unsigned, r);
}

__device__ __forceinline__ bf16x4 tr_read(const char* p) {
  return __builtin_amdgcn_ds_read_tr16_b64_v4i16((__attribute__((address_space(3))) bf16x4*)p);
}
template <typename T> __device__ __forceinline__ __attribute__((address_space(3))) T& lds(unsigned a) { return *(__attribute__((address_space(3))) T*)(size_t)a; }
__device__ __forceinline__ bf16x4 tr_read(unsigned a) { return __builtin_amdgcn_ds_read_tr16_b64_v4i16(&lds<bf16x4>(a)); }
__device__ __forceinline__ bf16x8 cat8(bf16x4 a, bf16x4 b) {
  bf16x8 f = {a[0], a[1], a[2], a[3], b[0], b[1], b[2], b[3]};
  return f;
}
__device__ __forceinline__ void glds16s(const void* sbase, unsigned voff, unsigned lds_base) {
  asm volatile("s_mov_b32 m0, %2\n\ts_nop 0\n\tglobal_load_lds_dwordx4 %0, %1" :: "v"(voff), "s"(sbase), "s"(lds_base) : "memory", "m0");
}
__device__ __forceinline__ void glds16c(const void* sbase, unsigned voff, unsigned lds_base) {
  asm volatile("s_mov_b32 m0, %2\n\ts_nop 0\n\tglobal_load_lds_dwordx4 %0, %1 sc1" :: "v"(voff), "s"(sbase), "s"(lds_base) : "memory", "m0");
}
#define GL_ "\n\tglobal_load_lds_dwordx4 "
__device__ __forceinline__ void glds4c(const void* sbase, unsigned v, unsigned lds_base) {
  asm volatile("s_mov_b32 m0, %2\n\ts_nop 0" GL_ "%0, %1 sc1" GL_ "%0, %1 offset:1024 sc1" GL_ "%0, %1 offset:2048 sc1" GL_ "%0, %1 offset:3072 sc1"
               :: "v"(v), "s"(sbase), "s"(lds_base) : "memory", "m0");
}
__device__ __forceinline__ void glds4v(const void* sbase, unsigned v0, unsigned v1, unsigned v2, unsigned v3, unsigned lds_base) {
  asm volatile("s_mov_b32 m0, %5\n\ts_nop 0" GL_ "%0, %4" GL_ "%1, %4 offset:1024" GL_ "%2, %4 offset:2048" GL_ "%3, %4 offset:3072"
               :: "v"(v0), "v"(v1), "v"(v2), "v"(v3), "s"(sbase), "s"(lds_base) : "memory", "m0");
}
__device__ __forceinline__ void glds2v(const void* sbase, unsigned v0, unsigned v1, unsigned lds_base) {
  asm volatile("s_mov_b32 m0, %3\n\ts_nop 0" GL_ "%0, %2" GL_ "%1, %2 offset:1024" :: "v"(v0), "v"(v1), "s"(sbase), "s"(lds_base) : "memory", "m0");
}
__device__ __forceinline__ void gld16(u32x4& d, unsigned voff, const void* sbase) {
  asm volatile("global_load_dwordx4 %0, %1, %2" : "=v"(d) : "v"(voff), "s"(sbase) : "memory");
}
__device__ __forceinline__ unsigned lds_addr(const void* p) { return (unsigned)(size_t)p; }
#define WAITV(n) asm volatile("s_waitcnt vmcnt(" #n ")" ::: "memory")
#define WAITVN(n) asm volatile("s_waitcnt vmcnt(%0)" :: "n"(n) : "memory")
#define WAITL0 do { asm volatile("" ::: "memory"); __builtin_amdgcn_s_waitcnt(0xC07F); asm volatile("" ::: "memory"); } while (0)
#define BAR __builtin_amdgcn_s_barrier()
#define ARRIVED(...) do { WAITV(0); __builtin_amdgcn_sched_barrier(0); asm volatile("" : __VA_ARGS__); } while (0)

__device__ __forceinline__ float vmaxf(float a, float b) { float r; asm("v_max_f32 %0, %1, %2" : "=v"(r) : "v"(a), "v"(b)); return r; }
template <int N> __device__ __forceinline__ float dpp_ror(float x) {
  return __uint_as_float(__builtin_amdgcn_update_dpp(0u, __float_as_uint(x), 0x120 + N, 0xf, 0xf, false));
}
__device__ __forceinline__ float r16_sum(float x) { x += dpp_ror<8>(x); x += dpp_ror<4>(x); x += dpp_ror<2>(x); return x + dpp_ror<1>(x); }
__device__ __forceinline__ float vmax3f(float a, float b, float c) { float r; asm("v_max3_f32 %0, %1, %2, %3" : "=v"(r) : "v"(a), "v"(b), "v"(c)); return r; }
__device__ __forceinline__ void max8pn(const f32x4& a, const f32x4& b, float& mx, float& mn) {
  asm("v_max3_f32 %0, %2, %3, %4\n\tv_max3_f32 %1, -%2, -%3, -%4\n\tv_max3_f32 %0, %0, %5, %6\n\tv_max3_f32 %1, %1, -%5, -%6\n\t"
      "v_max3_f32 %0, %0, %7, %8\n\tv_max3_f32 %1, %1, -%7, -%8\n\tv_max_f32 %0, %0, %9\n\tv_max_f32 %1, %1, -%9\n\ts_nop 0"
      : "=&v"(mx), "=&v"(mn) : "v"(a[0]), "v"(a[1]), "v"(a[2]), "v"(a[3]), "v"(b[0]), "v"(b[1]), "v"(b[2]), "v"(b[3]));
}
__device__ __forceinline__ float vmaxf_n(float a, float b) { float r; asm("v_max_f32 %0, %1, %2\n\ts_nop 0" : "=v"(r) : "v"(a), "v"(b)); return r; }
__device__ __forceinline__ float vmax3f_n(float a, float b, float c) { float r; asm("v_max3_f32 %0, %1, %2, %3\n\ts_nop 0" : "=v"(r) : "v"(a), "v"(b), "v"(c)); return r; }
template <bool MAXOP> __device__ __forceinline__ float rop(float a, float b) { return MAXOP ? vmaxf_n(a, b) : a + b; }
template <bool MAXOP> __device__ __forceinline__ void xr2(float& a, float& b) {
  auto r = __builtin_amdgcn_permlane16_swap(__float_as_uint(a), __float_as_uint(b), false, false);
  unsigned u = __float_as_uint(rop<MAXOP>(__uint_as_float(r[0]), __uint_as_float(r[1])));
  auto r2 = __builtin_amdgcn_permlane32_swap(u, u, false, false);
  u = __float_as_uint(rop<MAXOP>(__uint_as_float(r2[0]), __uint_as_float(r2[1])));
  auto r3 = __builtin_amdgcn_permlane16_swap(u, u, false, false);
  a = __uint_as_float(r3[0]); b = __uint_as_float(r3[1]);
}
template <bool MAXOP> __device__ __forceinline__ void xr4(float& a, float& b, float& c, float& d) {
  auto r = __builtin_amdgcn_permlane16_swap(__float_as_uint(a), __float_as_uint(b), false, false);
  auto q = __builtin_amdgcn_permlane16_swap(__float_as_uint(c), __float_as_uint(d), false, false);
  const unsigned ab = __float_as_uint(rop<MAXOP>(__uint_as_float(r[0]), __uint_as_float(r[1])));
  const unsigned cd = __float_as_uint(rop<MAXOP>(__uint_as_float(q[0]), __uint_as_float(q[1])));
  auto t = __builtin_amdgcn_permlane32_swap(ab, cd, false, false);
  const unsigned u = __float_as_uint(rop<MAXOP>(__uint_as_float(t[0]), __uint_as_float(t[1])));
  auto h = __builtin_amdgcn_permlane32_swap(u, u, false, false);
  auto p0 = __builtin_amdgcn_permlane16_swap(h[0], h[0], false, false);
  auto p1 = __builtin_amdgcn_permlane16_swap(h[1], h[1], false, false);
  a = __uint_as_float(p0[0]); b = __uint_as_float(p0[1]); c = __uint_as_float(p1[0]); d = __uint_as_float(p1[1]);
}

__device__ __forceinline__ unsigned xs4(float a, float b, float c, float d) {
  auto r = __builtin_amdgcn_permlane16_swap(__float_as_uint(a), __float_as_uint(b), false, false);
  auto q = __builtin_amdgcn_permlane16_swap(__float_as_uint(c), __float_as_uint(d), false, false);
  auto t = __builtin_amdgcn_permlane32_swap(__float_as_uint(__uint_as_float(r[0]) + __uint_as_float(r[1])), __float_as_uint(__uint_as_float(q[0]) + __uint_as_float(q[1])), false, false);
  return __float_as_uint(__uint_as_float(t[0]) + __uint_as_float(t[1]));
}

#define MFMA(a, b, c) __builtin_amdgcn_mfma_f32_16x16x32_bf16(a, b, c, 0, 0, 0)

typedef unsigned long long u64;
typedef __attribute__((address_space(1))) unsigned long long gu64;
constexpr unsigned WS_PL = 0x1a0000, WS_PR = 1024;
constexpr unsigned WS_FLAGS = 16384;
constexpr int FLS = 64;
constexpr unsigned WS_G0 = 65536;
constexpr unsigned WS_CSP = WS_G0 + SEQ * 16;
constexpr unsigned WS_ST = WS_CSP + 16 * 16 * 768;
constexpr unsigned WS_X = 1u << 21;
constexpr unsigned WS_O = WS_X + 3u * SEQ * DM * 2;
constexpr unsigned EMPTY = 0xffffffffu;
constexpr unsigned WS_KV = WS_O + 4u * SEQ * DM * 2;
constexpr unsigned KVLB = 256u * 16384;
constexpr unsigned WS_KF = 0x110000, KFLB = 17u * 16384;
constexpr unsigned WS_HL = WS_KV + 2u * KVLB;
static_assert(WS_ST % 16384 == 0 && WS_ST + 3u * SEQ * 128 <= WS_KF && WS_KF + 2 * KFLB <= WS_PL && WS_PL + 16384 <= WS_X, "first 2 MB");
static_assert((WS_X >> 21 & 1) && (WS_O >> 21 & 1) && (WS_KV >> 21 & 1) && (WS_HL >> 21 & 1) && WS_HL % (1u << 21) == 0, "odd multiples of 2 MB");
constexpr unsigned WS_CSA = WS_HL + 512u * 4096;
constexpr unsigned WS_CPA = WS_CSA + 256u * 1024;
constexpr unsigned WS_END = WS_CPA + 16 * 16 * 768;
constexpr int LDS_TOTAL = 163840;
constexpr int OFF_E = 139264;
constexpr int OFF_WO = 49152;
template <int V> using IC = std::integral_constant<int, V>;
__device__ __forceinline__ bf16x8 ones8() { unsigned e = 0x3F803F80u; asm volatile("" : "+v"(e)); return __builtin_bit_cast(bf16x8, u32x4{e, e, e, e}); }
__device__ __forceinline__ float zero1() { float z; asm volatile("v_mov_b32 %0, 0" : "=v"(z)); return z; }
__device__ __forceinline__ u32x4 empty4() { unsigned e = EMPTY; asm volatile("" : "+v"(e)); return u32x4{e, e, e, e}; }
__device__ __forceinline__ u64 korder(int v, int own) {
  const u64 t = v == 0 ? 0x3bf7210edca98654ull : v == 1 ? 0xb37f654210edca98ull : v == 2 ? 0x3bf7a98654210edcull : 0xb37fedca98654210ull;
  const u64 inv = v == 0 ? 0xd876e543c210fba9ull : v == 1 ? 0xc543f210dba9e876ull : v == 2 ? 0xd210eba9c876f543ull : 0xcba9f876d543e210ull;
  const u64 low = (1ull << (4 * ((inv >> (4 * own)) & 15))) - 1ull;
  return (u64)own | ((t & low) << 4) | (t & ~((low << 4) | 15ull));
}
__device__ __forceinline__ u64 korder0(int rt) {
  const int r = rt & 7;
  const u64 own = (0x765432765432ull >> (8 * ((0x4924 >> (2 * r)) & 3))) & 0xffffffull, oth = (0x7654321076543210ull >> (4 * r)) & 0xffffffffull;
  const u64 t = 0x10ull | (oth << 8) | (own << 40);
  return t | (rt < 8 ? 0x0000008888888800ull : 0x8888880000000088ull);
}
__device__ __forceinline__ int knib(u64 t, int s) { return (int)(t >> (4 * s)) & 15; }
__device__ __forceinline__ unsigned kfirst(u64 t) { return (1u << knib(t, 1)) | (1u << knib(t, 2)) | (1u << knib(t, 3)) | (1u << knib(t, 4)); }
__device__ __forceinline__ int kv1(int bid) { return (bid + (0x4924 >> ((bid >> 2) & 14))) & 3; }

template <typename T> __device__ __forceinline__ T* uni(T* p) {
  const unsigned long long v = (unsigned long long)p;
  const unsigned lo = __builtin_amdgcn_readfirstlane((unsigned)v), hi = __builtin_amdgcn_readfirstlane((unsigned)(v >> 32));
  return (T*)(((unsigned long long)hi << 32) | lo);
}
__device__ __forceinline__ u64 uni2(unsigned lo, unsigned hi) {
  lo = __builtin_amdgcn_readfirstlane(lo); hi = __builtin_amdgcn_readfirstlane(hi);
  return ((u64)hi << 32) | lo;
}
__device__ __forceinline__ int lane_id() {
  int l;
  asm volatile("v_mbcnt_lo_u32_b32 %0, -1, 0\n\tv_mbcnt_hi_u32_b32 %0, -1, %0" : "=v"(l));
  __builtin_assume(l >= 0 && l < 64);
  return l;
}
#define CODE_TOUCH(n, from, to) \
  unsigned long long pc; unsigned v; \
  { unsigned o, len; \
    asm volatile("s_getpc_b64 %0\n.Lmk_t" #n ":\n\ts_mov_b32 %1, " from "-.Lmk_t" #n "+0x40000\n\ts_mov_b32 %2, " to "-" from : "=s"(pc), "=s"(o), "=s"(len)); \
    pc -= 0x40000; v = (unsigned)(((bid >> 3) & 7) + lane * 8) * 128; v = o + ((int)v < (int)len ? v : 0u); }
#define IDS \
  const int lane = lane_id(); \
  asm volatile("" : "+s"(w)); \
  const int tid = w * 64 + lane; \
  int bid = blockIdx.x; asm volatile("" : "+s"(bid)); \
  ws = uni(ws); asm volatile("" : "+s"(ws)); \
  layer = __builtin_amdgcn_readfirstlane(layer); x0 = uni(x0); wo = uni(wo); \
    \
  const int rt = 2 * (bid & 4) + ((bid >> 3) & 7); \
  const int hp = 4 * (bid & 3) + (bid >> 6); \
  const int m0 = rt * 128; \
  P = __builtin_amdgcn_readfirstlane(P); \
  const unsigned SX = (unsigned)((rt & 7) * 524288) + ((((rt >> 3) ^ (rt >> 2) ^ 1) & 1) * 8192 ^ P); (void)SX; \
  const int fr = lane & 15, g = lane >> 4; \
  const int q4 = (lane & 15) >> 2, p4 = lane & 3; \
  const __amdgpu_buffer_rsrc_t rsW = __builtin_amdgcn_make_buffer_rsrc((void*)ws, (short)0, (int)WS_END, 0x00020000); \
  (void)m0; (void)fr; (void)q4; (void)p4; (void)rsW; (void)g;

__device__ __forceinline__ void publish(__amdgpu_buffer_rsrc_t rsW, int tid, unsigned idx, u64 v, unsigned P) {
  unsigned off = (WS_FLAGS + idx * FLS) ^ P;
  asm volatile("" : "+v"(off), "+v"(v));
  WAITV(0);
  WAITL0; BAR;
  if (tid == 192) __builtin_amdgcn_raw_buffer_store_b64(__builtin_bit_cast(u32x2, v), rsW, off, 0, 16);
  asm volatile("" ::: "memory");
}
__device__ __forceinline__ void poll_flags(char* ws, unsigned P, u64 E, int set, int rt, int hp, int lane, unsigned n, int grp) {
  const int l = lane & 15, g = lane >> 4;
  const int fi = set * 256 + (g == 2 ? l * 16 + hp : rt * 16 + l);
  const unsigned need = g == grp ? n : 0u;
  const unsigned off = need ? (WS_FLAGS + fi * FLS) ^ P : 0u;
  _Pragma("unroll 1") for (unsigned spins = 0; spins < (1u << 18); ++spins) {
    u64 d;
    asm volatile("global_load_dwordx2 %0, %1, %2 sc1\n\ts_waitcnt vmcnt(0)" : "=v"(d) : "v"(off), "s"(ws) : "memory");
    d -= E;
    const bool ok = (need == 0u) || ((unsigned)(d >> 32) == 0u && (unsigned)d >= need && (unsigned)d <= 16u);
    if (__all(ok)) break;
    __builtin_amdgcn_s_sleep(1);
  }
}

__device__ __forceinline__ u64 poll_eq(const char* ws, unsigned off, u64 want, u64 mask, u64 now) {
  u64 miss;
  _Pragma("unroll 1") for (unsigned spins = 0; spins < (1u << 18); ++spins) {
    u64 d;
    asm volatile("global_load_dwordx2 %0, %1, %2 sc1\n\ts_waitcnt vmcnt(0)" : "=v"(d) : "v"(off), "s"(ws) : "memory");
    miss = __ballot(d != want) & mask;
    if (!(miss & now)) break;
    __builtin_amdgcn_s_sleep(1);
  }
  return miss;
}

#define G1_MACROS \
  ".macro G1M rb, j, s, z\n" \
  " .set g1d, 160+24*(\\rb)+4*(\\j)\n .set g1a, 80+40*(\\s)+4*(\\rb)\n .set g1b, 96+40*(\\s)+4*(\\j)\n" \
  " .if (\\j) >= 4\n  .set g1t, g1a\n  .set g1a, g1b\n  .set g1b, g1t\n .endif\n" \
  " .if \\z\n  v_mfma_f32_16x16x32_bf16 v[g1d:g1d+3], v[g1b:g1b+3], v[g1a:g1a+3], 0\n" \
  " .else\n  v_mfma_f32_16x16x32_bf16 v[g1d:g1d+3], v[g1b:g1b+3], v[g1a:g1a+3], v[g1d:g1d+3]\n .endif\n" \
  ".endm\n" \
  ".macro G1RA rb, s, ra, oa\n" \
  " ds_read_b128 v[80+40*(\\s)+4*(\\rb):83+40*(\\s)+4*(\\rb)], \\ra offset:(\\oa)+(((\\rb)*2048)^(4096*(1-%[hh])))\n" \
  ".endm\n" \
  ".macro G1RB k, s, r0, r1, ob\n" \
  " .if (\\k) & 2\n  ds_read_b64_tr_b16 v[96+40*(\\s)+2*(\\k):97+40*(\\s)+2*(\\k)], \\r1 offset:(\\ob)+((\\k)>>2)*2048+((\\k)&1)*512\n" \
  " .else\n  ds_read_b64_tr_b16 v[96+40*(\\s)+2*(\\k):97+40*(\\s)+2*(\\k)], \\r0 offset:(\\ob)+((\\k)>>2)*2048+((\\k)&1)*512\n .endif\n" \
  ".endm\n" \
    \
  ".macro G1BK i, g, r0, r1, ob\n G1RB 4*(\\g)+((\\i)&3), (\\i)>>2, \\r0, \\r1, (\\ob)+((\\i)>>2)*12288\n.endm\n" \
  ".macro G1AK rb, s, a0, a1, oa\n .if \\s\n  G1RA \\rb, 1, \\a1, \\oa\n .else\n  G1RA \\rb, 0, \\a0, \\oa\n .endif\n.endm\n" \
  ".macro G1RDB g, r0, r1, ob\n .set g1i, 0\n .rept 8\n  G1BK g1i, \\g, \\r0, \\r1, \\ob\n  .set g1i, g1i+1\n .endr\n.endm\n" \
  ".macro G1RDA a0, a1, oa\n .set g1i, 0\n .rept 8\n  G1AK (g1i>>1)^2, g1i&1, \\a0, \\a1, \\oa\n  .set g1i, g1i+1\n .endr\n.endm\n" \
    \
  ".macro G1Q g, rb, q, i, z\n G1M \\rb, 2*(\\g)+((((\\i)>>1)^(\\q))&1), (((\\i)+1)>>1)&1, (\\z)&&(((\\i)&1)==0)\n.endm\n" \
    \
  ".macro G1SG g, z, rd, a0, a1, r0, r1, o\n" \
  " .set g1i, 0\n .rept 16\n" \
  "  .if ((\\g) == 0) && (g1i == 8) && (\\rd)\n   s_waitcnt lgkmcnt(7)\n   s_nop 0\n  .endif\n" \
  "  .if ((\\g) == 0) && (g1i == 15)\n   s_waitcnt lgkmcnt(0)\n  .endif\n" \
  "  G1Q \\g, (g1i>>2)^(2-((\\g)&1)), g1i>>2, g1i&3, \\z\n" \
  "  .if \\rd\n" \
  "   .if (\\g) == 0\n    .if g1i < 7\n     G1BK g1i, 2, \\r0, \\r1, \\o\n    .elseif g1i == 8\n     G1BK 7, 2, \\r0, \\r1, \\o\n    .endif\n" \
  "   .elseif (\\g) == 1\n    .if g1i < 8\n     G1BK g1i, 0, \\r0, \\r1, \\o\n    .endif\n" \
  "   .elseif (g1i & 2) == 0\n    G1BK (g1i&1)+2*(g1i>>2), 1, \\r0, \\r1, \\o\n" \
  "   .elseif (\\rd) == 1\n    G1AK (g1i>>2)^2, 1-(g1i&1), \\a0, \\a1, \\o\n   .endif\n" \
  "  .endif\n" \
  "  .set g1i, g1i+1\n .endr\n" \
  ".endm\n" \
    \
    \
  ".macro G1END h, i0, n, l, xs, r0, r1\n" \
  " .set g1i, \\i0\n .rept \\n\n" \
  "  .if (\\l) && (g1i-(\\i0) < 12)\n   .set g1x, ((\\l)>>(4*(g1i-(\\i0))))&15\n" \
  "   .if g1x < 15\n    ds_write_b128 \\xs, v[208+4*g1x:211+4*g1x] offset:(g1x+2*%[hh]*(g1x/6))*1024\n   .endif\n  .endif\n" \
  "  G1Q g1i>>3, 2*(\\h)+(((g1i>>2)^(g1i>>3))&1), g1i>>2, g1i&3, 0\n" \
  "  .if ((\\l) == 0) && (g1i < 8)\n   G1BK g1i, 2, \\r0, \\r1, 0\n  .endif\n" \
  "  .set g1i, g1i+1\n .endr\n" \
  ".endm\n"
#define G1_PURGE \
  ".purgem G1M\n .purgem G1RA\n .purgem G1RB\n .purgem G1BK\n .purgem G1AK\n .purgem G1RDB\n .purgem G1RDA\n .purgem G1Q\n .purgem G1SG\n .purgem G1END\n"
#define G1_CLOBBER \
  "v80", "v81", "v82", "v83", "v84", "v85", "v86", "v87", "v88", "v89", "v90", "v91", "v92", "v93", "v94", "v95", "v96", "v97", "v98", "v99", "v100", \
  "v101", "v102", "v103", "v104", "v105", "v106", "v107", "v108", "v109", "v110", "v111", "v112", "v113", "v114", "v115", "v116", "v117", "v118", \
  "v119", "v120", "v121", "v122", "v123", "v124", "v125", "v126", "v127", "v128", "v129", "v130", "v131", "v132", "v133", "v134", "v135", "v136", \
  "v137", "v138", "v139", "v140", "v141", "v142", "v143", "v144", "v145", "v146", "v147", "v148", "v149", "v150", "v151", "v152", "v153", "v154", \
  "v155", "v156", "v157", "v158", "v159", "v208", "v209", "v210", "v211", "v212", "v213", "v214", "v215", "v216", "v217", "v218", "v219", "v220", \
  "v221", "v222", "v223", "v232", "v233", "v234", "v235", "v236", "v237", "v238", "v239", "v240", "v241", "v242", "v243", "v244", "v245", "v246", "v247"

__device__ __forceinline__ u32x4 g1_frag_addr(unsigned sb, int w, int lane) {
  const int wm = (w >> 1) & 1, wn = w & 1, fr = lane & 15, g = lane >> 4, q4 = fr >> 2, p4 = lane & 3;
  const int fl = (q4 >> 1) | ((g & 1) << 1);
  const unsigned bo = sb + 16384 + (g >> 1) * 6144 + (g & 1) * 1024 + q4 * 128 + p4 * 8;
  const unsigned ar = sb + (wm * 64 + fr) * 128;
  u32x4 r = {ar + ((g ^ (fr & 7)) << 4), ar + (((4 + g) ^ (fr & 7)) << 4), bo + (((wn * 2 + 0) ^ fl) << 5), bo + (((wn * 2 + 1) ^ fl) << 5)};
  asm volatile("" : "+v"(r));
  return r;
}
template <int MODE>
__device__ __forceinline__ void g1_phase(char* smem, char* ws, unsigned& P, u32x4& nv, u64 nk, u64& E, int w, f32x16& xq, int layer, const u16* __restrict__ x0, const u16* __restrict__ W,
                                         const u16* __restrict__ wo, const u16* __restrict__ wphi,
                                         const u16* __restrict__ wn1, const u16* __restrict__ wn2) {
  constexpr bool HH = (MODE == 1);
  constexpr int ABYTES = 128 * 128;
  constexpr int STAGE = ABYTES + 3 * 8192;
  constexpr int OFF_KS = 147456;
  constexpr int OFF_SP = 3 * STAGE;
  constexpr int OFF_RS = !HH ? OFF_SP : OFF_SP + 16384;
  constexpr int OFF_CS = OFF_RS + 1152;
  constexpr int OFF_WP = OFF_CS + 1536;
  constexpr int OFF_JK = LDS_TOTAL - 1024;
  constexpr int OFF_PK = 0;
  constexpr int OFF_VF = OFF_WP + 4096;
  constexpr int OFF_KV = 16384;
  constexpr int OFF_R2 = 135168;
  const int XS = HH ? 98304 + ((w >> 1) & 1) * 16384 + (w & 1) * 4096 : (w & 3) * 12288;
  constexpr int PSTR = 144;
  if constexpr (HH) asm volatile(".Lmk_g1b:"); else asm volatile(".Lmk_g1a:");
  IDS
  W = uni(W); wphi = uni(wphi); wn1 = uni(wn1); wn2 = uni(wn2);
  const int kg = w >> 2, wm = (w >> 1) & 1, wn = w & 1;
  const bool first = !HH && layer == 0;
  f32x4 acc[2][6];
  u32x4 pw[8];
  ST(layer * 24 + 0)
  const int R0 = wm * 64 + (HH ? kg : kg ^ 1) * 32;
  u32x16 pa;
#define pcs pa[0]
#define prs pa[1]
#define opo pa[2]
#define ogo pa[3]
#define pkw pa[5]
#define hof pa[7]
#define dm pa[8]
#define dm2 pa[9]
#define pcv pa[4]
#define pwf pa[5]
#define prv pa[6]
#define pvf pa[7]
#define ppk pa[10]
#define ppa pa[11]
#define pvr pa[12]
#define gkv pa[13]
#define gkz pa[14]
  f32x4 bias[3];
  const unsigned sb = lds_addr(smem);
  auto prep = [&](int part = 3) {
    if (part & 1) {
    pcs = sb + OFF_CS + wn * 128 + g * 16;
    prs = sb + OFF_RS + (R0 + fr) * 8;
    opo = (unsigned)(wn * 8192 + R0 * 64 + lane * 16);
    ogo = opo + WS_O + layer * (SEQ * DM * 2) + SX + (unsigned)(wn * 8192 + hp * 32768);
    opo += sb;
    pcv = sb + OFF_CS + (wn * 32 + 128 + fr) * 4;
    prv = sb + OFF_R2 + (R0 + 4 * g) * 4;
    if (layer < 2) {
      constexpr unsigned kvn = WS_KV + MODE * KVLB;
      dm = kvn + (unsigned)((rt * 16 + hp) * 16384 + (w >> 2) * 8192 + (tid & 255) * 16);
      dm2 = w >= 4 ? WS_HL + (unsigned)((1 << 20) + ((rt & 7) * 8 + (hp >> 1)) * 16384 + (rt >> 3) * 8192 + (hp & 1) * 4096 + (tid - 256) * 16)
          : tid < 16 ? kvn + (unsigned)((rt * 16 + hp) * 16384 + (tid >> 3) * 8192 + 4096 + (tid & 7) * 16)
          : tid < 18 ? WS_KF + MODE * KFLB + (unsigned)(16 * 16384 + (tid & 1) * 8192 + hp * 256 + rt * 16)
          : tid >= 64 && tid < 128 ? WS_KF + MODE * KFLB + (unsigned)(hp * 16384 + (tid & 32) * 256 + rt * 512 + (tid & 31) * 16) : 0x7fff0000u;
    }
    }
    if (!(part & 2)) {
    } else if constexpr (!HH) {
      pkw = sb + OFF_KS - 4096 + R0 * 128 + wn * 2048 + lane * 16;
      const int rr = R0 ? rt : rt - 1;
      hof = m0 + R0 == 0 ? 0x7fff0000u : WS_HL + (unsigned)(layer * 524288 + ((rr & 7) * 8 + (hp >> 1)) * 16384 + (hp & 1) * 4096 + wn * 2048 + lane * 16) + ((((rr + 1) >> 3 ^ 1) & 1) * 8192 ^ P);
      const unsigned out = ~((0x5555u << 2 << fr) >> (4 * g));
#pragma unroll
      for (int i = 0; i < 8; ++i) {
        unsigned t;
        asm("v_bfe_i32 %0, %1, %2, 1" : "=v"(t) : "v"(out), "n"((i >> 2) * 16 + (i & 3)));
        bias[i >> 2][i & 3] = __uint_as_float(t & 0xff61b1e6u);
      }
      bias[2] = m0 + R0 == 0 ? f32x4{-3.0e38f, -3.0e38f, -3.0e38f, -3.0e38f} : bias[0];
    } else {
      const int u = wm * 2 + kg;
      pwf = sb + OFF_WP + wn * 2048 + (4 * g + q4) * 64 + p4 * 8;
      pvf = sb + OFF_VF + w * 2048 + lane * 16;
      ppk = sb + OFF_PK + w * (32 * PSTR) + fr * PSTR + g * 8;
      ppa = sb + OFF_PK + wn * (32 * PSTR) + (4 * g + q4) * PSTR + u * 32 + p4 * 8;
      pvr = sb + OFF_VF + wn * 2048 + lane * 16;
      const unsigned doff = WS_KV + (layer >> 1) * KVLB + (unsigned)((rt * 16 + hp) * 16384) + (((wn ^ (rt >> 3) ^ 1) & 1) * 8192 ^ P);
      gkv = doff + u * 1024 + lane * 16;
      gkz = fr == 0 ? doff + 4096 + u * 32 + g * 8 : 0x7fff0000u;
    }
    if (part & 2) asm volatile("" : "+{v[48:63]}"(pa), "+{v[36:39]}"(bias[0]), "+{v[40:43]}"(bias[1]), "+{v[44:47]}"(bias[2]));
    else asm volatile("" : "+{v[48:63]}"(pa));
  };

  if (kg == 1) {
    const int lw = w & 3;
    const int kB = 16 * lw + (lane >> 3);
    const int fB = (kB >> 1) & 1;
    const int cB0 = ((((lane & 7) >> 1) ^ fB) << 1) | (lane & 1), cB1 = ((((lane & 7) >> 1) ^ (fB | 2)) << 1) | (lane & 1);

    unsigned vW0, vW1;
    u64 ko2;
    auto run = [&](auto fst) {
      constexpr bool FIRST = decltype(fst)::value;
      if constexpr (!HH) { if constexpr (FIRST) asm volatile(".Lmk_f1:"); else asm volatile(".Lmk_f0:"); }
      constexpr int N = 10;
      const unsigned vB0 = FIRST ? (unsigned)((kB * 3072 + hp * 64 + cB0 * 8) * 2) : nv[0];
      const unsigned vB1 = FIRST ? (unsigned)(((kB + 8) * 3072 + hp * 64 + cB1 * 8) * 2) - 1024u : nv[1];
      const unsigned vA = FIRST ? (unsigned)(((m0 + lw * 32 + (lane >> 3)) * DM + ((lane & 7) ^ (lane >> 3)) * 8) * 2)
                                : nv[2];
      const char* xa = FIRST ? (const char*)x0 : ws + WS_X + (layer - 1) * (SEQ * DM * 2);
      constexpr int KS = FIRST ? 128 : 32768;
      const u64 kord = FIRST ? korder0(rt) : uni2((unsigned)nk, (unsigned)(nk >> 32));
      auto ktile = [&](int ks) { return knib(kord, ks); };
      auto stageA = [&](int ks, int boff) {
        const int kt = ktile(ks);
        const unsigned sA = lds_addr(smem) + boff;
        if (FIRST) glds4v(xa + kt * KS, vA, vA + 15360u, vA + 2 * 15360u, vA + 3 * 15360u, sA + lw * 4096);
        else glds4c(xa + kt * KS, vA, sA + lw * 4096);
      };
      auto stageB = [&](const u16* Wp, int ks, int boff) {
        const int kt = ktile(ks);
        const unsigned sB = lds_addr(smem) + boff + ABYTES;
        const u16* b = Wp + (size_t)kt * 64 * 3072;
        glds4v(b, vB0, vB1, vB0, vB1, sB + lw * 6144);
        glds2v(b + 2048, vB0, vB1, sB + lw * 6144 + 4096);
      };
      bf16x8 sel[3];
      unsigned cso[3], cst;
      if (FIRST) {
        stageA(1, STAGE); stageB(W, 1, STAGE);
        __builtin_amdgcn_sched_barrier(0);
        WAITVN(N);
        BAR;
        asm volatile("" ::: "memory");
        const u32x2 ev = *(const u32x2*)(smem + OFF_E);
        stageA(2, 2 * STAGE); stageB(W, 2, 2 * STAGE);
        __builtin_amdgcn_sched_barrier(0);
        const int fl = (q4 >> 1) | ((g & 1) << 1), j = 3 * wm + (fr < 3 ? fr : 0);
#pragma unroll
        for (int i = 0; i < 3; ++i) {
          const unsigned o = fr == i ? 0x3F803F80u : 0u;
          sel[i] = __builtin_bit_cast(bf16x8, u32x4{o, o, o, o});
          cso[i] = (unsigned)(2 * STAGE + ABYTES + (g >> 1) * 6144 + (g & 1) * 1024 + q4 * 128 + p4 * 8 + (((wn * 2 + ((wm + i) & 1)) ^ fl) << 5) + ((3 * wm + i) >> 1) * 2048);
          asm volatile("" : "+v"(sel[i]), "+v"(cso[i]));
        }
        cst = fr < 3 ? WS_CPA + (unsigned)(((hp * 16 + rt) * 192 + (j >> 1) * 64 + wn * 32 + (j & 1) * 16 + 4 * g) * 4) : 0x7fff0000u;
        asm volatile("" : "+v"(cst));
        E = uni2(ev[0], ev[1]);
      }
      u64 fall, fal2;
      unsigned fao, fa2, vP, vS, vC, vX, dS, dC, dR;
      auto ld_pw = [&](int i0) {
#pragma unroll
        for (int i = i0; i < i0 + 4; ++i) gld16(pw[i], vP, wn1 + i * 8 * 3072);
      };
      auto setup = [&]() {
        fao = (unsigned)((256 + rt * 16 + (lane & 15)) * FLS) ^ P;
        fa2 = (unsigned)(((FIRST ? 512 : 256) + (lane & 15) * 16 + (FIRST ? hp : (hp - ((1 << (lane >> 4)) >> 1)) & 15)) * FLS) ^ P;
        vP = (unsigned)((rt * 64 + (lane >> 3)) * 6144 + lw * 2048 + ((hp + (1 << lw)) & 15) * 128 + (lane & 7) * 16);
        vS = FIRST ? (unsigned)(WS_G0 + (m0 + lw * 32 + (lane & 31)) * 16)
                   : WS_ST + (unsigned)((layer - 1) * (SEQ * 128) + (rt & 7) * 32768 + g * 1024 + lw * 256 + fr * 16) + ((rt >> 3) * 8192 ^ P);
        vC = (FIRST ? WS_CPA : WS_CSP) + (unsigned)(hp * 12288 + g * 768 + lw * 192 + (fr < 12 ? fr : 0) * 16);
        if (!FIRST) vX = HH ? (unsigned)(hp * 4096 + lw * 1024 + lane * 16) : (unsigned)((rt * 16 + hp) * 1024 + lane * 16);
        vW0 = (unsigned)((kB * DM + hp * 64 + ((lane & 7) ^ ((lane >> 4) << 1)) * 8) * 2); vW1 = vW0 + 8 * DM * 2 - 1024u;
        dS = OFF_RS + lw * 256 + (FIRST ? (lane & 31) * 8 : fr * 16 + (g & 1) * 8); dC = OFF_CS + lw * 192 + (fr < 12 ? fr : 0) * 16 + g * 4;
        ko2 = korder(bid & 3, hp);
        dR = OFF_R2 + lw * 128 + (FIRST ? (lane & 31) * 4 : fr * 8 + (g & 1) * 4);
        asm volatile("" : "+v"(dR));
        asm volatile("" : "+v"(vS), "+v"(vX), "+v"(vW0), "+v"(vW1), "+v"(dS));
        asm volatile("" : "+s"(ko2));
        if (FIRST || HH) asm volatile("" : "+v"(vC), "+v"(dC), "+v"(fa2));
        if (FIRST) asm volatile("" : "+v"(vP)); else asm volatile("" : "+v"(fao));
      };
      if (!FIRST) {
        const char* xa1 = xa + ktile(1) * KS;
        const char* xa2 = xa + ktile(2) * KS;
        const u16* b2 = W + (size_t)ktile(2) * 64 * 3072;
        unsigned sA = lds_addr(smem) + STAGE + lw * 4096, sB = lds_addr(smem) + 2 * STAGE + ABYTES + lw * 6144;
        const unsigned fo = nv[3];
        u64 fl1;
        asm volatile("global_load_dwordx2 %0, %1, %2 sc1" : "=v"(fl1) : "v"(fo), "s"(ws) : "memory");
        glds4v(b2, vB0, vB1, vB0, vB1, sB);
        glds2v(b2 + 2048, vB0, vB1, sB + 4096);
        asm volatile("" : "+s"(xa1), "+s"(xa2), "+s"(sA));
        setup();
        {
          WAITV(6);
          __builtin_amdgcn_sched_barrier(0);
          asm volatile("" : "+v"(fl1));
          const u64 m = __ballot(fo != 0u);
          if (__ballot(fl1 != E + (unsigned)layer) & m) poll_eq(ws, fo, E + (unsigned)layer, m, ~0ull);
        }
        glds4c(xa1, vA, sA); glds4c(xa2, vA, sA + STAGE);
        __builtin_amdgcn_sched_barrier(0);
        prep();
      }
      bf16x8 cf[6];
      f32x4 csum = {0, 0, 0, 0};
      if (FIRST) { fall = E + 1u; vX = (unsigned)(bid * 8192 + lw * 2048 + lane * 16); asm volatile("" : "+v"(fall), "+v"(vX)); }
#pragma unroll
      for (int kt = 0; kt < 13; ++kt) {
        const int cur = (kt % 3) * STAGE;
        if (!FIRST && kt == 0) { WAITV(4); }
        else if (FIRST && kt == 4 && lw == 1) { WAITVN(N + 2); }
        else if ((!FIRST && kt == 1 && lw == 3) || ((FIRST || HH) && kt == 10 && lw == 3) || (FIRST && (kt == 2 || kt == 4 || kt == 5 || (kt == 11 && lw == 0)))) { WAITVN(N + 1); }
        else { WAITVN(N); }
        if (!FIRST && kt == 2 && lw == 3) {
          asm volatile("" : "+v"(fall));
          if (__any(fall != E + (unsigned)layer)) poll_flags(ws, P, E, 1, rt, hp, lane, layer, 0);
        }
        if (HH && kt == 11 && lw == 3) {
          asm volatile("" : "+v"(fal2));
          if (__any(fal2 != E + (unsigned)layer)) {
            _Pragma("unroll 1") for (int j = 0; j < 4; ++j) poll_flags(ws, P, E, 1, rt, (hp - ((1 << j) >> 1)) & 15, lane, layer, 2);
          }
        }
        if (FIRST && kt == 11 && lw == 3) {
          asm volatile("" : "+v"(fal2));
          if (__any(fal2 != E + 1u)) poll_flags(ws, P, E, 2, rt, hp, lane, 1u, 2);
        }
        BAR;
        if (FIRST && kt == 1) {
          asm volatile("" ::: "memory");
#pragma unroll
          for (int i = 0; i < 6; ++i) { const char* t = smem + cso[i >> 1] + (i & 1) * 12288; cf[i] = cat8(tr_read(t), tr_read(t + 512)); }
        }
        if (FIRST && kt == 2 && lw == 2 && bid < 64 && (bid & 7) == ((bid >> 3) & 3)) {
          u64 t0, t1; unsigned d;
          asm volatile("s_memtime %0\n\ts_load_dword %2, %3, 0x0\n\ts_waitcnt lgkmcnt(0)\n\ts_memtime %1\n\ts_waitcnt lgkmcnt(0)"
                       : "=&s"(t0), "=&s"(t1), "=&s"(d) : "s"(ws + WS_PL + (bid >> 5) * 8192 + (bid & 31) * 256) : "memory");
          d = (unsigned)(t1 - t0);
          const u64 v = ((E + 1u) << 16) | (d < 65535u ? d : 65535u);
          __builtin_amdgcn_raw_buffer_store_b64(__builtin_bit_cast(u32x2, v), rsW, lane == 0 ? WS_PR + (unsigned)(bid & 56) : 0x7fff0000u, 0, 16);
        }
        if (FIRST && kt == 12 && lw == 2) asm volatile("global_load_dwordx2 %0, %1, %2 sc1" : "=v"(fal2) : "v"((lane & 7) * 8), "s"(ws + WS_PR) : "memory");
        if (FIRST && kt >= 2 && kt < 6 && lw == kt - 2) setup();
        if (FIRST && kt >= 6 && kt < 10 && (lw >> 1) == ((kt - 6) >> 1)) prep(1 + (kt & 1));
        stageA(kt + 3, cur);
        if (FIRST && kt == 1) {
          asm volatile("" : "+v"(sel[0]));
#pragma unroll
          for (int i = 0; i < 6; ++i) csum = MFMA(cf[i], sel[i >> 1], csum);
          asm volatile("" : "+v"(csum));
        }
        stageB(W, kt + 3, cur);
        if (FIRST) {
          if (kt == 1) __builtin_amdgcn_raw_buffer_store_b128(__builtin_bit_cast(u32x4, csum), rsW, cst, 0, 16);
          if (kt == 3 && tid == 320) __builtin_amdgcn_raw_buffer_store_b64(__builtin_bit_cast(u32x2, fall), rsW, (WS_FLAGS + (unsigned)(512 + rt * 16 + hp) * FLS) ^ P, 0, 16);
          if (kt == 3 || kt == 4) glds16s(wo + (kt - 3) * 512, vX, lds_addr(smem) + OFF_JK);
        }
        if constexpr (!HH) if (FIRST && kt == 10 && lw == 0) {
          CODE_TOUCH(1, ".Lmk_ep0", ".Lmk_end")
          asm volatile("s_mov_b32 m0, %2\n\ts_nop 0\n\tglobal_load_lds_dword %0, %1" :: "v"(v), "s"(pc), "s"(lds_addr(smem) + OFF_JK) : "memory", "m0");
        }
        if (!FIRST && kt == 0 && lw == 3) asm volatile("global_load_dwordx2 %0, %1, %2 sc1" : "=v"(fall) : "v"(fao), "s"(ws + WS_FLAGS) : "memory");
        if ((FIRST || HH) && kt == 9 && lw == 3) asm volatile("global_load_dwordx2 %0, %1, %2 sc1" : "=v"(fal2) : "v"(fa2), "s"(ws + WS_FLAGS) : "memory");
      }
      if (FIRST && lw < 3) { ld_pw(0); WAITVN(N + 4); } else { WAITVN(N); }
      BAR;
      u32x4 sv[4], cv[4];
      auto ld_c = [&](u32x4& d, unsigned v, int so) { asm volatile("buffer_load_dwordx4 %0, %1, %2, %3 offen sc1" : "=v"(d) : "v"(v), "s"(rsW), "s"(so) : "memory"); };
      auto wo_tile = [&](int t) { glds2v(wo + (size_t)knib(ko2, t) * 64 * DM, vW0, vW1, lds_addr(smem) + OFF_WO + t * 8192 + lw * 2048); };
#pragma unroll
      for (int i = 0; i < (FIRST ? 1 : 4); ++i) ld_c(sv[i], vS, (i & 1) * 4096 + (i >> 1) * 16384);
      if (FIRST || HH) {
#pragma unroll
        for (int i = 0; i < 4; ++i) ld_c(cv[i], vC, i * 3072);
      }
      if (HH) glds16s(wphi, vX, lds_addr(smem) + OFF_WP + lw * 1024);
      else if (!FIRST) glds16c(ws + WS_CSA, vX, lds_addr(smem) + OFF_CS);
      wo_tile(0); wo_tile(1);
      if (HH || (FIRST && lw < 3)) { WAITV(13); } else { WAITV(9); }
      BAR;
      wo_tile(2); wo_tile(3); wo_tile(4); wo_tile(5);
      if (FIRST) {
        glds16s(wn2, vX, lds_addr(smem) + OFF_JK); glds16s(wn2 + 512, vX, lds_addr(smem) + OFF_JK);
        WAITV(14);
        if (lw < 3) ld_pw(4);
      } else {
        WAITV(12);
      }
      auto fin = [&]() {
        if (FIRST) {
          const unsigned tag = (unsigned)E + 1u;
          asm volatile("" : "+v"(sv[0]));
          _Pragma("unroll 1") for (unsigned spins = 0; spins < (1u << 18); ++spins) {
            if (__all(sv[0][1] == tag && sv[0][3] == tag)) break;
            __builtin_amdgcn_s_sleep(1);
            asm volatile("buffer_load_dwordx4 %0, %1, %2, 0 offen sc1\n\ts_waitcnt vmcnt(0)" : "=v"(sv[0]) : "v"(vS), "s"(rsW) : "memory");
          }
          *(u32x2*)(smem + dS) = u32x2{sv[0][0], sv[0][2]};
          { unsigned* p = (unsigned*)(smem + dR); p[0] = sv[0][0]; p[128] = sv[0][2]; }
        } else {
          asm volatile("" : "+v"(sv[0]), "+v"(sv[1]), "+v"(sv[2]), "+v"(sv[3]));
          const f32x4 t = (__builtin_bit_cast(f32x4, sv[0]) + __builtin_bit_cast(f32x4, sv[1])) + (__builtin_bit_cast(f32x4, sv[2]) + __builtin_bit_cast(f32x4, sv[3]));
          const unsigned u = xs4(t[0], t[2], t[1], t[3]);
          auto h = __builtin_amdgcn_permlane32_swap(u, u, false, false);
          const float mean = __uint_as_float(h[0]) * (1.0f / 1024.0f);
          const float var = fmaxf(__uint_as_float(h[1]) * (1.0f / 1024.0f) - mean * mean, 0.f);
          const float rs = __builtin_amdgcn_rsqf(var + 1e-5f);
          *(float2*)(smem + dS) = make_float2(-mean, rs);
          { float* p = (float*)(smem + dR); p[0] = -mean; p[128] = rs; }
        }
      };
      auto cfin = [&]() {
        if (FIRST || HH) {
          asm volatile("" : "+v"(cv[0]), "+v"(cv[1]), "+v"(cv[2]), "+v"(cv[3]));
          const f32x4 t = (__builtin_bit_cast(f32x4, cv[0]) + __builtin_bit_cast(f32x4, cv[1])) + (__builtin_bit_cast(f32x4, cv[2]) + __builtin_bit_cast(f32x4, cv[3]));
          *(unsigned*)(smem + dC) = xs4(t[0], t[1], t[2], t[3]);
        }
      };
      cfin();
      if (FIRST && lw == 2) {
        asm volatile("" : "+v"(fal2));
        _Pragma("unroll 1") for (unsigned spins = 0; spins < (1u << 18); ++spins) {
          if (__all(((fal2 ^ ((E + 1u) << 16)) >> 16) == 0)) break;
          __builtin_amdgcn_s_sleep(1);
          asm volatile("global_load_dwordx2 %0, %1, %2 sc1\n\ts_waitcnt vmcnt(0)" : "=v"(fal2) : "v"((lane & 7) * 8), "s"(ws + WS_PR) : "memory");
        }
        unsigned d = (unsigned)fal2 & 0xffffu;
        d = min(d, (unsigned)__builtin_amdgcn_update_dpp(0u, d, 0xb1, 0xf, 0xf, false));
        d = min(d, (unsigned)__builtin_amdgcn_update_dpp(0u, d, 0x4e, 0xf, 0xf, false));
        *(unsigned*)(smem + OFF_E + 256) = __builtin_amdgcn_readlane(d, 4) < __builtin_amdgcn_readlane(d, 0) ? 8192u : 0u;
      }
      if (!HH) BAR;
      fin();
      WAITL0;
    };
    typedef std::integral_constant<bool, true> T_;
    typedef std::integral_constant<bool, false> F_;
    __builtin_amdgcn_s_setprio(3);
    if (first) run(T_{}); else run(F_{});
    __builtin_amdgcn_s_setprio(0);
    BAR;
    if constexpr (!HH) asm volatile(".Lmk_ep0:");
    {
      const char* xr = smem + XS + lane * 16;
#pragma unroll
      for (int i = 0; i < 2; ++i)
#pragma unroll
        for (int j = HH ? 2 : 0; j < (HH ? 4 : 6); ++j) acc[i][j] = *(const f32x4*)(xr + (i * (HH ? 8 : 6) + j) * 1024);
      if (HH) asm volatile("" : "=v"(acc[0][0]), "=v"(acc[0][1]), "=v"(acc[1][0]), "=v"(acc[1][1]));
    }
    asm volatile("" : "={v[64:79]}"(xq));
  } else {
    prep(1);
    if (!first) publish(rsW, tid, 256 + rt * 16 + hp, E + (unsigned)layer, P);
    prep(2);
    if (first) nv = g1_frag_addr(lds_addr(smem), w, lane);
    const unsigned aK0 = nv[0], aK1 = nv[1], bo0 = nv[2], bo1 = nv[3];
    if (first) {
      const int row = m0 + hp * 8 + w * 2 + (lane >> 5);
      static_assert(HH || (OFF_SP == 122880 && OFF_KS + 12288 <= OFF_JK), "addresses used at the top of the kernel");
      asm volatile("" :: "v"(aK0), "v"(aK1), "v"(bo0), "v"(bo1));
      unsigned go = (lane & 31) == 0 ? (unsigned)(WS_G0 + row * 16) : 0x7fff0000u;
      unsigned ho = WS_HL + (unsigned)(((rt & 7) * 8 + (hp >> 1)) * 16384 + (hp & 1) * 4096 + tid * 16 + (rt >> 3) * 8192);
      asm volatile("" : "+v"(go), "+v"(ho));
      WAITV(0);
      u32x4 xs[4];
      const u32x2 ev = *(const u32x2*)(smem + OFF_E);
#pragma unroll
      for (int i = 0; i < 4; ++i) xs[i] = *(const u32x4*)(smem + OFF_SP + w * 4096 + (lane >> 5) * 2048 + (lane & 31) * 16 + i * 512);
      typedef __attribute__((ext_vector_type(2))) __bf16 bf2;
      const bf2 one2 = __builtin_bit_cast(bf2, 0x3F803F80u);
      float sa[2] = {zero1(), zero1()}, sq[2] = {zero1(), zero1()};
#pragma unroll
      for (int i = 0; i < 16; ++i) {
        const unsigned u = xs[i >> 2][i & 3];
        const bf2 t = __builtin_bit_cast(bf2, u);
        sa[i & 1] = __builtin_amdgcn_fdot2_f32_bf16(t, one2, sa[i & 1], false);
        sq[i & 1] = __builtin_amdgcn_fdot2_f32_bf16(t, t, sq[i & 1], false);
      }
      float sm = r16_sum(sa[0] + sa[1]), m2 = r16_sum(sq[0] + sq[1]);
      {
        auto r = __builtin_amdgcn_permlane16_swap(__float_as_uint(sm), __float_as_uint(sm), false, false);
        auto q = __builtin_amdgcn_permlane16_swap(__float_as_uint(m2), __float_as_uint(m2), false, false);
        sm = __uint_as_float(r[0]) + __uint_as_float(r[1]); m2 = __uint_as_float(q[0]) + __uint_as_float(q[1]);
      }
      const float mean = sm * (1.0f / 1024.0f);
      m2 = fmaxf(m2 - sm * mean, 0.f);
      const unsigned tag = ev[0] + 1u;
      u32x4 gw = {__float_as_uint(-mean), tag, __float_as_uint(__builtin_amdgcn_rsqf(m2 * (1.0f / 1024.0f) + 1e-5f)), tag};
      __builtin_amdgcn_raw_buffer_store_b128(gw, rsW, go, 0, 16);
      __builtin_amdgcn_raw_buffer_store_b128(empty4(), rsW, ho, 0, 16);
      if (bid < 64 && tid == 0) __builtin_amdgcn_raw_buffer_store_b32(tag, rsW, WS_PL + (unsigned)((bid >> 5) * 8192 + (bid & 31) * 256), 0, 16);
      E = uni2(ev[0], ev[1]);
    }
    f32x32 ac0;
    f32x16 ac1;
    f32x8 av[2];
    asm volatile("" :: "v"(aK0), "v"(aK1), "v"(bo0), "v"(bo1));
    if (first) BAR;
#define G1_OUT
#define G1_T(a)
#define G1_TEND
#define G1_TCL
    asm volatile(G1_MACROS
        ".p2align 3\n"
        "G1RDB 0, %[b0], %[b1], 0\n G1RDA %[a0], %[a1], 0\n G1RDB 1, %[b0], %[b1], 0\n"
        "ds_read_b128 v[64:67], %[xa]\n ds_read_b128 v[68:71], %[xa] offset:2048\n"
        "ds_read_b128 v[72:75], %[xa] offset:4096\n ds_read_b128 v[76:79], %[xa] offset:6144\n"
        "s_cmp_eq_u32 %[lay], 0\n s_waitcnt lgkmcnt(12)\n"
        "G1SG 0, 1, 1, %[a0], %[a1], %[b0], %[b1], 0\n"
        "s_mov_b32 vcc_lo, 16\n"
        "G1SG 1, 1, 1, %[a0], %[a1], %[b0], %[b1], 40960\n"
        "G1SG 2, 1, 2, %[a0], %[a1], %[b0], %[b1], 40960\n"
        "s_cbranch_scc1 .Lg1a%=\n"
        "s_nop 0\n"
        G1_T(88)
        "s_barrier\n"
        G1_T(92)
        "G1RDA %[a0], %[a1], 40960\n"
        "s_waitcnt lgkmcnt(4)\n"
        "s_branch .Lg1b%=\n"
        ".Lg1a%=:\n"
        G1_T(88)
        "s_barrier\n"
        G1_T(92)
        "G1RDB 0, %[b0], %[b1], 40960\n G1RDA %[a0], %[a1], 40960\n G1RDB 1, %[b0], %[b1], 40960\n"
        ".p2align 3\n s_nop 0\n"
        ".Lg1b%=:\n"
        "s_waitcnt lgkmcnt(8)\n"
        "G1SG 0, 0, 1, %[a0], %[a1], %[b0], %[b1], 40960\n"
        "s_barrier\n"
        "G1SG 1, 0, 1, %[a2], %[a3], %[b2], %[b3], 0\n"
        "G1SG 2, 0, 1, %[a2], %[a3], %[b2], %[b3], 0\n"
        "s_lshr_b32 vcc_lo, vcc_lo, 1\n s_waitcnt vmcnt(0) lgkmcnt(8)\n"
        "G1SG 0, 0, 1, %[a2], %[a3], %[b2], %[b3], 0\n"
        "s_barrier\n"
        "G1SG 1, 0, 1, %[a0], %[a1], %[b0], %[b1], 0\n"
        "G1SG 2, 0, 1, %[a0], %[a1], %[b0], %[b1], 0\n"
        "s_cbranch_scc0 .Lg1c%=\n s_waitcnt lgkmcnt(8)\n"
        "G1SG 0, 0, 1, %[a0], %[a1], %[b0], %[b1], 0\n"
        "s_barrier\n"
        "G1SG 1, 0, 1, %[a0], %[a1], %[b0], %[b1], 40960\n"
        "G1SG 2, 0, 1, %[a0], %[a1], %[b0], %[b1], 40960\n"
        "s_branch .Lg1b%=\n"
        ".Lg1c%=:\n"
        "s_waitcnt lgkmcnt(8)\n"
        "G1END 1, 0, 8, 0, %[xs], %[b0], %[b1]\n"
        "s_waitcnt lgkmcnt(8)\n s_nop 0\n"
        "G1END 1, 8, 8, 0, %[xs], %[b0], %[b1]\n"
        "s_waitcnt lgkmcnt(0)\n s_nop 0\n"
        "G1END 1, 16, 8, 0, %[xs], %[b0], %[b1]\n"
        ".if %[hh] == 0\n s_nop 0\n s_barrier\n .endif\n"
        G1_T(96)
        ".if %[hh]\n G1END 0, 0, 20, 0xffff76103298, %[xs], %[b0], %[b1]\n .else\n G1END 0, 0, 18, 0xba5498327610, %[xs], %[b0], %[b1]\n .endif\n"
        "s_waitcnt lgkmcnt(0)\n s_barrier\n"
        ".if %[hh]\n G1END 0, 20, 24-20, 0xffffffffffff, %[xs], %[b0], %[b1]\n .else\n G1END 0, 18, 24-18, 0xffffffffffff, %[xs], %[b0], %[b1]\n .endif\n"
        "s_nop 7\n"
        G1_TEND G1_PURGE
        : "=&{v[160:191]}"(ac0), "=&{v[192:207]}"(ac1), "=&{v[64:79]}"(xq), "=&{v[224:231]}"(av[0]), "=&{v[248:255]}"(av[1]),
          "+{v[48:63]}"(pa), "+{v[36:39]}"(bias[0]), "+{v[40:43]}"(bias[1]), "+{v[44:47]}"(bias[2]) G1_OUT
        : [a0] "v"(aK0), [a1] "v"(aK1), [a2] "v"(aK0 + 2 * STAGE), [a3] "v"(aK1 + 2 * STAGE), [b0] "v"(bo0), [b1] "v"(bo1),
          [b2] "v"(bo0 + 2 * STAGE), [b3] "v"(bo1 + 2 * STAGE), [xs] "v"(lds_addr(smem) + XS + lane * 16), [xa] "v"(wn ? aK1 : aK0),
          [lay] "s"(layer), [hh] "n"(MODE)
        : "memory", "scc", "vcc", G1_TCL G1_CLOBBER);
    acc[0][0] = quad<0>(ac0); acc[0][1] = quad<1>(ac0); acc[0][2] = quad<2>(ac0); acc[0][3] = quad<3>(ac0); acc[0][4] = quad<4>(ac0); acc[0][5] = quad<5>(ac0);
    acc[1][0] = quad<6>(ac0); acc[1][1] = quad<7>(ac0); acc[1][2] = quad<0>(ac1); acc[1][3] = quad<1>(ac1); acc[1][4] = quad<2>(ac1); acc[1][5] = quad<3>(ac1);
    if constexpr (HH) {
      const float cs[2] = {lds<float>(pcv), lds<float>(pcv + 64)};
      f32x4 rm[4], rr[4];
#pragma unroll
      for (int rb = 0; rb < 4; ++rb) { rm[rb] = lds<f32x4>(prv + rb * 64); rr[rb] = lds<f32x4>(prv + 512 + rb * 64); }
#pragma unroll
      for (int h = 0; h < 2; ++h) {
        f32x2 f[8];
#pragma unroll
        for (int i = 0; i < 8; ++i) {
          const int c = i >> 2, rb = (i >> 1) & 1, e = (i & 1) * 2;
          const f32x4 a = h == 0 ? acc[rb][4 + c] : c ? quad<1>(av[rb]) : quad<0>(av[rb]);
          f[i] = __builtin_elementwise_fma(f32x2{rm[2 * h + rb][e], rm[2 * h + rb][e + 1]}, f32x2{cs[c], cs[c]}, f32x2{a[e], a[e + 1]});
        }
        asm volatile("" : "+v"(f[0]), "+v"(f[1]), "+v"(f[2]), "+v"(f[3]), "+v"(f[4]), "+v"(f[5]), "+v"(f[6]), "+v"(f[7]));
#pragma unroll
        for (int i = 0; i < 8; ++i) f[i] *= f32x2{rr[2 * h + ((i >> 1) & 1)][(i & 1) * 2], rr[2 * h + ((i >> 1) & 1)][(i & 1) * 2 + 1]};
        asm volatile("" : "+v"(f[0]), "+v"(f[1]), "+v"(f[2]), "+v"(f[3]), "+v"(f[4]), "+v"(f[5]), "+v"(f[6]), "+v"(f[7]));
#pragma unroll
        for (int c = 0; c < 2; ++c) lds<u32x4>(pvf + h * 8192 + c * 1024) = u32x4{pk2(f[4 * c][0], f[4 * c][1]), pk2(f[4 * c + 1][0], f[4 * c + 1][1]), pk2(f[4 * c + 2][0], f[4 * c + 2][1]), pk2(f[4 * c + 3][0], f[4 * c + 3][1])};
      }
    }
  }
  ST(layer * 24 + 4)
  {
  const int lane = lane_id(), tid = w * 64 + lane, fr = lane & 15, g = lane >> 4;
  (void)fr; (void)g;
  auto o_put = [&](int rb, const f32x4& a, const f32x4& b) {
    const u32x4 v = {pk2(a[0], a[1]), pk2(a[2], a[3]), pk2(b[0], b[1]), pk2(b[2], b[3])};
    __builtin_amdgcn_raw_buffer_store_b128(v, rsW, ogo + rb * 1024, 0, 16);
    return v;
  };
  auto marks = [&]() {
    const u32x4 pz = empty4();
    __builtin_amdgcn_raw_buffer_store_b128(pz, rsW, dm, 0, 16);
    if (w < 2 || (!HH && w >= 4)) __builtin_amdgcn_raw_buffer_store_b128(pz, rsW, dm2, 0, 16);
  };
  if (first) { P = __builtin_amdgcn_readfirstlane(*(const unsigned*)(smem + OFF_E + 256)); ogo ^= P; hof ^= P; }
  f32x4 csq[2], csk[2], rm[2], rr[2];
  float csv[2];
  f32x2 st2[2] = {lds<f32x2>(prs), lds<f32x2>(prs + 128)};
#pragma unroll
  for (int c = 0; c < 2; ++c) {
    csk[c] = lds<f32x4>(pcs + 256 + c * 64);
    if (!HH) { csv[c] = lds<float>(pcv + c * 64); rm[1 - c] = lds<f32x4>(prv + (1 - c) * 64); rr[1 - c] = lds<f32x4>(prv + 512 + (1 - c) * 64); }
  }
#pragma unroll
  for (int c = 0; c < 2; ++c) csq[c] = lds<f32x4>(pcs + c * 64);
  if constexpr (!HH) {
    auto fold = [&](const f32x4& a, const f32x4& cs, const f32x2& st, bool scale) {
      f32x2 lo = __builtin_elementwise_fma(f32x2{cs[0], cs[1]}, f32x2{st[0], st[0]}, f32x2{a[0], a[1]});
      f32x2 hi = __builtin_elementwise_fma(f32x2{cs[2], cs[3]}, f32x2{st[0], st[0]}, f32x2{a[2], a[3]});
      if (scale) { lo *= f32x2{st[1], st[1]}; hi *= f32x2{st[1], st[1]}; }
      return u32x2{pk2(lo[0], lo[1]), pk2(hi[0], hi[1])};
    };
    auto cat2 = [](u32x2 a, u32x2 b) { return __builtin_bit_cast(bf16x8, u32x4{a[0], a[1], b[0], b[1]}); };
    auto foldv = [&](int rb, int c) {
      const f32x4& a = acc[rb][4 + c];
      const f32x2 lo = __builtin_elementwise_fma(f32x2{rm[rb][0], rm[rb][1]}, f32x2{csv[c], csv[c]}, f32x2{a[0], a[1]}) * f32x2{rr[rb][0], rr[rb][1]};
      const f32x2 hi = __builtin_elementwise_fma(f32x2{rm[rb][2], rm[rb][3]}, f32x2{csv[c], csv[c]}, f32x2{a[2], a[3]}) * f32x2{rr[rb][2], rr[rb][3]};
      return u32x2{pk2(lo[0], lo[1]), pk2(hi[0], hi[1])};
    };
    bf16x8 qf[2], kf[2];
    u32x2 vt[3][2];
#pragma unroll
    for (int rb = 1; rb >= 0; --rb) {
      kf[rb] = cat2(fold(acc[rb][2], csk[0], st2[rb], true), fold(acc[rb][3], csk[1], st2[rb], true));
      const u32x2 va0 = vt[rb + 1][0] = foldv(rb, 0), va1 = vt[rb + 1][1] = foldv(rb, 1);
      if (rb == 1) {
        if (R0 == 96) {
          u32x4 hk = __builtin_bit_cast(u32x4, kf[1]), hv = {va0[0], va0[1], va1[0], va1[1]};
#pragma unroll
          for (int e = 0; e < 4; ++e) { hk[e] = hk[e] < EMPTY - 1u ? hk[e] : EMPTY - 1u; hv[e] = hv[e] < EMPTY - 1u ? hv[e] : EMPTY - 1u; }
          __builtin_amdgcn_raw_buffer_store_b128(hk, rsW, hof, 0, 16);
          __builtin_amdgcn_raw_buffer_store_b128(hv, rsW, hof + 1024, 0, 16);
        } else {
          lds<bf16x8>(pkw + 4096) = kf[1];
          lds<u32x2>(pkw + 5120) = va0; lds<u32x2>(pkw + 5128) = va1;
        }
        asm volatile("" : "+v"(st2[0]), "+v"(st2[1]), "+v"(csv[0]), "+v"(csv[1]));
      }
    }
#pragma unroll
    for (int rb = 0; rb < 2; ++rb) qf[rb] = cat2(fold(acc[rb][0], csq[0], st2[rb], false), fold(acc[rb][1], csq[1], st2[rb], false));
    f32x4 sc[2][2];
    sc[1][0] = MFMA(kf[0], qf[1], bias[0]);
    sc[1][1] = MFMA(kf[1], qf[1], bias[1]);
    sc[0][1] = MFMA(kf[0], qf[0], bias[1]);
    constexpr float SCL = 0.17677669529663687f * 1.4426950408889634f;
    const bf16x8 ones = ones8();
    auto attn = [&](int rb) {
      const f32x4 z4 = {0, 0, 0, 0};
      const float scl = st2[rb][1] * SCL;
      asm volatile("s_nop 7" : "+v"(sc[rb][0]), "+v"(sc[rb][1]));
      unsigned m = __float_as_uint(vmax3f_n(vmax3f(sc[rb][0][0], sc[rb][0][1], sc[rb][0][2]), vmax3f(sc[rb][0][3], sc[rb][1][0], sc[rb][1][1]), vmaxf(sc[rb][1][2], sc[rb][1][3])));
      auto r = __builtin_amdgcn_permlane16_swap(m, m, false, false);
      m = __float_as_uint(vmaxf_n(__uint_as_float(r[0]), __uint_as_float(r[1])));
      auto r2 = __builtin_amdgcn_permlane32_swap(m, m, false, false);
      const float mxs = -vmaxf(__uint_as_float(r2[0]), __uint_as_float(r2[1])) * scl;
      u32x4 pv;
#pragma unroll
      for (int i = 0; i < 4; ++i) {
        const f32x2 t = __builtin_elementwise_fma(f32x2{sc[rb][i >> 1][(i & 1) * 2], sc[rb][i >> 1][(i & 1) * 2 + 1]}, f32x2{scl, scl}, f32x2{mxs, mxs});
        pv[i] = pk2(__builtin_amdgcn_exp2f(t[0]), __builtin_amdgcn_exp2f(t[1]));
      }
      const bf16x8 pf = __builtin_bit_cast(bf16x8, pv);
      const f32x4 den = MFMA(ones, pf, z4);
      const f32x4 o0 = MFMA(cat2(vt[rb][0], vt[rb + 1][0]), pf, z4), o1 = MFMA(cat2(vt[rb][1], vt[rb + 1][1]), pf, z4);
      const float inv = __builtin_amdgcn_rcpf(den[0]);
      return o_put(rb, o0 * inv, o1 * inv);
    };
    u32x4 hk, hv;
    auto ld_h = [&]() {
      asm volatile("s_nop 7\n\tbuffer_load_dwordx4 %0, %2, %3, 0 offen sc1\n\tbuffer_load_dwordx4 %1, %2, %3, 0 offen offset:1024 sc1" : "=&v"(hk), "=&v"(hv) : "v"(hof), "s"(rsW) : "memory");
    };
    asm volatile("" : "+v"(sc[0][1]));
    if (R0 == 0) ld_h();
    const u32x4 ov = attn(1);
    if (R0 == 0) {
      WAITL0; BAR;
      WAITV(1);
      _Pragma("unroll 1") for (unsigned spins = 0; spins < (1u << 18); ++spins) {
        __builtin_amdgcn_sched_barrier(0); asm volatile("" : "+v"(hk), "+v"(hv));
        unsigned mxw = 0;
#pragma unroll
        for (int e = 0; e < 4; ++e) { mxw = hk[e] > mxw ? hk[e] : mxw; mxw = hv[e] > mxw ? hv[e] : mxw; }
        if (__all(mxw != EMPTY)) break;
        ld_h();
        WAITV(0);
      }
      asm volatile("" : "+v"(hk), "+v"(hv));
      if (first) marks();
    } else {
      if (first) marks();
      if (first && tid >= 128 && tid < 176) __builtin_amdgcn_raw_buffer_store_b128(*(const u32x4*)(smem + OFF_CS + (tid - 128) * 16), rsW, WS_CSA + (unsigned)((rt * 16 + hp) * 1024 + (tid - 128) * 16), 0, 16);
      WAITL0; BAR;
      asm volatile("" ::: "memory");
      hk = lds<u32x4>(pkw); hv = lds<u32x4>(pkw + 1024);
    }
    ST(layer * 24 + 5)
    asm volatile("" ::: "memory");
    vt[0][0] = u32x2{hv[0], hv[1]}; vt[0][1] = u32x2{hv[2], hv[3]};
    sc[0][0] = MFMA(__builtin_bit_cast(bf16x8, hk), qf[0], bias[2]);
    lds<u32x4>(opo + 1024) = ov;
    lds<u32x4>(opo) = attn(0);
  } else {
    bf16x8 wf[2];
#pragma unroll
    for (int fb = 0; fb < 2; ++fb) wf[fb] = cat8(tr_read(pwf + fb * 32), tr_read(pwf + fb * 32 + 16 * 64));
    __builtin_amdgcn_sched_barrier(0);
    if (layer == 1) marks();
    const f32x4 z4 = {0, 0, 0, 0};
    constexpr float L2E = 1.4426950408889634f;
    const unsigned kvo = WS_KV + (layer >> 1) * KVLB, kfo = WS_KF + (layer >> 1) * KFLB + hp * 16384, kzo = WS_KF + (layer >> 1) * KFLB + 16 * 16384 + hp * 256;
    bf16x8 qp[2][2];
    auto phi2 = [&](auto qktag) {
      constexpr int QK = decltype(qktag)::value & 1;
      constexpr int RB0 = (decltype(qktag)::value >> 1) & 1, RB1 = (decltype(qktag)::value & 4) ? RB0 + 1 : 2;
      float pv[2][8];
      f32x4 pm[2][2];
      f32x2 f[2][4];
#pragma unroll
      for (int rb = RB0; rb < RB1; ++rb) {
        asm volatile("" : "+v"(st2[rb]));
        const f32x2 nm = {st2[rb][0], st2[rb][0]};
#pragma unroll
        for (int i = 0; i < 4; ++i) {
          const f32x4& a = acc[rb][2 * QK + (i >> 1)];
          const f32x4& cs = QK ? csk[i >> 1] : csq[i >> 1];
          f[rb][i] = __builtin_elementwise_fma(f32x2{cs[(i & 1) * 2], cs[(i & 1) * 2 + 1]}, nm, f32x2{a[(i & 1) * 2], a[(i & 1) * 2 + 1]});
        }
      }
#pragma unroll
      for (int rb = RB0; rb < RB1; ++rb) asm volatile("" : "+v"(f[rb][0]), "+v"(f[rb][1]), "+v"(f[rb][2]), "+v"(f[rb][3]));
#pragma unroll
      for (int rb = RB0; rb < RB1; ++rb) {
        const bf16x8 xf = __builtin_bit_cast(bf16x8, u32x4{pk2(f[rb][0][0], f[rb][0][1]), pk2(f[rb][1][0], f[rb][1][1]), pk2(f[rb][2][0], f[rb][2][1]), pk2(f[rb][3][0], f[rb][3][1])});
        pm[rb][0] = MFMA(wf[0], xf, z4); pm[rb][1] = MFMA(wf[1], xf, z4);
      }
      if (QK) asm volatile("s_nop 7" : "+v"(pm[0][0]), "+v"(pm[0][1]), "+v"(pm[1][0]), "+v"(pm[1][1]));
      else asm volatile("s_nop 7" : "+v"(pm[RB0][0]), "+v"(pm[RB0][1]));
#pragma unroll
      for (int rb = RB0; rb < RB1; ++rb)
#pragma unroll
        for (int i = 0; i < 4; ++i) { pv[rb][i] = pm[rb][0][i]; pv[rb][4 + i] = pm[rb][1][i]; }
      float mx[2], mn[2];
#pragma unroll
      for (int u = RB0; u < RB1; ++u) max8pn(pm[u][0], pm[u][1], mx[u], mn[u]);
      if (RB1 - RB0 == 2) xr4<true>(mx[0], mn[0], mx[1], mn[1]); else xr2<true>(mx[RB0], mn[RB0]);
      float ep[2][8], en[2][8], sp[2], sn[2];
#pragma unroll
      for (int u = RB0; u < RB1; ++u) {
        const float c = st2[u][1] * L2E, mxs = -mx[u] * c, mns = -mn[u] * c;
#pragma unroll
        for (int i = 0; i < 4; ++i) {
          const f32x2 p2 = {pv[u][2 * i], pv[u][2 * i + 1]};
          const f32x2 tp = __builtin_elementwise_fma(p2, f32x2{c, c}, f32x2{mxs, mxs});
          const f32x2 tn = __builtin_elementwise_fma(-p2, f32x2{c, c}, f32x2{mns, mns});
          ep[u][2 * i] = __builtin_amdgcn_exp2f(tp[0]); ep[u][2 * i + 1] = __builtin_amdgcn_exp2f(tp[1]);
          en[u][2 * i] = __builtin_amdgcn_exp2f(tn[0]); en[u][2 * i + 1] = __builtin_amdgcn_exp2f(tn[1]);
        }
        const f32x2 a2 = (f32x2{ep[u][0], ep[u][1]} + f32x2{ep[u][2], ep[u][3]}) + (f32x2{ep[u][4], ep[u][5]} + f32x2{ep[u][6], ep[u][7]});
        const f32x2 b2 = (f32x2{en[u][0], en[u][1]} + f32x2{en[u][2], en[u][3]}) + (f32x2{en[u][4], en[u][5]} + f32x2{en[u][6], en[u][7]});
        sp[u] = a2[0] + a2[1]; sn[u] = b2[0] + b2[1];
      }
      if (RB1 - RB0 == 2) xr4<false>(sp[0], sn[0], sp[1], sn[1]); else xr2<false>(sp[RB0], sn[RB0]);
#pragma unroll
      for (int rb = RB0; rb < RB1; ++rb) {
        float ip = __builtin_amdgcn_rcpf(sp[rb]), in = __builtin_amdgcn_rcpf(sn[rb]);
        u32x2 o0, o1, o2, o3;
        o0[0] = pk2(ep[rb][0] * ip, ep[rb][1] * ip); o0[1] = pk2(ep[rb][2] * ip, ep[rb][3] * ip);
        o1[0] = pk2(ep[rb][4] * ip, ep[rb][5] * ip); o1[1] = pk2(ep[rb][6] * ip, ep[rb][7] * ip);
        o2[0] = pk2(en[rb][0] * in, en[rb][1] * in); o2[1] = pk2(en[rb][2] * in, en[rb][3] * in);
        o3[0] = pk2(en[rb][4] * in, en[rb][5] * in); o3[1] = pk2(en[rb][6] * in, en[rb][7] * in);
        if (QK == 0) {
          qp[rb][0] = __builtin_bit_cast(bf16x8, (u32x4){o0[0], o0[1], o1[0], o1[1]});
          qp[rb][1] = __builtin_bit_cast(bf16x8, (u32x4){o2[0], o2[1], o3[0], o3[1]});
          asm volatile("" : "+v"(qp[rb][0]), "+v"(qp[rb][1]));
        } else {
          const unsigned dst = ppk + rb * 16 * PSTR;
          lds<u32x2>(dst) = o0; lds<u32x2>(dst + 32) = o1; lds<u32x2>(dst + 64) = o2; lds<u32x2>(dst + 96) = o3;
        }
      }
    };
    phi2(std::integral_constant<int, 1>{});
    bf16x8 one8 = ones8();
    asm volatile("" : "+v"(one8));
    WAITL0; BAR;
    ST(layer * 24 + 5)
    {
      bf16x8 af[4], v0[4], v1[4];
      asm volatile("" ::: "memory");
#pragma unroll
      for (int sw = 0; sw < 4; ++sw) {
        af[sw] = cat8(tr_read(ppa + sw * (64 * PSTR)), tr_read(ppa + sw * (64 * PSTR) + 16 * PSTR));
        v0[sw] = lds<bf16x8>(pvr + sw * 4096);
        v1[sw] = lds<bf16x8>(pvr + sw * 4096 + 1024);
      }
      __builtin_amdgcn_sched_barrier(0);
      f32x4 kv[3] = {z4, z4, z4};
#pragma unroll
      for (int sw = 0; sw < 4; ++sw) {
        kv[0] = MFMA(af[sw], v0[sw], kv[0]);
        kv[1] = MFMA(af[sw], v1[sw], kv[1]);
        kv[2] = MFMA(af[sw], one8, kv[2]);
      }
      auto pc = [](float x, float y) { const unsigned t = pk2(x, y); return t < EMPTY - 1u ? t : EMPTY - 1u; };
      __builtin_amdgcn_raw_buffer_store_b128(u32x4{pc(kv[0][0], kv[0][1]), pc(kv[0][2], kv[0][3]), pc(kv[1][0], kv[1][1]), pc(kv[1][2], kv[1][3])}, rsW, gkv, 0, 16);
      __builtin_amdgcn_raw_buffer_store_b64(u32x2{pc(kv[2][0], kv[2][1]), pc(kv[2][2], kv[2][3])}, rsW, gkz, 0, 16);
      __builtin_amdgcn_sched_barrier(0);
      if (kg) {
#pragma unroll
        for (int i = 0; i < 4; ++i) acc[i >> 1][i & 1] = lds<f32x4>(sb + XS + lane * 16 + ((i >> 1) * 8 + (i & 1)) * 1024);
      }
    }
    ST(layer * 24 + 6)
    u32x4 l1[8];
    const bool red = w == 7 || (w == 6 && (rt & 7) == 0);
    unsigned lo = kvo + (unsigned)((hp + (lane >> 5) * 128) * 16384) + ((((rt >> 3) ^ (lane >> 5) ^ 1) & 1) * 8192 ^ P) +
                  (w == 7 ? (unsigned)((rt & 4) * 512 + (g & 1) * 1024 + (rt & 3) * 256 + fr * 16) : (unsigned)(4096 + (lane & 7) * 16));
    unsigned fs = w == 7 ? kfo + (unsigned)((rt >> 2) * 2048 + (lane >> 5) * 1024 + (rt & 3) * 256 + fr * 16 + (g & 1) * 8)
                : (lane & 24) ? 0x7fff0000u : kzo + (unsigned)((rt >> 3) * 128 + (lane & 4) * 16 + (lane & 1) * 32 + (lane >> 5) * 16 + (lane & 2) * 4);
    auto l1_load = [&]() {
      asm volatile("" ::: "memory");
#pragma unroll
      for (int p = 0; p < 8; ++p) l1[p] = __builtin_amdgcn_raw_buffer_load_b128(rsW, lo, p * (16 * 16384), 16);
      __builtin_amdgcn_sched_barrier(0);
    };
    if (red) asm volatile("" : "+v"(lo), "+v"(fs));
    BAR;
    if (red) {
      typedef __attribute__((ext_vector_type(2))) __bf16 bf2;
      unsigned c0 = 0x00003F80u, c1 = 0x3F800000u;
      asm volatile("" : "+v"(c0), "+v"(c1));
      __builtin_amdgcn_s_setprio(3);
      __builtin_amdgcn_s_sleep(5);
      _Pragma("unroll 1") for (unsigned spins = 0; spins < (1u << 18); ++spins) {
        l1_load();
        unsigned mxw = 0;
#pragma unroll
        for (int p = 0; p < 8; ++p)
#pragma unroll
          for (int e = 0; e < 4; ++e) mxw = l1[p][e] > mxw ? l1[p][e] : mxw;
        if (__all(mxw != EMPTY)) break;
      }
      float s4[4];
      float a[8] = {zero1(), zero1(), zero1(), zero1(), zero1(), zero1(), zero1(), zero1()};
#pragma unroll
      for (int p = 0; p < 8; ++p)
#pragma unroll
        for (int e = 0; e < 4; ++e) {
          const unsigned u = l1[p][e];
          a[2 * e] = __builtin_amdgcn_fdot2_f32_bf16(__builtin_bit_cast(bf2, u), __builtin_bit_cast(bf2, c0), a[2 * e], false);
          a[2 * e + 1] = __builtin_amdgcn_fdot2_f32_bf16(__builtin_bit_cast(bf2, u), __builtin_bit_cast(bf2, c1), a[2 * e + 1], false);
        }
#pragma unroll
      for (int i = 0; i < 4; ++i) {
        auto r = __builtin_amdgcn_permlane32_swap(__float_as_uint(a[i]), __float_as_uint(a[i + 4]), false, false);
        s4[i] = __uint_as_float(r[0]) + __uint_as_float(r[1]);
      }
      unsigned o0 = pk2(s4[0], s4[1]), o1 = pk2(s4[2], s4[3]);
      o0 = o0 < EMPTY - 1u ? o0 : EMPTY - 1u; o1 = o1 < EMPTY - 1u ? o1 : EMPTY - 1u;
      __builtin_amdgcn_raw_buffer_store_b64(u32x2{o0, o1}, rsW, fs, 0, 16);
      __builtin_amdgcn_raw_buffer_store_b64(u32x2{o0, o1}, rsW, fs, 8192, 16);
    }
    phi2(std::integral_constant<int, 4>{});
    ST(layer * 24 + 7)
    phi2(std::integral_constant<int, 6>{});
    __builtin_amdgcn_s_setprio(0);
    ST(layer * 24 + 8)
    ST(layer * 24 + 9)
    {
      unsigned pfo = OFF_KV + wn * 4096 + lane * 16, pzo = OFF_KV + 8192 + wn * 128 + g * 16;
      asm volatile("" : "+v"(pfo), "+v"(pzo));
      if (w < 3) {
        u32x4 fa[3];
        unsigned fo = kfo + (unsigned)(w * 3072 + lane * 16) + ((rt >> 3) * 8192 ^ P);
        unsigned fo2 = w < 2 ? fo + 2048 : kzo + (unsigned)((lane & 15) * 16) + ((rt >> 3) * 8192 ^ P);
        unsigned lo1 = sb + OFF_KV + w * 3072 + lane * 16, lo2 = w < 2 ? lo1 + 2048 : sb + OFF_KV + 8192 + (lane & 15) * 16;
        __builtin_amdgcn_s_sleep(8);
        _Pragma("unroll 1") for (unsigned spins = 0; spins < (1u << 18); ++spins) {
          asm volatile("" ::: "memory");
          fa[0] = __builtin_amdgcn_raw_buffer_load_b128(rsW, fo, 0, 16);
          fa[1] = __builtin_amdgcn_raw_buffer_load_b128(rsW, fo + 1024, 0, 16);
          fa[2] = __builtin_amdgcn_raw_buffer_load_b128(rsW, fo2, 0, 16);
          unsigned mxw = 0;
#pragma unroll
          for (int e = 0; e < 12; ++e) mxw = fa[e >> 2][e & 3] > mxw ? fa[e >> 2][e & 3] : mxw;
          if (__all(mxw != EMPTY)) break;
          __builtin_amdgcn_s_sleep(1);
        }
        lds<u32x4>(lo1) = fa[0]; lds<u32x4>(lo1 + 1024) = fa[1]; lds<u32x4>(lo2) = fa[2];
      }
      ST(layer * 24 + 10)
      WAITL0; BAR;
      ST(layer * 24 + 11)
      bf16x8 kvf[3][2];
#pragma unroll
      for (int ks = 0; ks < 2; ++ks) {
        kvf[0][ks] = *(const bf16x8*)(smem + pfo + ks * 2048);
        kvf[1][ks] = *(const bf16x8*)(smem + pfo + ks * 2048 + 1024);
        kvf[2][ks] = *(const bf16x8*)(smem + pzo + ks * 64);
      }
#pragma unroll
      for (int rb = 0; rb < 2; ++rb) {
        f32x4 oa[3];
#pragma unroll
        for (int nb = 2; nb >= 0; --nb) {
          oa[nb] = MFMA(kvf[nb][0], qp[rb][0], z4);
          oa[nb] = MFMA(kvf[nb][1], qp[rb][1], oa[nb]);
        }
        float den = oa[2][0];
        den += 1e-6f;
        float inv = __builtin_amdgcn_rcpf(den);
        lds<u32x4>(opo + rb * 1024) = o_put(rb, oa[0] * inv, oa[1] * inv);
      }
    }
  }
  ST(layer * 24 + 12)
  if (w >= 6) {
    const unsigned v0 = (unsigned)((((w & 1) * 32 + (lane >> 3)) * DM + hp * 64 + ((lane & 7) ^ ((lane >> 4) << 1)) * 8) * 2);
    const u64 ko = korder(bid & 3, hp);
#pragma unroll
    for (int t = 6; t < 10; ++t) glds4v(wo + (size_t)knib(ko, t) * 64 * DM, v0, v0 + 15360u, v0 + 2 * 15360u, v0 + 3 * 15360u, sb + OFF_WO + t * 8192 + (w & 1) * 4096);
    WAITV(16); WAITL0; BAR;
    __builtin_amdgcn_s_sleep(2);
#pragma unroll
    for (int t = 10; t < 12; ++t) glds4v(wo + (size_t)knib(ko, t) * 64 * DM, v0, v0 + 15360u, v0 + 2 * 15360u, v0 + 3 * 15360u, sb + OFF_WO + t * 8192 + (w & 1) * 4096);
  } else if (w >= 4) { WAITV(0); WAITL0; BAR; }
  if (first && w >= 4 && w < 7) {
    typedef __attribute__((ext_vector_type(2))) __bf16 bf2;
    unsigned c0 = 0x00003F80u, c1 = 0x3F800000u;
    asm volatile("" : "+v"(c0), "+v"(c1));
    const bf2 lo1 = __builtin_bit_cast(bf2, c0), hi1 = __builtin_bit_cast(bf2, c1);
    float a[8] = {zero1(), zero1(), zero1(), zero1(), zero1(), zero1(), zero1(), zero1()};
#pragma unroll
    for (int i = 0; i < 8; ++i) {
      asm volatile("" : "+v"(pw[i]));
#pragma unroll
      for (int e = 0; e < 4; ++e) {
        const unsigned u = pw[i][e];
        a[2 * e] = __builtin_amdgcn_fdot2_f32_bf16(__builtin_bit_cast(bf2, u), lo1, a[2 * e], false);
        a[2 * e + 1] = __builtin_amdgcn_fdot2_f32_bf16(__builtin_bit_cast(bf2, u), hi1, a[2 * e + 1], false);
      }
    }
#pragma unroll
    for (int i = 0; i < 4; ++i) {
      auto r = __builtin_amdgcn_permlane32_swap(__float_as_uint(a[i]), __float_as_uint(a[i + 4]), false, false);
      a[i] = __uint_as_float(r[0]) + __uint_as_float(r[1]);
    }
#pragma unroll
    for (int i = 0; i < 2; ++i) {
      auto r = __builtin_amdgcn_permlane16_swap(__float_as_uint(a[i]), __float_as_uint(a[i + 2]), false, false);
      a[i] = __uint_as_float(r[0]) + __uint_as_float(r[1]);
      a[i] += dpp_ror<8>(a[i]);
    }
    if (!(lane & 8))
      __builtin_amdgcn_raw_buffer_store_b64(u32x2{__float_as_uint(a[0]), __float_as_uint(a[1])}, rsW,
          (unsigned)(WS_CSP + ((((hp + (1 << (w - 4))) & 15) * 16 + rt) * 192 + (w - 4) * 64 + (lane & 7) * 8 + (lane >> 5) * 4 + ((lane >> 4) & 1) * 2) * 4), 0, 16);
  }
  }
}

#undef pcs
#undef prs
#undef opo
#undef ogo
#undef pkw
#undef hof
#undef dm
#undef dm2
#undef pcv
#undef pwf
#undef prv
#undef pvf
#undef ppk
#undef ppa
#undef pvr
#undef gkv
#undef gkz

__device__ __forceinline__ void g2_phase(char* smem, char* ws, unsigned P, u32x4& nv, u64& nk, u64 E, int w, f32x16& xq, int layer, const u16* __restrict__ x0,
                                         const u16* __restrict__ wo, const u16* __restrict__ wnx, u16* __restrict__ out) {
  IDS
  wnx = uni(wnx);
  const unsigned sb = lds_addr(smem);
  const int lw = w & 3, k2 = 16 * lw + (lane >> 3);
  const unsigned vW0 = (unsigned)((k2 * DM + hp * 64 + ((lane & 7) ^ ((lane >> 4) << 1)) * 8) * 2), vW1 = vW0 + 8 * DM * 2 - 1024u;
  const int r0 = w * 16 + (lane >> 3);
  unsigned pxr = sb + r0 * 128 + (lane & 7) * 16;
  const unsigned rmo = (unsigned)(((m0 + r0) * DM + hp * 64 + ((lane & 7) ^ (lane >> 3)) * 8) * 2);
  const unsigned imo = SX + (unsigned)(hp * 32768 + (w >> 2) * 8192 + r0 * 128 + (lane & 7) * 16);
  constexpr int S0 = 0, S1 = 16384, S2 = 32768, S3 = 147456, S4 = OFF_WO, S5 = 98304, S6 = 114688;
  constexpr int AS[16] = {S0, S1, S2, S3, S0, S1, S4, S2, S3, S0, S1, S4, S5, S3, S0, S6};
  constexpr int OFF_C = 0;
  auto bs = [](int t) { return OFF_WO + (t < 12 ? t : t - 10) * 8192; };
  if (w >= 4) {
    constexpr int WT[15] = {0, 8, 12, 14, 16, 18, 20, 18, 16, 14, 12, 12, 10, 10, 8};
    const char* o = ws + WS_O + layer * (SEQ * DM * 2) + SX;
    const unsigned vA = (unsigned)((lw >> 1) * 16384 + (lw & 1) * 4096 + lane * 16);
    const u64 kord = korder(bid & 3, hp);
    auto ldA = [&](int s) { glds4c(o + knib(kord, s) * 32768, vA, sb + AS[s] + lw * 4096); };
    auto ldW = [&](int t) { glds2v(wo + (size_t)knib(kord, t) * 64 * DM, vW0, vW1, sb + bs(t) + lw * 2048); };
    const int fB = (k2 >> 1) & 1;
    unsigned vB0 = (unsigned)((k2 * 3072 + hp * 64 + (((((lane & 7) >> 1) ^ fB) << 1) | (lane & 1)) * 8) * 2);
    unsigned vB1 = (unsigned)(((k2 + 8) * 3072 + hp * 64 + (((((lane & 7) >> 1) ^ (fB | 2)) << 1) | (lane & 1)) * 8) * 2) - 1024u;
    nk = korder(kv1(bid), hp);
    const int kn1 = knib(nk, 1);
    nv = u32x4{vB0, vB1, SX + vA, lane < 16 && ((kfirst(nk) >> lane) & 1) ? (WS_FLAGS + (unsigned)(256 + rt * 16 + lane) * FLS) ^ P : 0u};
    asm volatile("" : "+v"(nv));
    asm volatile("" : "+s"(nk));
    vB0 = nv[0]; vB1 = nv[1];
    auto ldB = [&](int s, int part) {
      const u16* b = wnx + (size_t)(s ? kn1 : hp) * 64 * 3072;
      if (part) glds4v(b, vB0, vB1, vB0, vB1, sb + s * 40960 + 16384 + lw * 6144);
      else glds2v(b + 2048, vB0, vB1, sb + s * 40960 + 16384 + lw * 6144 + 4096);
    };
    BAR;
    ldA(1); ldA(2); ldA(3); ldA(4);
    WAITV(12);
    BAR;
    auto step = [&](auto ktc) {
      constexpr int kt = decltype(ktc)::value;
      WAITVN(WT[kt]);
      BAR;
      if constexpr (kt == 1) { ldA(5); ldA(6); }
      if constexpr (kt >= 2 && kt <= 10) ldA(kt + 5);
      if constexpr (kt >= 2 && kt <= 5) ldW(kt + 10);
      if constexpr (kt >= 11) ldB((kt - 11) >> 1, (kt - 11) & 1);
      if constexpr (kt >= 6) { if (layer == 1) __builtin_amdgcn_s_sleep(kt >= 11 ? 4 : 3); }
    };
    step(IC<1>{}); step(IC<2>{}); step(IC<3>{}); step(IC<4>{}); step(IC<5>{}); step(IC<6>{}); step(IC<7>{});
    step(IC<8>{}); step(IC<9>{}); step(IC<10>{}); step(IC<11>{}); step(IC<12>{}); step(IC<13>{}); step(IC<14>{});
  } else {
    const int wm = w >> 1, wn = w & 1;
    f32x4 acc[4][2];
    unsigned co[2];
#pragma unroll
    for (int cb = 0; cb < 2; ++cb) co[cb] = sb + OFF_C + (unsigned)((wm * 64 + fr) * 128 + (((wn * 4 + cb * 2 + (g >> 1)) ^ (fr & 7)) << 4) + (g & 1) * 8);
    const int fl = (q4 >> 1) | ((g & 1) << 1);
    const int bb0 = (4 * g + q4) * 128 + p4 * 8 + (((wn * 2) ^ fl) << 5), bb1 = (4 * g + q4) * 128 + p4 * 8 + (((wn * 2 + 1) ^ fl) << 5);
    const int abase = wm * 4096 + lane * 16;
    bf16x8 fa[2][2][4], fb[2][2][2];
    unsigned ra[3], rb0[2], rb1[2];
#pragma unroll
    for (int i = 0; i < 3; ++i) { ra[i] = abase + i * 65536; asm volatile("" : "+v"(ra[i])); }
#pragma unroll
    for (int i = 0; i < 2; ++i) { rb0[i] = bb0 + OFF_WO + i * 49152; rb1[i] = bb1 + OFF_WO + i * 49152; asm volatile("" : "+v"(rb0[i]), "+v"(rb1[i])); }
    auto loadf = [&](int kt, bf16x8 (&a)[2][4], bf16x8 (&b)[2][2]) {
#pragma unroll
      for (int s = 0; s < 2; ++s) {
        const char* sA = smem + ra[AS[kt] >> 16] + (AS[kt] & 65535) + s * 8192;
        const int hi = bs(kt) >= OFF_WO + 49152, ob = bs(kt) - OFF_WO - hi * 49152 + s * 4096;
        b[s][0] = cat8(tr_read(smem + rb0[hi] + ob), tr_read(smem + rb0[hi] + ob + 2048));
        b[s][1] = cat8(tr_read(smem + rb1[hi] + ob), tr_read(smem + rb1[hi] + ob + 2048));
#pragma unroll
        for (int rb = 0; rb < 4; ++rb) a[s][rb] = *(const bf16x8*)(sA + rb * 1024);
      }
    };
    auto mma = [&](bf16x8 (&a)[2][4], bf16x8 (&b)[2][2]) {
#pragma unroll
      for (int i = 0; i < 16; ++i) {
        const int rb = i >> 2, cb = ((i >> 1) ^ rb) & 1, s = ((i + 1) >> 1) & 1;
        acc[rb][cb] = MFMA(b[s][cb], a[s][rb], acc[rb][cb]);
      }
    };
    unsigned fo = lane < 16 && lane != hp ? (WS_FLAGS + (unsigned)(rt * 16 + lane) * FLS) ^ P : 0u;
    const u64 ko = korder(bid & 3, hp);
    unsigned kf = kfirst(ko), kf2 = kfirst(ko >> 16) | (1u << knib(ko, 9));
    unsigned jo = (unsigned)((((rt & 7) * 128 + (w - 1) * 64 + lane) * 3072 + (hp & 3) * 1024 + (hp | 3) * 64) * 2);
    unsigned fo1 = ((kf >> (lane & 31)) & 1) ? fo : 0u, fo2 = ((kf2 >> (lane & 31)) & 1) ? fo : 0u;
    fo = (((kf | kf2) >> (lane & 31)) & 1) ? 0u : fo;
    asm volatile("" : "+v"(fo), "+v"(fo1), "+v"(fo2));
    asm volatile("" : "+s"(kf), "+s"(kf2));
    publish(rsW, tid, rt * 16 + hp, E + (unsigned)(layer + 1), P);
    ST(layer * 24 + 13)
    u64 fl1 = 0;
    if (w == 0) asm volatile("global_load_dwordx2 %0, %1, %2 sc1" : "+v"(fl1) : "v"(fo1), "s"(ws) : "memory");
    if (layer == 0) {
      const unsigned xo = (unsigned)(((m0 + wm * 64 + fr) * DM + hp * 64 + wn * 32 + g * 8) * 2);
      asm volatile("global_load_dwordx4 v[64:67], %1, %3\n\tglobal_load_dwordx4 v[68:71], %1, %4\n\tglobal_load_dwordx4 v[72:75], %2, %3\n\tglobal_load_dwordx4 v[76:79], %2, %4"
                   : "+{v[64:79]}"(xq) : "v"(xo), "v"(xo + 32 * DM * 2), "s"(x0), "s"(x0 + 16 * DM) : "memory");
    }
    loadf(0, fa[0], fb[0]);
    unsigned junk = lane;
    if (layer < 3 && (hp & 7) < 3 && (w == 1 || w == 2))
      asm volatile("global_load_dword %0, %1, %2" : "+v"(junk) : "v"(jo), "s"(wnx) : "memory");
    if (layer == 0 && w == 3) {
      CODE_TOUCH(2, ".Lmk_g1b", ".Lmk_g1a")
      asm volatile("global_load_dword %0, %1, %2" : "+v"(junk) : "v"(v), "s"(pc) : "memory");
    }
    if (layer == 1 && w == 3) {
      CODE_TOUCH(3, ".Lmk_f0", ".Lmk_f1")
      asm volatile("global_load_dword %0, %1, %2" : "+v"(junk) : "v"(v), "s"(pc) : "memory");
    }
    __builtin_amdgcn_sched_barrier(0);
    bf16x8 id[2];
#pragma unroll
    for (int cb = 0; cb < 2; ++cb) {
      const unsigned one = (g >> 1) == cb && (g & 1) == (fr >> 3) ? 0x3F80u << ((fr & 1) * 16) : 0u;
      u32x4 t;
#pragma unroll
      for (int j = 0; j < 4; ++j) t[j] = ((fr >> 1) & 3) == j ? one : 0u;
      id[cb] = __builtin_bit_cast(bf16x8, t);
      asm volatile("" : "+v"(id[cb]));
    }
    asm volatile("" : "+v"(co[0]), "+v"(co[1]));
#pragma unroll
    for (int rb = 0; rb < 4; ++rb) { acc[rb][0] = f32x4{0, 0, 0, 0}; acc[rb][1] = f32x4{0, 0, 0, 0}; }
    __builtin_amdgcn_sched_barrier(0);
    WAITL0;
    u64 miss = 0;
    if (w == 0) { ARRIVED("+v"(fl1)); if (__ballot(fl1 != E + (unsigned)(layer + 1)) & kf) poll_eq(ws, fo1, E + (unsigned)(layer + 1), kf, kf); miss = 0xffffu & ~(kf | kf2 | (1u << hp)); }
    ST(layer * 24 + 14)
    __builtin_amdgcn_sched_barrier(0);
    BAR;
    __builtin_amdgcn_sched_barrier(0);
    asm volatile("" ::: "memory");
    if (w == 0) asm volatile("global_load_dwordx2 %0, %1, %2 sc1" : "+v"(fl1) : "v"(fo2), "s"(ws) : "memory");
    mma(fa[0], fb[0]);
    __builtin_amdgcn_sched_barrier(0);
    if (layer == 0) WAITV(0);
    __builtin_amdgcn_sched_barrier(0);
    asm volatile("" : "+{v[64:79]}"(xq));
    const bf16x8 xf[4] = {__builtin_bit_cast(bf16x8, quad<0>(xq)), __builtin_bit_cast(bf16x8, quad<1>(xq)), __builtin_bit_cast(bf16x8, quad<2>(xq)), __builtin_bit_cast(bf16x8, quad<3>(xq))};
#pragma unroll
    for (int rb = 0; rb < 4; ++rb) { acc[rb][0] = MFMA(id[0], xf[rb], acc[rb][0]); acc[rb][1] = MFMA(id[1], xf[rb], acc[rb][1]); }
    nv = g1_frag_addr(sb, w, lane);
    if (w == 0) { ARRIVED("+v"(fl1)); if (__ballot(fl1 != E + (unsigned)(layer + 1)) & kf2) poll_eq(ws, fo2, E + (unsigned)(layer + 1), kf2, kf2); }
    u64 fl2 = 0;
    auto step = [&](auto ktc) {
      constexpr int kt = decltype(ktc)::value;
      __builtin_amdgcn_sched_barrier(0);
      if (kt == 5 && miss) {
        ARRIVED("+v"(fl2));
        if (__ballot(fl2 != E + (unsigned)(layer + 1)) & miss) poll_eq(ws, fo, E + (unsigned)(layer + 1), miss, ~0ull);
      }
      WAITL0;
      BAR;
      if (kt == 2 && miss) asm volatile("global_load_dwordx2 %0, %1, %2 sc1" : "+v"(fl2) : "v"(fo), "s"(ws) : "memory");
      if (kt == 0) { ST(layer * 24 + 15) }
      __builtin_amdgcn_sched_barrier(0);
      loadf(kt + 1, fa[(kt + 1) & 1], fb[(kt + 1) & 1]);
      if (kt == 0) return;
      mma(fa[kt & 1], fb[kt & 1]);
#pragma unroll
      for (int i = 0; i < 4; ++i) {
        __builtin_amdgcn_sched_group_barrier(0x008, 1, 0);
        __builtin_amdgcn_sched_group_barrier(0x100, 1, 0);
        __builtin_amdgcn_sched_group_barrier(0x008, 1, 0);
        __builtin_amdgcn_sched_group_barrier(0x100, 3, 0);
      }
#pragma unroll
      for (int i = 0; i < 8; ++i) __builtin_amdgcn_sched_group_barrier(0x008, 1, 0);
    };
    step(IC<0>{}); step(IC<1>{}); step(IC<2>{}); step(IC<3>{}); step(IC<4>{}); step(IC<5>{}); step(IC<6>{}); step(IC<7>{});
    step(IC<8>{}); step(IC<9>{}); step(IC<10>{}); step(IC<11>{}); step(IC<12>{}); step(IC<13>{}); step(IC<14>{});
    WAITV(0);
    asm volatile("" :: "v"(junk));
    __builtin_amdgcn_sched_barrier(0);
    auto put = [&](int rb) {
#pragma unroll
      for (int cb = 0; cb < 2; ++cb) lds<u32x2>(co[cb] + rb * 2048) = u32x2{pk2(acc[rb][cb][0], acc[rb][cb][1]), pk2(acc[rb][cb][2], acc[rb][cb][3])};
    };
#pragma unroll
    for (int rb = 0; rb < 4; ++rb)
#pragma unroll
      for (int cb = 0; cb < 2; ++cb) acc[rb][cb] = MFMA(fb[1][0][cb], fa[1][0][rb], acc[rb][cb]);
#pragma unroll
    for (int rb = 0; rb < 4; ++rb) {
      __builtin_amdgcn_sched_barrier(0);
#pragma unroll
      for (int cb = 0; cb < 2; ++cb) acc[rb][cb] = MFMA(fb[1][1][cb], fa[1][1][rb], acc[rb][cb]);
      __builtin_amdgcn_sched_barrier(0);
      if (rb >= 2) put(rb - 2);
    }
    __builtin_amdgcn_sched_barrier(0);
    ST(layer * 24 + 16)
    put(2); put(3);
  }
  WAITL0; BAR;
  asm volatile("" ::: "memory");
  u32x4 xn[2];
#pragma unroll
  for (int i = 0; i < 2; ++i) xn[i] = lds<u32x4>(pxr + OFF_C + i * 1024);
  if (layer == 3) {
    const __amdgpu_buffer_rsrc_t rsO = __builtin_amdgcn_make_buffer_rsrc((void*)uni(out), (short)0, SEQ * DM * 2, 0x00020000);
    __builtin_amdgcn_raw_buffer_store_b128(xn[0], rsO, rmo, 0, 16);
    __builtin_amdgcn_raw_buffer_store_b128(xn[1], rsO, rmo + 8 * DM * 2, 0, 16);
  } else {
    __builtin_amdgcn_raw_buffer_store_b128(xn[0], rsW, WS_X + layer * (SEQ * DM * 2) + imo, 0, 16);
    __builtin_amdgcn_raw_buffer_store_b128(xn[1], rsW, WS_X + layer * (SEQ * DM * 2) + imo + 1024, 0, 16);
    float mean[2], m2[2];
#pragma unroll
    for (int i = 0; i < 2; ++i) {
      typedef __attribute__((ext_vector_type(2))) __bf16 bf2;
      const bf2 one2 = __builtin_bit_cast(bf2, 0x3F803F80u);
      float sa = zero1(), sq = zero1();
#pragma unroll
      for (int e = 0; e < 4; ++e) {
        const unsigned u = xn[i][e];
        const bf2 t = __builtin_bit_cast(bf2, u);
        sa = __builtin_amdgcn_fdot2_f32_bf16(t, one2, sa, false);
        sq = __builtin_amdgcn_fdot2_f32_bf16(t, t, sq, false);
      }
      mean[i] = sa;
      m2[i] = sq;
    }
#define DPA(c) "v_add_f32_dpp %0, %0, %0 " c "\n\tv_add_f32_dpp %1, %1, %1 " c "\n\tv_add_f32_dpp %2, %2, %2 " c "\n\tv_add_f32_dpp %3, %3, %3 " c "\n\t"
    asm volatile("s_nop 3\n\t" DPA("quad_perm:[1,0,3,2] row_mask:0xf bank_mask:0xf") DPA("quad_perm:[2,3,0,1] row_mask:0xf bank_mask:0xf")
                 DPA("row_half_mirror row_mask:0xf bank_mask:0xf") "s_nop 0" : "+v"(mean[0]), "+v"(m2[0]), "+v"(mean[1]), "+v"(m2[1]));
    if ((lane & 7) < 2) {
      const int i = lane & 1;
      const u32x2 sv = {__float_as_uint(i ? mean[1] : mean[0]), __float_as_uint(i ? m2[1] : m2[0])};
      __builtin_amdgcn_raw_buffer_store_b64(sv, rsW, WS_ST + layer * (SEQ * 128) + (unsigned)((rt & 7) * 32768 + (hp >> 3) * 16384 + (hp & 7) * 1024 + (r0 + i * 8) * 8) + ((rt >> 3) * 8192 ^ P), 0, 16);
    }
  }
  ST(layer * 24 + 17)
  if (layer < 3 && w >= 4) { WAITV(0); BAR; }
  ST(layer * 24 + 18)
}

__global__ __launch_bounds__(512, 2) void mega_kernel(const u16* __restrict__ x0, const u16* __restrict__ wqkvA,
                                                      const u16* __restrict__ woA, const u16* __restrict__ wqkvB,
                                                      const u16* __restrict__ wphi, const u16* __restrict__ woB,
                                                      u16* __restrict__ out, char* __restrict__ ws) {
  unsigned P = 0;
  extern __shared__ __attribute__((aligned(16))) char smem[];
  const int w = __builtin_amdgcn_readfirstlane(threadIdx.x >> 6);
  u64 E = 0;
  {
    const int bid = blockIdx.x, lane = threadIdx.x & 63;
    const int rt = 2 * (bid & 4) + ((bid >> 3) & 7), hp = 4 * (bid & 3) + (bid >> 6), m0 = rt * 128;
    if (w < 4) {
      if (w == 3) {
        unsigned long long pc;
        unsigned len;
        asm volatile("s_getpc_b64 %0\n.Lmk_pc:\n\ts_mov_b32 %1, .Lmk_end-.Lmk_pc" : "=s"(pc), "=s"(len));
        const unsigned per = ((len >> 10) + 1) * 128, off = ((bid >> 3) & 7) * per + (lane * 128 < per ? lane * 128 : 0);
        asm volatile("s_mov_b32 m0, %2\n\ts_nop 0\n\tglobal_load_lds_dword %0, %1" :: "v"(off < len - 4 ? off : 0u), "s"(pc), "s"(lds_addr(smem) + LDS_TOTAL - 1024) : "memory", "m0");
      }
      const unsigned xo = (unsigned)((m0 + hp * 8 + w * 2) * (DM * 2) + lane * 16);
      glds4v(x0, xo, xo, xo, xo, lds_addr(smem) + 122880 + w * 4096);
      asm volatile("s_mov_b32 m0, %2\n\ts_nop 0\n\tglobal_load_lds_dword %0, %1" :: "v"(lane * 4), "s"(ws), "s"(lds_addr(smem) + OFF_E) : "memory", "m0");
    } else {
      const int lw = w & 3;
      const int kB = 16 * lw + (lane >> 3), fB = (kB >> 1) & 1;
      const unsigned vB0 = (unsigned)((kB * 3072 + hp * 64 + (((((lane & 7) >> 1) ^ fB) << 1) | (lane & 1)) * 8) * 2);
      const unsigned vB1 = (unsigned)(((kB + 8) * 3072 + hp * 64 + (((((lane & 7) >> 1) ^ (fB | 2)) << 1) | (lane & 1)) * 8) * 2) - 1024u;
      const unsigned vA = (unsigned)(((m0 + lw * 32 + (lane >> 3)) * DM + ((lane & 7) ^ (lane >> 3)) * 8) * 2);
#pragma unroll
      for (int s = 0; s < 1; ++s) {
        const int kt = (rt & 8) + s;
        const unsigned sA = lds_addr(smem) + s * 40960, sB = sA + 16384;
        glds4v(x0 + kt * 64, vA, vA + 15360u, vA + 2 * 15360u, vA + 3 * 15360u, sA + lw * 4096);
        const u16* b = wqkvA + (size_t)kt * 64 * 3072;
        glds4v(b, vB0, vB1, vB0, vB1, sB + lw * 6144);
        glds2v(b + 2048, vB0, vB1, sB + lw * 6144 + 4096);
      }
    }
  }
  f32x16 xq;
  u32x4 nv;
  u64 nk;
  asm volatile("" : "=v"(nv));
  asm volatile("" : "=s"(nk));
#pragma unroll 1
  for (int layer = 0; layer < 4; ++layer) {
    if (layer & 1) g1_phase<1>(smem, ws, P, nv, nk, E, w, xq, layer, x0, wqkvB, woB, wphi, nullptr, nullptr);
    else g1_phase<0>(smem, ws, P, nv, nk, E, w, xq, layer, x0, wqkvA, woA, nullptr, wqkvB, woB);
    g2_phase(smem, ws, P, nv, nk, E, w, xq, layer, x0, (layer & 1) ? woB : woA, (layer & 1) ? wqkvA : wqkvB, out);
  }
  asm volatile(".Lmk_end:");
  WAITV(0);
  if (blockIdx.x == 0 && w == 0 && lane_id() == 0) __hip_atomic_store((gu64*)ws, E + 16u, __ATOMIC_RELAXED, __HIP_MEMORY_SCOPE_AGENT);
}

extern "C" void kernel_launch(void* const* d_in, const int* in_sizes, int n_in,
                              void* d_out, int out_size, void* d_ws, size_t ws_size,
                              hipStream_t stream) {
  hipFuncSetAttribute((const void*)mega_kernel, hipFuncAttributeMaxDynamicSharedMemorySize, LDS_TOTAL);
  mega_kernel<<<256, 512, LDS_TOTAL, stream>>>((const u16*)d_in[0], (const u16*)d_in[1], (const u16*)d_in[2], (const u16*)d_in[4],
                                               (const u16*)d_in[5], (const u16*)d_in[6], (u16*)d_out, (char*)d_ws);
}
```
